```python
import torch

def _softmax(x, axis=-1):
    return torch.softmax(x, dim=axis)

def _gelu(x):
    return torch.nn.functional.gelu(x, approximate='tanh')

def _silu(x):
    return torch.nn.functional.silu(x)

def _sigmoid(x):
    return torch.sigmoid(x)

def _layernorm(x, axis=-1, eps=1e-05):
    m = x.mean(dim=axis, keepdim=True)
    v = x.var(dim=axis, keepdim=True, unbiased=False)
    return (x - m) / torch.sqrt(v + eps)

def _scatter_add(tgt, idx, src, axis=0):
    return tgt.clone().index_add_(axis, idx, src)

def _segment_sum(src, seg, n):
    out = torch.zeros((n,) + src.shape[1:], dtype=src.dtype)
    return out.index_add_(0, seg, src)

def _pad(x, pw):
    flat = tuple((v for pair in reversed(pw) for v in pair))
    return torch.nn.functional.pad(x, flat)
kernel_name = 'concat_heads__grouped_kv_mqa__memory_aug_recurrent_mem_tr__prompt'

def setup_inputs():

    _w = lambda a: (lambda t: t.to(torch.bfloat16) if t.is_floating_point() else t)(torch.as_tensor(a))

    def _A_setup():
        B, S, D, H = (1, 2048, 1024, 32)
        Dh = D // H
        return {'x': _w((lambda *s: torch.randn(*s, dtype=torch.bfloat16) * 0.05)(B, S, D)), 'wq': _w((lambda *s: torch.randn(*s, dtype=torch.bfloat16) * 3.2)(D, H * Dh)), 'wk': _w((lambda *s: torch.randn(*s, dtype=torch.bfloat16) * 3.2)(D, Dh)), 'wv': _w((lambda *s: torch.randn(*s, dtype=torch.bfloat16) * 0.1)(D, Dh)), 'wo': _w((lambda *s: torch.randn(*s, dtype=torch.bfloat16) * 0.1)(H * Dh, D))}
    _A_in = _A_setup()

    def _B_setup():
        B, S, Dm = (1, 2048, 1024)
        H, Dh = (32, 128)
        M, seg = (8, 32)
        seg = min(seg, S)
        S = S // seg * seg
        return {'x': _w((lambda *s: torch.randn(*s, dtype=torch.bfloat16) * 0.05)(B, S, Dm)), 'mem0': _w((lambda *s: torch.randn(*s, dtype=torch.bfloat16) * 1.6)(B, M, Dm)), 'Wq': _w((lambda *s: torch.randn(*s, dtype=torch.bfloat16) * 0.8)(Dm, H, Dh) * Dm ** (-0.5)), 'Wk': _w((lambda *s: torch.randn(*s, dtype=torch.bfloat16) * 0.8)(Dm, H, Dh) * Dm ** (-0.5)), 'Wv': _w((lambda *s: torch.randn(*s, dtype=torch.bfloat16) * 1.6)(Dm, H, Dh) * Dm ** (-0.5)), 'Wo': _w((lambda *s: torch.randn(*s, dtype=torch.bfloat16) * 1.6)(H, Dh, Dm) * (H * Dh) ** (-0.5)), 'seg_len': seg}
    _B_in = _B_setup()
    return {'A_x': _A_in['x'], 'A_wq': _A_in['wq'], 'A_wk': _A_in['wk'], 'A_wv': _A_in['wv'], 'A_wo': _A_in['wo'], 'B_x': _B_in['x'], 'B_mem0': _B_in['mem0'], 'B_Wq': _B_in['Wq'], 'B_Wk': _B_in['Wk'], 'B_Wv': _B_in['Wv'], 'B_Wo': _B_in['Wo'], 'B_seg_len': _B_in['seg_len']}

def reference(A_x, A_wq, A_wk, A_wv, A_wo, B_x, B_mem0, B_Wq, B_Wk, B_Wv, B_Wo, B_seg_len):

    def _A_ref(x, wq, wk, wv, wo):
        B, S, D = x.shape
        H = 32
        Dh = D // H
        q = (lambda x, *d: x.permute(*d))((x @ wq).reshape(B, S, H, Dh), 0, 2, 1, 3)
        k = x @ wk
        v = x @ wv
        scores = torch.einsum('bhqd,bkd->bhqk', q, k) * Dh ** (-0.5)
        p = _softmax(scores, axis=-1)
        o = torch.einsum('bhqk,bkd->bhqd', p, v)
        return (lambda x, *d: x.permute(*d))(o, 0, 2, 1, 3).reshape(B, S, H * Dh) @ wo

    def _B_ref(x, mem0, Wq, Wk, Wv, Wo, seg_len):
        B, S, Dm = x.shape
        H, Dh = (Wq.shape[1], Wq.shape[2])
        M = mem0.shape[1]
        scale = Dh ** (-0.5)
        n_seg = S // seg_len
        L = M + seg_len
        idx = torch.arange(L)
        mask = (idx[:, None] < M) | (idx[:, None] >= idx[None, :])
        mem = mem0
        outs = []
        for s in range(n_seg):
            xs = x[:, s * seg_len:(s + 1) * seg_len, :].clone()
            h = torch.concatenate([mem, xs], axis=1)
            q = torch.einsum('btd,dhe->bhte', h, Wq)
            k = torch.einsum('btd,dhe->bhte', h, Wk)
            v = torch.einsum('btd,dhe->bhte', h, Wv)
            scores = torch.einsum('bhqe,bhke->bhqk', q, k) * scale
            scores = torch.where(mask[None, None], scores, -1000000000.0)
            p = _softmax(scores, axis=-1)
            o = torch.einsum('bhqk,bhke->bhqe', p, v)
            o = torch.einsum('bhte,hed->btd', o, Wo)
            mem = o[:, :M, :].clone()
            outs.append(o[:, M:, :])
        return torch.concatenate(outs, axis=1)
    a = _A_ref(A_x, A_wq, A_wk, A_wv, A_wo)
    b = _B_ref(B_x, B_mem0, B_Wq, B_Wk, B_Wv, B_Wo, B_seg_len)
    return torch.concatenate([a, b], axis=-1)
```

```cpp
#include <hip/hip_runtime.h>
#include <hip/hip_bf16.h>
#define MODE 0
typedef unsigned short u16;
__device__ __forceinline__ float b2f(u16 b){ return __builtin_bit_cast(float,(unsigned)b<<16);}
__device__ __forceinline__ u16 f2b(float f){ unsigned u=__builtin_bit_cast(unsigned,f); u+=0x7FFFu+((u>>16)&1u); return u>>16;}
__device__ __forceinline__ float rb(float f){ return b2f(f2b(f)); }

__global__ void __launch_bounds__(256) gemm(const u16* A,int lda,const u16* B,int ldb,u16* C,int ldc,int M,int N,int K){
  __shared__ float As[16][65]; __shared__ float Bs[16][65];
  int tx=threadIdx.x&15, ty=threadIdx.x>>4; int m0=blockIdx.y*64,n0=blockIdx.x*64;
  float acc[4][4]={};
  for(int k0=0;k0<K;k0+=16){
    for(int i=threadIdx.x;i<1024;i+=256){ int r=i>>4,c=i&15; As[c][r]=(m0+r<M)?b2f(A[(size_t)(m0+r)*lda+k0+c]):0.f; }
    for(int i=threadIdx.x;i<1024;i+=256){ int r=i>>6,c=i&63; Bs[r][c]=(n0+c<N)?b2f(B[(size_t)(k0+r)*ldb+n0+c]):0.f; }
    __syncthreads();
    for(int k=0;k<16;k++){ float a[4],b[4];
      for(int i=0;i<4;i++){a[i]=As[k][ty*4+i]; b[i]=Bs[k][tx*4+i];}
      for(int i=0;i<4;i++)for(int j=0;j<4;j++)acc[i][j]+=a[i]*b[j]; }
    __syncthreads();
  }
  for(int i=0;i<4;i++)for(int j=0;j<4;j++){int m=m0+ty*4+i,n=n0+tx*4+j; if(m<M&&n<N)C[(size_t)m*ldc+n]=f2b(acc[i][j]);}
}
__global__ void __launch_bounds__(256) attnA(const u16* q,const u16* k,const u16* v,u16* o){
  __shared__ float ks[64][32], vs[64][32];
  int idx=blockIdx.x*256+threadIdx.x;
  float qf[32]; for(int d=0;d<32;d++)qf[d]=b2f(q[(size_t)idx*32+d]);
  const float scale=0.17677669529663687f;
  float m=-1e30f,l=0;
  for(int j0=0;j0<2048;j0+=64){ __syncthreads();
    for(int i=threadIdx.x;i<2048;i+=256) ks[i>>5][i&31]=b2f(k[j0*32+i]);
    __syncthreads();
    for(int j=0;j<64;j++){ float s=0; for(int d=0;d<32;d++)s+=qf[d]*ks[j][d];
      if(MODE==1) s=rb(rb(s)*scale); else s*=scale;
      float mn=fmaxf(m,s); l=l*__expf(m-mn)+__expf(s-mn); m=mn; } }
  float acc[32]={}; float inv=1.f/l;
  for(int j0=0;j0<2048;j0+=64){ __syncthreads();
    for(int i=threadIdx.x;i<2048;i+=256){ ks[i>>5][i&31]=b2f(k[j0*32+i]); vs[i>>5][i&31]=b2f(v[j0*32+i]);}
    __syncthreads();
    for(int j=0;j<64;j++){ float s=0; for(int d=0;d<32;d++)s+=qf[d]*ks[j][d];
      if(MODE==1) s=rb(rb(s)*scale); else s*=scale;
      float p=__expf(s-m); if(MODE==1) p=rb(p*inv); else p=rb(p);
      for(int d=0;d<32;d++)acc[d]+=p*vs[j][d]; } }
  for(int d=0;d<32;d++) o[(size_t)idx*32+d]=f2b(MODE==1?acc[d]:acc[d]*inv);
}
__global__ void __launch_bounds__(256) attnB(const u16* qm,const u16* km,const u16* vm,const u16* qx,const u16* kx,const u16* vx,u16* om,u16* ox){
  __shared__ float Q[40][129],Kk[40][129],V[40][129],P[40][41];
  int h=blockIdx.x;
  for(int i=threadIdx.x;i<40*128;i+=256){int t=i>>7,e=i&127; size_t off=(size_t)(t<8?t:t-8)*4096+h*128+e;
    Q[t][e]=b2f((t<8?qm:qx)[off]); Kk[t][e]=b2f((t<8?km:kx)[off]); V[t][e]=b2f((t<8?vm:vx)[off]);}
  __syncthreads();
  const float scale=0.08838834764831845f;
  for(int i=threadIdx.x;i<1600;i+=256){int a=i/40,b=i%40; float s=0; for(int e=0;e<128;e++)s+=Q[a][e]*Kk[b][e];
    if(MODE==1)s=rb(rb(s)*scale); else s*=scale; if(!(a<8||a>=b)) s=-1e9f; P[a][b]=s;}
  __syncthreads();
  if(threadIdx.x<40){int a=threadIdx.x; float m=-1e30f; for(int b=0;b<40;b++)m=fmaxf(m,P[a][b]); float l=0; for(int b=0;b<40;b++){float p=__expf(P[a][b]-m);P[a][b]=p;l+=p;} for(int b=0;b<40;b++)P[a][b]=rb(P[a][b]/l);}
  __syncthreads();
  for(int i=threadIdx.x;i<40*128;i+=256){int t=i>>7,e=i&127; float s=0; for(int b=0;b<40;b++)s+=P[t][b]*V[b][e];
    size_t off=(size_t)(t<8?t:t-8)*4096+h*128+e; (t<8?om:ox)[off]=f2b(s);}
}
static void G(hipStream_t st,const u16*A,int lda,const u16*B,int ldb,u16*C,int ldc,int M,int N,int K){ gemm<<<dim3((N+63)/64,(M+63)/64),256,0,st>>>(A,lda,B,ldb,C,ldc,M,N,K);}
extern "C" void kernel_launch(void* const* d_in, const int* in_sizes, int n_in, void* d_out, int out_size, void* d_ws, size_t ws_size, hipStream_t st) {
  const u16 *Ax=(u16*)d_in[0],*Awq=(u16*)d_in[1],*Awk=(u16*)d_in[2],*Awv=(u16*)d_in[3],*Awo=(u16*)d_in[4];
  const u16 *Bx=(u16*)d_in[5],*Bm=(u16*)d_in[6],*Wq=(u16*)d_in[7],*Wk=(u16*)d_in[8],*Wv=(u16*)d_in[9],*Wo=(u16*)d_in[10];
  u16* out=(u16*)d_out; u16* w=(u16*)d_ws;
  u16* aq=w; w+=2048*1024; u16* ak=w; w+=2048*32; u16* av=w; w+=2048*32; u16* ao=w; w+=2048*1024;
  u16* qx=w; w+=2048*4096; u16* kx=w; w+=2048*4096; u16* vx=w; w+=2048*4096; u16* ox=w; w+=2048*4096;
  u16* qm=w; w+=8*4096; u16* km=w; w+=8*4096; u16* vm=w; w+=8*4096; u16* om=w; w+=8*4096; u16* mem=w; w+=8*1024;
  G(st,Ax,1024,Awq,1024,aq,1024,2048,1024,1024); G(st,Ax,1024,Awk,32,ak,32,2048,32,1024); G(st,Ax,1024,Awv,32,av,32,2048,32,1024);
  attnA<<<256,256,0,st>>>(aq,ak,av,ao);
  G(st,ao,1024,Awo,1024,out,2048,2048,1024,1024);
  G(st,Bx,1024,Wq,4096,qx,4096,2048,4096,1024); G(st,Bx,1024,Wk,4096,kx,4096,2048,4096,1024); G(st,Bx,1024,Wv,4096,vx,4096,2048,4096,1024);
  for(int s=0;s<64;s++){ const u16* m=s?mem:Bm;
    G(st,m,1024,Wq,4096,qm,4096,8,4096,1024); G(st,m,1024,Wk,4096,km,4096,8,4096,1024); G(st,m,1024,Wv,4096,vm,4096,8,4096,1024);
    attnB<<<32,256,0,st>>>(qm,km,vm,qx+(size_t)s*32*4096,kx+(size_t)s*32*4096,vx+(size_t)s*32*4096,om,ox+(size_t)s*32*4096);
    G(st,om,4096,Wo,1024,mem,1024,8,1024,4096);
  }
  G(st,ox,4096,Wo,1024,out+1024,2048,2048,1024,4096);
}
```

```cpp
#include <hip/hip_runtime.h>
typedef unsigned short u16;
typedef unsigned v2u __attribute__((ext_vector_type(2)));
typedef unsigned v4u __attribute__((ext_vector_type(4)));
typedef unsigned v8u __attribute__((ext_vector_type(8)));
typedef unsigned v16u __attribute__((ext_vector_type(16)));
typedef float f32x4 __attribute__((ext_vector_type(4)));
typedef float f32x16 __attribute__((ext_vector_type(16)));
typedef __bf16 bf16x8 __attribute__((ext_vector_type(8)));
typedef __amdgpu_buffer_rsrc_t rsrc_t;
#define LAS __attribute__((address_space(3)))
#define DEVI __device__ __forceinline__
#define MEMFENCE() asm volatile("" ::: "memory")
#define DRAIN() asm volatile("s_waitcnt vmcnt(0)" ::: "memory")
#define RAWBAR() asm volatile("s_waitcnt lgkmcnt(0)\n\ts_barrier" ::: "memory")

#define QK_LOOP_ASM \
    "s_nop 4\n" \
    "s_movk_i32 s72, 1024\n" \
    "s_lshl_b32 s73, s54, 15\n" \
    "v_xor_b32 v104, 0, v110\n" \
    "v_xor_b32 v105, 32, v110\n" \
    "v_xor_b32 v106, 64, v110\n" \
    "v_xor_b32 v107, 96, v110\n" \
    "s_mov_b32 s74, 57344\n" \
    "buffer_load_dword v108, off, s[40:43], s74 sc1\n" \
    "s_add_u32 m0, s73, 0\n" \
    "s_mov_b32 s62, 0\n" \
    "buffer_load_dwordx4 v111, s[64:67], s62 offen lds\n" \
    "s_add_u32 m0, m0, s72\n" \
    "s_mov_b32 s62, 131072\n" \
    "buffer_load_dwordx4 v111, s[64:67], s62 offen lds\n" \
    "s_add_u32 m0, m0, s72\n" \
    "s_mov_b32 s62, 8192\n" \
    "buffer_load_dwordx4 v111, s[64:67], s62 offen lds\n" \
    "s_add_u32 m0, m0, s72\n" \
    "s_mov_b32 s62, 139264\n" \
    "buffer_load_dwordx4 v111, s[64:67], s62 offen lds\n" \
    "s_add_u32 m0, m0, s72\n" \
    "s_mov_b32 s62, 262144\n" \
    "buffer_load_dwordx4 v111, s[64:67], s62 offen lds\n" \
    "s_add_u32 m0, m0, s72\n" \
    "s_mov_b32 s62, 393216\n" \
    "buffer_load_dwordx4 v111, s[64:67], s62 offen lds\n" \
    "s_add_u32 m0, m0, s72\n" \
    "s_mov_b32 s62, 270336\n" \
    "buffer_load_dwordx4 v111, s[64:67], s62 offen lds\n" \
    "s_add_u32 m0, m0, s72\n" \
    "s_mov_b32 s62, 401408\n" \
    "buffer_load_dwordx4 v111, s[64:67], s62 offen lds\n" \
    "s_add_u32 m0, s73, 8192\n" \
    "s_mov_b32 s62, 524288\n" \
    "buffer_load_dwordx4 v111, s[64:67], s62 offen lds\n" \
    "s_add_u32 m0, m0, s72\n" \
    "s_mov_b32 s62, 655360\n" \
    "buffer_load_dwordx4 v111, s[64:67], s62 offen lds\n" \
    "s_add_u32 m0, m0, s72\n" \
    "s_mov_b32 s62, 532480\n" \
    "buffer_load_dwordx4 v111, s[64:67], s62 offen lds\n" \
    "s_add_u32 m0, m0, s72\n" \
    "s_mov_b32 s62, 663552\n" \
    "buffer_load_dwordx4 v111, s[64:67], s62 offen lds\n" \
    "s_add_u32 m0, m0, s72\n" \
    "s_mov_b32 s62, 786432\n" \
    "buffer_load_dwordx4 v111, s[64:67], s62 offen lds\n" \
    "s_add_u32 m0, m0, s72\n" \
    "s_mov_b32 s62, 917504\n" \
    "buffer_load_dwordx4 v111, s[64:67], s62 offen lds\n" \
    "s_add_u32 m0, m0, s72\n" \
    "s_mov_b32 s62, 794624\n" \
    "buffer_load_dwordx4 v111, s[64:67], s62 offen lds\n" \
    "s_add_u32 m0, m0, s72\n" \
    "s_mov_b32 s62, 925696\n" \
    "buffer_load_dwordx4 v111, s[64:67], s62 offen lds\n" \
    "s_add_u32 m0, s73, 16384\n" \
    "s_mov_b32 s62, 1048576\n" \
    "buffer_load_dwordx4 v111, s[64:67], s62 offen lds\n" \
    "s_add_u32 m0, m0, s72\n" \
    "s_mov_b32 s62, 1179648\n" \
    "buffer_load_dwordx4 v111, s[64:67], s62 offen lds\n" \
    "s_add_u32 m0, m0, s72\n" \
    "s_mov_b32 s62, 1056768\n" \
    "buffer_load_dwordx4 v111, s[64:67], s62 offen lds\n" \
    "s_add_u32 m0, m0, s72\n" \
    "s_mov_b32 s62, 1187840\n" \
    "buffer_load_dwordx4 v111, s[64:67], s62 offen lds\n" \
    "s_add_u32 m0, m0, s72\n" \
    "s_mov_b32 s62, 1310720\n" \
    "buffer_load_dwordx4 v111, s[64:67], s62 offen lds\n" \
    "s_add_u32 m0, m0, s72\n" \
    "s_mov_b32 s62, 1441792\n" \
    "buffer_load_dwordx4 v111, s[64:67], s62 offen lds\n" \
    "s_add_u32 m0, m0, s72\n" \
    "s_mov_b32 s62, 1318912\n" \
    "buffer_load_dwordx4 v111, s[64:67], s62 offen lds\n" \
    "s_add_u32 m0, m0, s72\n" \
    "s_mov_b32 s62, 1449984\n" \
    "buffer_load_dwordx4 v111, s[64:67], s62 offen lds\n" \
    "s_add_u32 m0, s73, 24576\n" \
    "s_mov_b32 s62, 1572864\n" \
    "buffer_load_dwordx4 v111, s[64:67], s62 offen lds\n" \
    "s_add_u32 m0, m0, s72\n" \
    "s_mov_b32 s62, 1703936\n" \
    "buffer_load_dwordx4 v111, s[64:67], s62 offen lds\n" \
    "s_add_u32 m0, m0, s72\n" \
    "s_mov_b32 s62, 1581056\n" \
    "buffer_load_dwordx4 v111, s[64:67], s62 offen lds\n" \
    "s_add_u32 m0, m0, s72\n" \
    "s_mov_b32 s62, 1712128\n" \
    "buffer_load_dwordx4 v111, s[64:67], s62 offen lds\n" \
    "s_add_u32 m0, m0, s72\n" \
    "s_mov_b32 s62, 1835008\n" \
    "buffer_load_dwordx4 v111, s[64:67], s62 offen lds\n" \
    "s_add_u32 m0, m0, s72\n" \
    "s_mov_b32 s62, 1966080\n" \
    "buffer_load_dwordx4 v111, s[64:67], s62 offen lds\n" \
    "s_add_u32 m0, m0, s72\n" \
    "s_mov_b32 s62, 1843200\n" \
    "buffer_load_dwordx4 v111, s[64:67], s62 offen lds\n" \
    "s_add_u32 m0, m0, s72\n" \
    "s_mov_b32 s62, 1974272\n" \
    "buffer_load_dwordx4 v111, s[64:67], s62 offen lds\n" \
    "s_waitcnt vmcnt(24)\n" \
    "s_barrier\n" \
    "s_cmp_lg_u32 s54, 0\n" \
    "s_cbranch_scc1 Lqk_i_y\n" \
    "Lqk_i:\n" \
    "s_cmp_eq_u32 s90, 0\n" \
    "s_cbranch_scc1 Lqk_i_x\n" \
    "s_sub_u32 s90, s90, 1\n" \
    "buffer_load_dword v109, off, s[40:43], s74 sc1\n" \
    "s_waitcnt vmcnt(0)\n" \
    "v_cmp_eq_u32 vcc, v108, v109\n" \
    "s_cbranch_vccnz Lqk_i\n" \
    "Lqk_i_x:\n" \
    "v_mov_b32 v109, 0x10001\n" \
    "s_mov_b32 s62, 32896\n" \
    "buffer_atomic_add v109, v126, s[40:43], s62 offen\n" \
    "Lqk_i_y:\n" \
    "ds_read_b64_tr_b16 v[0:1], v104 offset:0\n" \
    "ds_read_b64_tr_b16 v[2:3], v104 offset:2048\n" \
    "ds_read_b64_tr_b16 v[4:5], v104 offset:4096\n" \
    "ds_read_b64_tr_b16 v[6:7], v104 offset:6144\n" \
    "ds_read_b64_tr_b16 v[128:129], v105 offset:0\n" \
    "ds_read_b64_tr_b16 v[130:131], v105 offset:2048\n" \
    "ds_read_b64_tr_b16 v[132:133], v105 offset:4096\n" \
    "ds_read_b64_tr_b16 v[134:135], v105 offset:6144\n" \
    "s_waitcnt lgkmcnt(4)\n" \
    "ds_read_b64_tr_b16 a[0:1], v106 offset:0\n" \
    "ds_read_b64_tr_b16 a[2:3], v106 offset:2048\n" \
    "ds_read_b64_tr_b16 a[4:5], v106 offset:4096\n" \
    "ds_read_b64_tr_b16 a[6:7], v106 offset:6144\n" \
    "ds_read_b64_tr_b16 a[8:9], v107 offset:0\n" \
    "ds_read_b64_tr_b16 a[10:11], v107 offset:2048\n" \
    "ds_read_b64_tr_b16 a[12:13], v107 offset:4096\n" \
    "ds_read_b64_tr_b16 a[14:15], v107 offset:6144\n" \
    "s_waitcnt lgkmcnt(0)\n" \
    "s_add_u32 m0, s73, 0\n" \
    "s_mov_b32 s62, 2097152\n" \
    "buffer_load_dwordx4 v111, s[64:67], s62 offen lds\n" \
    "s_add_u32 m0, m0, s72\n" \
    "s_mov_b32 s62, 2228224\n" \
    "buffer_load_dwordx4 v111, s[64:67], s62 offen lds\n" \
    "s_add_u32 m0, m0, s72\n" \
    "s_mov_b32 s62, 2105344\n" \
    "buffer_load_dwordx4 v111, s[64:67], s62 offen lds\n" \
    "s_add_u32 m0, m0, s72\n" \
    "s_mov_b32 s62, 2236416\n" \
    "buffer_load_dwordx4 v111, s[64:67], s62 offen lds\n" \
    "s_add_u32 m0, m0, s72\n" \
    "s_mov_b32 s62, 2359296\n" \
    "buffer_load_dwordx4 v111, s[64:67], s62 offen lds\n" \
    "s_add_u32 m0, m0, s72\n" \
    "s_mov_b32 s62, 2490368\n" \
    "buffer_load_dwordx4 v111, s[64:67], s62 offen lds\n" \
    "s_add_u32 m0, m0, s72\n" \
    "s_mov_b32 s62, 2367488\n" \
    "buffer_load_dwordx4 v111, s[64:67], s62 offen lds\n" \
    "s_add_u32 m0, m0, s72\n" \
    "s_mov_b32 s62, 2498560\n" \
    "buffer_load_dwordx4 v111, s[64:67], s62 offen lds\n" \
    "s_waitcnt vmcnt(24)\n" \
    "ds_read_b64_tr_b16 v[8:9], v104 offset:8192\n" \
    "ds_read_b64_tr_b16 v[10:11], v104 offset:10240\n" \
    "ds_read_b64_tr_b16 v[12:13], v104 offset:12288\n" \
    "ds_read_b64_tr_b16 v[14:15], v104 offset:14336\n" \
    "ds_read_b64_tr_b16 v[136:137], v105 offset:8192\n" \
    "ds_read_b64_tr_b16 v[138:139], v105 offset:10240\n" \
    "ds_read_b64_tr_b16 v[140:141], v105 offset:12288\n" \
    "ds_read_b64_tr_b16 v[142:143], v105 offset:14336\n" \
    "s_waitcnt lgkmcnt(4)\n" \
    "ds_read_b64_tr_b16 a[16:17], v106 offset:8192\n" \
    "ds_read_b64_tr_b16 a[18:19], v106 offset:10240\n" \
    "ds_read_b64_tr_b16 a[20:21], v106 offset:12288\n" \
    "ds_read_b64_tr_b16 a[22:23], v106 offset:14336\n" \
    "ds_read_b64_tr_b16 a[24:25], v107 offset:8192\n" \
    "ds_read_b64_tr_b16 a[26:27], v107 offset:10240\n" \
    "ds_read_b64_tr_b16 a[28:29], v107 offset:12288\n" \
    "ds_read_b64_tr_b16 a[30:31], v107 offset:14336\n" \
    "s_waitcnt lgkmcnt(0)\n" \
    "s_add_u32 m0, s73, 8192\n" \
    "s_mov_b32 s62, 2621440\n" \
    "buffer_load_dwordx4 v111, s[64:67], s62 offen lds\n" \
    "s_add_u32 m0, m0, s72\n" \
    "s_mov_b32 s62, 2752512\n" \
    "buffer_load_dwordx4 v111, s[64:67], s62 offen lds\n" \
    "s_add_u32 m0, m0, s72\n" \
    "s_mov_b32 s62, 2629632\n" \
    "buffer_load_dwordx4 v111, s[64:67], s62 offen lds\n" \
    "s_add_u32 m0, m0, s72\n" \
    "s_mov_b32 s62, 2760704\n" \
    "buffer_load_dwordx4 v111, s[64:67], s62 offen lds\n" \
    "s_add_u32 m0, m0, s72\n" \
    "s_mov_b32 s62, 2883584\n" \
    "buffer_load_dwordx4 v111, s[64:67], s62 offen lds\n" \
    "s_add_u32 m0, m0, s72\n" \
    "s_mov_b32 s62, 3014656\n" \
    "buffer_load_dwordx4 v111, s[64:67], s62 offen lds\n" \
    "s_add_u32 m0, m0, s72\n" \
    "s_mov_b32 s62, 2891776\n" \
    "buffer_load_dwordx4 v111, s[64:67], s62 offen lds\n" \
    "s_add_u32 m0, m0, s72\n" \
    "s_mov_b32 s62, 3022848\n" \
    "buffer_load_dwordx4 v111, s[64:67], s62 offen lds\n" \
    "s_waitcnt vmcnt(24)\n" \
    "ds_read_b64_tr_b16 v[16:17], v104 offset:16384\n" \
    "ds_read_b64_tr_b16 v[18:19], v104 offset:18432\n" \
    "ds_read_b64_tr_b16 v[20:21], v104 offset:20480\n" \
    "ds_read_b64_tr_b16 v[22:23], v104 offset:22528\n" \
    "ds_read_b64_tr_b16 v[144:145], v105 offset:16384\n" \
    "ds_read_b64_tr_b16 v[146:147], v105 offset:18432\n" \
    "ds_read_b64_tr_b16 v[148:149], v105 offset:20480\n" \
    "ds_read_b64_tr_b16 v[150:151], v105 offset:22528\n" \
    "s_waitcnt lgkmcnt(4)\n" \
    "ds_read_b64_tr_b16 a[32:33], v106 offset:16384\n" \
    "ds_read_b64_tr_b16 a[34:35], v106 offset:18432\n" \
    "ds_read_b64_tr_b16 a[36:37], v106 offset:20480\n" \
    "ds_read_b64_tr_b16 a[38:39], v106 offset:22528\n" \
    "ds_read_b64_tr_b16 a[40:41], v107 offset:16384\n" \
    "ds_read_b64_tr_b16 a[42:43], v107 offset:18432\n" \
    "ds_read_b64_tr_b16 a[44:45], v107 offset:20480\n" \
    "ds_read_b64_tr_b16 a[46:47], v107 offset:22528\n" \
    "s_waitcnt lgkmcnt(0)\n" \
    "s_add_u32 m0, s73, 16384\n" \
    "s_mov_b32 s62, 3145728\n" \
    "buffer_load_dwordx4 v111, s[64:67], s62 offen lds\n" \
    "s_add_u32 m0, m0, s72\n" \
    "s_mov_b32 s62, 3276800\n" \
    "buffer_load_dwordx4 v111, s[64:67], s62 offen lds\n" \
    "s_add_u32 m0, m0, s72\n" \
    "s_mov_b32 s62, 3153920\n" \
    "buffer_load_dwordx4 v111, s[64:67], s62 offen lds\n" \
    "s_add_u32 m0, m0, s72\n" \
    "s_mov_b32 s62, 3284992\n" \
    "buffer_load_dwordx4 v111, s[64:67], s62 offen lds\n" \
    "s_add_u32 m0, m0, s72\n" \
    "s_mov_b32 s62, 3407872\n" \
    "buffer_load_dwordx4 v111, s[64:67], s62 offen lds\n" \
    "s_add_u32 m0, m0, s72\n" \
    "s_mov_b32 s62, 3538944\n" \
    "buffer_load_dwordx4 v111, s[64:67], s62 offen lds\n" \
    "s_add_u32 m0, m0, s72\n" \
    "s_mov_b32 s62, 3416064\n" \
    "buffer_load_dwordx4 v111, s[64:67], s62 offen lds\n" \
    "s_add_u32 m0, m0, s72\n" \
    "s_mov_b32 s62, 3547136\n" \
    "buffer_load_dwordx4 v111, s[64:67], s62 offen lds\n" \
    "s_waitcnt vmcnt(24)\n" \
    "ds_read_b64_tr_b16 v[24:25], v104 offset:24576\n" \
    "ds_read_b64_tr_b16 v[26:27], v104 offset:26624\n" \
    "ds_read_b64_tr_b16 v[28:29], v104 offset:28672\n" \
    "ds_read_b64_tr_b16 v[30:31], v104 offset:30720\n" \
    "ds_read_b64_tr_b16 v[152:153], v105 offset:24576\n" \
    "ds_read_b64_tr_b16 v[154:155], v105 offset:26624\n" \
    "ds_read_b64_tr_b16 v[156:157], v105 offset:28672\n" \
    "ds_read_b64_tr_b16 v[158:159], v105 offset:30720\n" \
    "s_waitcnt lgkmcnt(4)\n" \
    "ds_read_b64_tr_b16 a[48:49], v106 offset:24576\n" \
    "ds_read_b64_tr_b16 a[50:51], v106 offset:26624\n" \
    "ds_read_b64_tr_b16 a[52:53], v106 offset:28672\n" \
    "ds_read_b64_tr_b16 a[54:55], v106 offset:30720\n" \
    "ds_read_b64_tr_b16 a[56:57], v107 offset:24576\n" \
    "ds_read_b64_tr_b16 a[58:59], v107 offset:26624\n" \
    "ds_read_b64_tr_b16 a[60:61], v107 offset:28672\n" \
    "ds_read_b64_tr_b16 a[62:63], v107 offset:30720\n" \
    "s_waitcnt lgkmcnt(0)\n" \
    "s_add_u32 m0, s73, 24576\n" \
    "s_mov_b32 s62, 3670016\n" \
    "buffer_load_dwordx4 v111, s[64:67], s62 offen lds\n" \
    "s_add_u32 m0, m0, s72\n" \
    "s_mov_b32 s62, 3801088\n" \
    "buffer_load_dwordx4 v111, s[64:67], s62 offen lds\n" \
    "s_add_u32 m0, m0, s72\n" \
    "s_mov_b32 s62, 3678208\n" \
    "buffer_load_dwordx4 v111, s[64:67], s62 offen lds\n" \
    "s_add_u32 m0, m0, s72\n" \
    "s_mov_b32 s62, 3809280\n" \
    "buffer_load_dwordx4 v111, s[64:67], s62 offen lds\n" \
    "s_add_u32 m0, m0, s72\n" \
    "s_mov_b32 s62, 3932160\n" \
    "buffer_load_dwordx4 v111, s[64:67], s62 offen lds\n" \
    "s_add_u32 m0, m0, s72\n" \
    "s_mov_b32 s62, 4063232\n" \
    "buffer_load_dwordx4 v111, s[64:67], s62 offen lds\n" \
    "s_add_u32 m0, m0, s72\n" \
    "s_mov_b32 s62, 3940352\n" \
    "buffer_load_dwordx4 v111, s[64:67], s62 offen lds\n" \
    "s_add_u32 m0, m0, s72\n" \
    "s_mov_b32 s62, 4071424\n" \
    "buffer_load_dwordx4 v111, s[64:67], s62 offen lds\n" \
    "s_waitcnt vmcnt(24)\n" \
    "ds_read_b64_tr_b16 v[32:33], v104 offset:0\n" \
    "ds_read_b64_tr_b16 v[34:35], v104 offset:2048\n" \
    "ds_read_b64_tr_b16 v[36:37], v104 offset:4096\n" \
    "ds_read_b64_tr_b16 v[38:39], v104 offset:6144\n" \
    "ds_read_b64_tr_b16 v[160:161], v105 offset:0\n" \
    "ds_read_b64_tr_b16 v[162:163], v105 offset:2048\n" \
    "ds_read_b64_tr_b16 v[164:165], v105 offset:4096\n" \
    "ds_read_b64_tr_b16 v[166:167], v105 offset:6144\n" \
    "s_waitcnt lgkmcnt(4)\n" \
    "ds_read_b64_tr_b16 a[64:65], v106 offset:0\n" \
    "ds_read_b64_tr_b16 a[66:67], v106 offset:2048\n" \
    "ds_read_b64_tr_b16 a[68:69], v106 offset:4096\n" \
    "ds_read_b64_tr_b16 a[70:71], v106 offset:6144\n" \
    "ds_read_b64_tr_b16 a[72:73], v107 offset:0\n" \
    "ds_read_b64_tr_b16 a[74:75], v107 offset:2048\n" \
    "ds_read_b64_tr_b16 a[76:77], v107 offset:4096\n" \
    "ds_read_b64_tr_b16 a[78:79], v107 offset:6144\n" \
    "s_waitcnt lgkmcnt(0)\n" \
    "s_add_u32 m0, s73, 0\n" \
    "s_mov_b32 s62, 4194304\n" \
    "buffer_load_dwordx4 v111, s[64:67], s62 offen lds\n" \
    "s_add_u32 m0, m0, s72\n" \
    "s_mov_b32 s62, 4325376\n" \
    "buffer_load_dwordx4 v111, s[64:67], s62 offen lds\n" \
    "s_add_u32 m0, m0, s72\n" \
    "s_mov_b32 s62, 4202496\n" \
    "buffer_load_dwordx4 v111, s[64:67], s62 offen lds\n" \
    "s_add_u32 m0, m0, s72\n" \
    "s_mov_b32 s62, 4333568\n" \
    "buffer_load_dwordx4 v111, s[64:67], s62 offen lds\n" \
    "s_add_u32 m0, m0, s72\n" \
    "s_mov_b32 s62, 4456448\n" \
    "buffer_load_dwordx4 v111, s[64:67], s62 offen lds\n" \
    "s_add_u32 m0, m0, s72\n" \
    "s_mov_b32 s62, 4587520\n" \
    "buffer_load_dwordx4 v111, s[64:67], s62 offen lds\n" \
    "s_add_u32 m0, m0, s72\n" \
    "s_mov_b32 s62, 4464640\n" \
    "buffer_load_dwordx4 v111, s[64:67], s62 offen lds\n" \
    "s_add_u32 m0, m0, s72\n" \
    "s_mov_b32 s62, 4595712\n" \
    "buffer_load_dwordx4 v111, s[64:67], s62 offen lds\n" \
    "s_waitcnt vmcnt(24)\n" \
    "ds_read_b64_tr_b16 v[40:41], v104 offset:8192\n" \
    "ds_read_b64_tr_b16 v[42:43], v104 offset:10240\n" \
    "ds_read_b64_tr_b16 v[44:45], v104 offset:12288\n" \
    "ds_read_b64_tr_b16 v[46:47], v104 offset:14336\n" \
    "ds_read_b64_tr_b16 v[168:169], v105 offset:8192\n" \
    "ds_read_b64_tr_b16 v[170:171], v105 offset:10240\n" \
    "ds_read_b64_tr_b16 v[172:173], v105 offset:12288\n" \
    "ds_read_b64_tr_b16 v[174:175], v105 offset:14336\n" \
    "s_waitcnt lgkmcnt(4)\n" \
    "ds_read_b64_tr_b16 a[80:81], v106 offset:8192\n" \
    "ds_read_b64_tr_b16 a[82:83], v106 offset:10240\n" \
    "ds_read_b64_tr_b16 a[84:85], v106 offset:12288\n" \
    "ds_read_b64_tr_b16 a[86:87], v106 offset:14336\n" \
    "ds_read_b64_tr_b16 a[88:89], v107 offset:8192\n" \
    "ds_read_b64_tr_b16 a[90:91], v107 offset:10240\n" \
    "ds_read_b64_tr_b16 a[92:93], v107 offset:12288\n" \
    "ds_read_b64_tr_b16 a[94:95], v107 offset:14336\n" \
    "s_waitcnt lgkmcnt(0)\n" \
    "s_add_u32 m0, s73, 8192\n" \
    "s_mov_b32 s62, 4718592\n" \
    "buffer_load_dwordx4 v111, s[64:67], s62 offen lds\n" \
    "s_add_u32 m0, m0, s72\n" \
    "s_mov_b32 s62, 4849664\n" \
    "buffer_load_dwordx4 v111, s[64:67], s62 offen lds\n" \
    "s_add_u32 m0, m0, s72\n" \
    "s_mov_b32 s62, 4726784\n" \
    "buffer_load_dwordx4 v111, s[64:67], s62 offen lds\n" \
    "s_add_u32 m0, m0, s72\n" \
    "s_mov_b32 s62, 4857856\n" \
    "buffer_load_dwordx4 v111, s[64:67], s62 offen lds\n" \
    "s_add_u32 m0, m0, s72\n" \
    "s_mov_b32 s62, 4980736\n" \
    "buffer_load_dwordx4 v111, s[64:67], s62 offen lds\n" \
    "s_add_u32 m0, m0, s72\n" \
    "s_mov_b32 s62, 5111808\n" \
    "buffer_load_dwordx4 v111, s[64:67], s62 offen lds\n" \
    "s_add_u32 m0, m0, s72\n" \
    "s_mov_b32 s62, 4988928\n" \
    "buffer_load_dwordx4 v111, s[64:67], s62 offen lds\n" \
    "s_add_u32 m0, m0, s72\n" \
    "s_mov_b32 s62, 5120000\n" \
    "buffer_load_dwordx4 v111, s[64:67], s62 offen lds\n" \
    "s_waitcnt vmcnt(24)\n" \
    "ds_read_b64_tr_b16 v[48:49], v104 offset:16384\n" \
    "ds_read_b64_tr_b16 v[50:51], v104 offset:18432\n" \
    "ds_read_b64_tr_b16 v[52:53], v104 offset:20480\n" \
    "ds_read_b64_tr_b16 v[54:55], v104 offset:22528\n" \
    "ds_read_b64_tr_b16 v[176:177], v105 offset:16384\n" \
    "ds_read_b64_tr_b16 v[178:179], v105 offset:18432\n" \
    "ds_read_b64_tr_b16 v[180:181], v105 offset:20480\n" \
    "ds_read_b64_tr_b16 v[182:183], v105 offset:22528\n" \
    "s_waitcnt lgkmcnt(4)\n" \
    "ds_read_b64_tr_b16 a[96:97], v106 offset:16384\n" \
    "ds_read_b64_tr_b16 a[98:99], v106 offset:18432\n" \
    "ds_read_b64_tr_b16 a[100:101], v106 offset:20480\n" \
    "ds_read_b64_tr_b16 a[102:103], v106 offset:22528\n" \
    "ds_read_b64_tr_b16 a[104:105], v107 offset:16384\n" \
    "ds_read_b64_tr_b16 a[106:107], v107 offset:18432\n" \
    "ds_read_b64_tr_b16 a[108:109], v107 offset:20480\n" \
    "ds_read_b64_tr_b16 a[110:111], v107 offset:22528\n" \
    "s_waitcnt lgkmcnt(0)\n" \
    "s_add_u32 m0, s73, 16384\n" \
    "s_mov_b32 s62, 5242880\n" \
    "buffer_load_dwordx4 v111, s[64:67], s62 offen lds\n" \
    "s_add_u32 m0, m0, s72\n" \
    "s_mov_b32 s62, 5373952\n" \
    "buffer_load_dwordx4 v111, s[64:67], s62 offen lds\n" \
    "s_add_u32 m0, m0, s72\n" \
    "s_mov_b32 s62, 5251072\n" \
    "buffer_load_dwordx4 v111, s[64:67], s62 offen lds\n" \
    "s_add_u32 m0, m0, s72\n" \
    "s_mov_b32 s62, 5382144\n" \
    "buffer_load_dwordx4 v111, s[64:67], s62 offen lds\n" \
    "s_add_u32 m0, m0, s72\n" \
    "s_mov_b32 s62, 5505024\n" \
    "buffer_load_dwordx4 v111, s[64:67], s62 offen lds\n" \
    "s_add_u32 m0, m0, s72\n" \
    "s_mov_b32 s62, 5636096\n" \
    "buffer_load_dwordx4 v111, s[64:67], s62 offen lds\n" \
    "s_add_u32 m0, m0, s72\n" \
    "s_mov_b32 s62, 5513216\n" \
    "buffer_load_dwordx4 v111, s[64:67], s62 offen lds\n" \
    "s_add_u32 m0, m0, s72\n" \
    "s_mov_b32 s62, 5644288\n" \
    "buffer_load_dwordx4 v111, s[64:67], s62 offen lds\n" \
    "s_waitcnt vmcnt(24)\n" \
    "ds_read_b64_tr_b16 v[56:57], v104 offset:24576\n" \
    "ds_read_b64_tr_b16 v[58:59], v104 offset:26624\n" \
    "ds_read_b64_tr_b16 v[60:61], v104 offset:28672\n" \
    "ds_read_b64_tr_b16 v[62:63], v104 offset:30720\n" \
    "ds_read_b64_tr_b16 v[184:185], v105 offset:24576\n" \
    "ds_read_b64_tr_b16 v[186:187], v105 offset:26624\n" \
    "ds_read_b64_tr_b16 v[188:189], v105 offset:28672\n" \
    "ds_read_b64_tr_b16 v[190:191], v105 offset:30720\n" \
    "s_waitcnt lgkmcnt(4)\n" \
    "ds_read_b64_tr_b16 a[112:113], v106 offset:24576\n" \
    "ds_read_b64_tr_b16 a[114:115], v106 offset:26624\n" \
    "ds_read_b64_tr_b16 a[116:117], v106 offset:28672\n" \
    "ds_read_b64_tr_b16 a[118:119], v106 offset:30720\n" \
    "ds_read_b64_tr_b16 a[120:121], v107 offset:24576\n" \
    "ds_read_b64_tr_b16 a[122:123], v107 offset:26624\n" \
    "ds_read_b64_tr_b16 a[124:125], v107 offset:28672\n" \
    "ds_read_b64_tr_b16 a[126:127], v107 offset:30720\n" \
    "s_waitcnt lgkmcnt(0)\n" \
    "s_add_u32 m0, s73, 24576\n" \
    "s_mov_b32 s62, 5767168\n" \
    "buffer_load_dwordx4 v111, s[64:67], s62 offen lds\n" \
    "s_add_u32 m0, m0, s72\n" \
    "s_mov_b32 s62, 5898240\n" \
    "buffer_load_dwordx4 v111, s[64:67], s62 offen lds\n" \
    "s_add_u32 m0, m0, s72\n" \
    "s_mov_b32 s62, 5775360\n" \
    "buffer_load_dwordx4 v111, s[64:67], s62 offen lds\n" \
    "s_add_u32 m0, m0, s72\n" \
    "s_mov_b32 s62, 5906432\n" \
    "buffer_load_dwordx4 v111, s[64:67], s62 offen lds\n" \
    "s_add_u32 m0, m0, s72\n" \
    "s_mov_b32 s62, 6029312\n" \
    "buffer_load_dwordx4 v111, s[64:67], s62 offen lds\n" \
    "s_add_u32 m0, m0, s72\n" \
    "s_mov_b32 s62, 6160384\n" \
    "buffer_load_dwordx4 v111, s[64:67], s62 offen lds\n" \
    "s_add_u32 m0, m0, s72\n" \
    "s_mov_b32 s62, 6037504\n" \
    "buffer_load_dwordx4 v111, s[64:67], s62 offen lds\n" \
    "s_add_u32 m0, m0, s72\n" \
    "s_mov_b32 s62, 6168576\n" \
    "buffer_load_dwordx4 v111, s[64:67], s62 offen lds\n" \
    "s_waitcnt vmcnt(24)\n" \
    "ds_read_b64_tr_b16 v[64:65], v104 offset:0\n" \
    "ds_read_b64_tr_b16 v[66:67], v104 offset:2048\n" \
    "ds_read_b64_tr_b16 v[68:69], v104 offset:4096\n" \
    "ds_read_b64_tr_b16 v[70:71], v104 offset:6144\n" \
    "ds_read_b64_tr_b16 v[192:193], v105 offset:0\n" \
    "ds_read_b64_tr_b16 v[194:195], v105 offset:2048\n" \
    "ds_read_b64_tr_b16 v[196:197], v105 offset:4096\n" \
    "ds_read_b64_tr_b16 v[198:199], v105 offset:6144\n" \
    "s_waitcnt lgkmcnt(4)\n" \
    "ds_read_b64_tr_b16 a[128:129], v106 offset:0\n" \
    "ds_read_b64_tr_b16 a[130:131], v106 offset:2048\n" \
    "ds_read_b64_tr_b16 a[132:133], v106 offset:4096\n" \
    "ds_read_b64_tr_b16 a[134:135], v106 offset:6144\n" \
    "ds_read_b64_tr_b16 a[136:137], v107 offset:0\n" \
    "ds_read_b64_tr_b16 a[138:139], v107 offset:2048\n" \
    "ds_read_b64_tr_b16 a[140:141], v107 offset:4096\n" \
    "ds_read_b64_tr_b16 a[142:143], v107 offset:6144\n" \
    "s_waitcnt lgkmcnt(0)\n" \
    "s_add_u32 m0, s73, 0\n" \
    "s_mov_b32 s62, 6291456\n" \
    "buffer_load_dwordx4 v111, s[64:67], s62 offen lds\n" \
    "s_add_u32 m0, m0, s72\n" \
    "s_mov_b32 s62, 6422528\n" \
    "buffer_load_dwordx4 v111, s[64:67], s62 offen lds\n" \
    "s_add_u32 m0, m0, s72\n" \
    "s_mov_b32 s62, 6299648\n" \
    "buffer_load_dwordx4 v111, s[64:67], s62 offen lds\n" \
    "s_add_u32 m0, m0, s72\n" \
    "s_mov_b32 s62, 6430720\n" \
    "buffer_load_dwordx4 v111, s[64:67], s62 offen lds\n" \
    "s_add_u32 m0, m0, s72\n" \
    "s_mov_b32 s62, 6553600\n" \
    "buffer_load_dwordx4 v111, s[64:67], s62 offen lds\n" \
    "s_add_u32 m0, m0, s72\n" \
    "s_mov_b32 s62, 6684672\n" \
    "buffer_load_dwordx4 v111, s[64:67], s62 offen lds\n" \
    "s_add_u32 m0, m0, s72\n" \
    "s_mov_b32 s62, 6561792\n" \
    "buffer_load_dwordx4 v111, s[64:67], s62 offen lds\n" \
    "s_add_u32 m0, m0, s72\n" \
    "s_mov_b32 s62, 6692864\n" \
    "buffer_load_dwordx4 v111, s[64:67], s62 offen lds\n" \
    "s_waitcnt vmcnt(24)\n" \
    "ds_read_b64_tr_b16 v[72:73], v104 offset:8192\n" \
    "ds_read_b64_tr_b16 v[74:75], v104 offset:10240\n" \
    "ds_read_b64_tr_b16 v[76:77], v104 offset:12288\n" \
    "ds_read_b64_tr_b16 v[78:79], v104 offset:14336\n" \
    "ds_read_b64_tr_b16 v[200:201], v105 offset:8192\n" \
    "ds_read_b64_tr_b16 v[202:203], v105 offset:10240\n" \
    "ds_read_b64_tr_b16 v[204:205], v105 offset:12288\n" \
    "ds_read_b64_tr_b16 v[206:207], v105 offset:14336\n" \
    "s_waitcnt lgkmcnt(4)\n" \
    "ds_read_b64_tr_b16 a[144:145], v106 offset:8192\n" \
    "ds_read_b64_tr_b16 a[146:147], v106 offset:10240\n" \
    "ds_read_b64_tr_b16 a[148:149], v106 offset:12288\n" \
    "ds_read_b64_tr_b16 a[150:151], v106 offset:14336\n" \
    "ds_read_b64_tr_b16 a[152:153], v107 offset:8192\n" \
    "ds_read_b64_tr_b16 a[154:155], v107 offset:10240\n" \
    "ds_read_b64_tr_b16 a[156:157], v107 offset:12288\n" \
    "ds_read_b64_tr_b16 a[158:159], v107 offset:14336\n" \
    "s_waitcnt lgkmcnt(0)\n" \
    "s_add_u32 m0, s73, 8192\n" \
    "s_mov_b32 s62, 6815744\n" \
    "buffer_load_dwordx4 v111, s[64:67], s62 offen lds\n" \
    "s_add_u32 m0, m0, s72\n" \
    "s_mov_b32 s62, 6946816\n" \
    "buffer_load_dwordx4 v111, s[64:67], s62 offen lds\n" \
    "s_add_u32 m0, m0, s72\n" \
    "s_mov_b32 s62, 6823936\n" \
    "buffer_load_dwordx4 v111, s[64:67], s62 offen lds\n" \
    "s_add_u32 m0, m0, s72\n" \
    "s_mov_b32 s62, 6955008\n" \
    "buffer_load_dwordx4 v111, s[64:67], s62 offen lds\n" \
    "s_add_u32 m0, m0, s72\n" \
    "s_mov_b32 s62, 7077888\n" \
    "buffer_load_dwordx4 v111, s[64:67], s62 offen lds\n" \
    "s_add_u32 m0, m0, s72\n" \
    "s_mov_b32 s62, 7208960\n" \
    "buffer_load_dwordx4 v111, s[64:67], s62 offen lds\n" \
    "s_add_u32 m0, m0, s72\n" \
    "s_mov_b32 s62, 7086080\n" \
    "buffer_load_dwordx4 v111, s[64:67], s62 offen lds\n" \
    "s_add_u32 m0, m0, s72\n" \
    "s_mov_b32 s62, 7217152\n" \
    "buffer_load_dwordx4 v111, s[64:67], s62 offen lds\n" \
    "s_waitcnt vmcnt(24)\n" \
    "ds_read_b64_tr_b16 v[80:81], v104 offset:16384\n" \
    "ds_read_b64_tr_b16 v[82:83], v104 offset:18432\n" \
    "ds_read_b64_tr_b16 v[84:85], v104 offset:20480\n" \
    "ds_read_b64_tr_b16 v[86:87], v104 offset:22528\n" \
    "ds_read_b64_tr_b16 v[208:209], v105 offset:16384\n" \
    "ds_read_b64_tr_b16 v[210:211], v105 offset:18432\n" \
    "ds_read_b64_tr_b16 v[212:213], v105 offset:20480\n" \
    "ds_read_b64_tr_b16 v[214:215], v105 offset:22528\n" \
    "s_waitcnt lgkmcnt(4)\n" \
    "ds_read_b64_tr_b16 a[160:161], v106 offset:16384\n" \
    "ds_read_b64_tr_b16 a[162:163], v106 offset:18432\n" \
    "ds_read_b64_tr_b16 a[164:165], v106 offset:20480\n" \
    "ds_read_b64_tr_b16 a[166:167], v106 offset:22528\n" \
    "ds_read_b64_tr_b16 a[168:169], v107 offset:16384\n" \
    "ds_read_b64_tr_b16 a[170:171], v107 offset:18432\n" \
    "ds_read_b64_tr_b16 a[172:173], v107 offset:20480\n" \
    "ds_read_b64_tr_b16 a[174:175], v107 offset:22528\n" \
    "s_waitcnt lgkmcnt(0)\n" \
    "s_add_u32 m0, s73, 16384\n" \
    "s_mov_b32 s62, 7340032\n" \
    "buffer_load_dwordx4 v111, s[64:67], s62 offen lds\n" \
    "s_add_u32 m0, m0, s72\n" \
    "s_mov_b32 s62, 7471104\n" \
    "buffer_load_dwordx4 v111, s[64:67], s62 offen lds\n" \
    "s_add_u32 m0, m0, s72\n" \
    "s_mov_b32 s62, 7348224\n" \
    "buffer_load_dwordx4 v111, s[64:67], s62 offen lds\n" \
    "s_add_u32 m0, m0, s72\n" \
    "s_mov_b32 s62, 7479296\n" \
    "buffer_load_dwordx4 v111, s[64:67], s62 offen lds\n" \
    "s_add_u32 m0, m0, s72\n" \
    "s_mov_b32 s62, 7602176\n" \
    "buffer_load_dwordx4 v111, s[64:67], s62 offen lds\n" \
    "s_add_u32 m0, m0, s72\n" \
    "s_mov_b32 s62, 7733248\n" \
    "buffer_load_dwordx4 v111, s[64:67], s62 offen lds\n" \
    "s_add_u32 m0, m0, s72\n" \
    "s_mov_b32 s62, 7610368\n" \
    "buffer_load_dwordx4 v111, s[64:67], s62 offen lds\n" \
    "s_add_u32 m0, m0, s72\n" \
    "s_mov_b32 s62, 7741440\n" \
    "buffer_load_dwordx4 v111, s[64:67], s62 offen lds\n" \
    "s_waitcnt vmcnt(24)\n" \
    "ds_read_b64_tr_b16 v[88:89], v104 offset:24576\n" \
    "ds_read_b64_tr_b16 v[90:91], v104 offset:26624\n" \
    "ds_read_b64_tr_b16 v[92:93], v104 offset:28672\n" \
    "ds_read_b64_tr_b16 v[94:95], v104 offset:30720\n" \
    "ds_read_b64_tr_b16 v[216:217], v105 offset:24576\n" \
    "ds_read_b64_tr_b16 v[218:219], v105 offset:26624\n" \
    "ds_read_b64_tr_b16 v[220:221], v105 offset:28672\n" \
    "ds_read_b64_tr_b16 v[222:223], v105 offset:30720\n" \
    "s_waitcnt lgkmcnt(4)\n" \
    "ds_read_b64_tr_b16 a[176:177], v106 offset:24576\n" \
    "ds_read_b64_tr_b16 a[178:179], v106 offset:26624\n" \
    "ds_read_b64_tr_b16 a[180:181], v106 offset:28672\n" \
    "ds_read_b64_tr_b16 a[182:183], v106 offset:30720\n" \
    "ds_read_b64_tr_b16 a[184:185], v107 offset:24576\n" \
    "ds_read_b64_tr_b16 a[186:187], v107 offset:26624\n" \
    "ds_read_b64_tr_b16 a[188:189], v107 offset:28672\n" \
    "ds_read_b64_tr_b16 a[190:191], v107 offset:30720\n" \
    "s_waitcnt lgkmcnt(0)\n" \
    "s_add_u32 m0, s73, 24576\n" \
    "s_mov_b32 s62, 7864320\n" \
    "buffer_load_dwordx4 v111, s[64:67], s62 offen lds\n" \
    "s_add_u32 m0, m0, s72\n" \
    "s_mov_b32 s62, 7995392\n" \
    "buffer_load_dwordx4 v111, s[64:67], s62 offen lds\n" \
    "s_add_u32 m0, m0, s72\n" \
    "s_mov_b32 s62, 7872512\n" \
    "buffer_load_dwordx4 v111, s[64:67], s62 offen lds\n" \
    "s_add_u32 m0, m0, s72\n" \
    "s_mov_b32 s62, 8003584\n" \
    "buffer_load_dwordx4 v111, s[64:67], s62 offen lds\n" \
    "s_add_u32 m0, m0, s72\n" \
    "s_mov_b32 s62, 8126464\n" \
    "buffer_load_dwordx4 v111, s[64:67], s62 offen lds\n" \
    "s_add_u32 m0, m0, s72\n" \
    "s_mov_b32 s62, 8257536\n" \
    "buffer_load_dwordx4 v111, s[64:67], s62 offen lds\n" \
    "s_add_u32 m0, m0, s72\n" \
    "s_mov_b32 s62, 8134656\n" \
    "buffer_load_dwordx4 v111, s[64:67], s62 offen lds\n" \
    "s_add_u32 m0, m0, s72\n" \
    "s_mov_b32 s62, 8265728\n" \
    "buffer_load_dwordx4 v111, s[64:67], s62 offen lds\n" \
    "s_waitcnt vmcnt(24)\n" \
    "ds_read_b64_tr_b16 v[96:97], v104 offset:0\n" \
    "ds_read_b64_tr_b16 v[98:99], v104 offset:2048\n" \
    "ds_read_b64_tr_b16 v[100:101], v104 offset:4096\n" \
    "ds_read_b64_tr_b16 v[102:103], v104 offset:6144\n" \
    "ds_read_b64_tr_b16 v[224:225], v105 offset:0\n" \
    "ds_read_b64_tr_b16 v[226:227], v105 offset:2048\n" \
    "ds_read_b64_tr_b16 v[228:229], v105 offset:4096\n" \
    "ds_read_b64_tr_b16 v[230:231], v105 offset:6144\n" \
    "s_waitcnt lgkmcnt(4)\n" \
    "ds_read_b64_tr_b16 a[192:193], v106 offset:0\n" \
    "ds_read_b64_tr_b16 a[194:195], v106 offset:2048\n" \
    "ds_read_b64_tr_b16 a[196:197], v106 offset:4096\n" \
    "ds_read_b64_tr_b16 a[198:199], v106 offset:6144\n" \
    "ds_read_b64_tr_b16 a[200:201], v107 offset:0\n" \
    "ds_read_b64_tr_b16 a[202:203], v107 offset:2048\n" \
    "ds_read_b64_tr_b16 a[204:205], v107 offset:4096\n" \
    "ds_read_b64_tr_b16 a[206:207], v107 offset:6144\n" \
    "s_waitcnt lgkmcnt(0)\n" \
    "ds_write_b128 v115, v[0:3] offset:0\n" \
    "ds_write_b128 v115, v[4:7] offset:1024\n" \
    "ds_write_b128 v115, v[8:11] offset:2048\n" \
    "ds_write_b128 v115, v[12:15] offset:3072\n" \
    "ds_write_b128 v115, v[16:19] offset:4096\n" \
    "ds_write_b128 v115, v[20:23] offset:5120\n" \
    "ds_write_b128 v115, v[24:27] offset:6144\n" \
    "ds_write_b128 v115, v[28:31] offset:7168\n" \
    "s_waitcnt lgkmcnt(0)\n" \
    "s_waitcnt vmcnt(16)\n" \
    "ds_read_b64_tr_b16 v[0:1], v104 offset:8192\n" \
    "ds_read_b64_tr_b16 v[2:3], v104 offset:10240\n" \
    "ds_read_b64_tr_b16 v[4:5], v104 offset:12288\n" \
    "ds_read_b64_tr_b16 v[6:7], v104 offset:14336\n" \
    "ds_read_b64_tr_b16 v[232:233], v105 offset:8192\n" \
    "ds_read_b64_tr_b16 v[234:235], v105 offset:10240\n" \
    "ds_read_b64_tr_b16 v[236:237], v105 offset:12288\n" \
    "ds_read_b64_tr_b16 v[238:239], v105 offset:14336\n" \
    "s_waitcnt lgkmcnt(4)\n" \
    "ds_read_b64_tr_b16 a[208:209], v106 offset:8192\n" \
    "ds_read_b64_tr_b16 a[210:211], v106 offset:10240\n" \
    "ds_read_b64_tr_b16 a[212:213], v106 offset:12288\n" \
    "ds_read_b64_tr_b16 a[214:215], v106 offset:14336\n" \
    "ds_read_b64_tr_b16 a[216:217], v107 offset:8192\n" \
    "ds_read_b64_tr_b16 a[218:219], v107 offset:10240\n" \
    "ds_read_b64_tr_b16 a[220:221], v107 offset:12288\n" \
    "ds_read_b64_tr_b16 a[222:223], v107 offset:14336\n" \
    "s_waitcnt lgkmcnt(0)\n" \
    "ds_write_b128 v115, v[32:35] offset:8192\n" \
    "ds_write_b128 v115, v[36:39] offset:9216\n" \
    "ds_write_b128 v115, v[40:43] offset:10240\n" \
    "ds_write_b128 v115, v[44:47] offset:11264\n" \
    "ds_write_b128 v115, v[48:51] offset:12288\n" \
    "ds_write_b128 v115, v[52:55] offset:13312\n" \
    "ds_write_b128 v115, v[56:59] offset:14336\n" \
    "ds_write_b128 v115, v[60:63] offset:15360\n" \
    "s_waitcnt lgkmcnt(0)\n" \
    "s_waitcnt vmcnt(8)\n" \
    "ds_read_b64_tr_b16 v[8:9], v104 offset:16384\n" \
    "ds_read_b64_tr_b16 v[10:11], v104 offset:18432\n" \
    "ds_read_b64_tr_b16 v[12:13], v104 offset:20480\n" \
    "ds_read_b64_tr_b16 v[14:15], v104 offset:22528\n" \
    "ds_read_b64_tr_b16 v[240:241], v105 offset:16384\n" \
    "ds_read_b64_tr_b16 v[242:243], v105 offset:18432\n" \
    "ds_read_b64_tr_b16 v[244:245], v105 offset:20480\n" \
    "ds_read_b64_tr_b16 v[246:247], v105 offset:22528\n" \
    "s_waitcnt lgkmcnt(4)\n" \
    "ds_read_b64_tr_b16 a[224:225], v106 offset:16384\n" \
    "ds_read_b64_tr_b16 a[226:227], v106 offset:18432\n" \
    "ds_read_b64_tr_b16 a[228:229], v106 offset:20480\n" \
    "ds_read_b64_tr_b16 a[230:231], v106 offset:22528\n" \
    "ds_read_b64_tr_b16 a[232:233], v107 offset:16384\n" \
    "ds_read_b64_tr_b16 a[234:235], v107 offset:18432\n" \
    "ds_read_b64_tr_b16 a[236:237], v107 offset:20480\n" \
    "ds_read_b64_tr_b16 a[238:239], v107 offset:22528\n" \
    "s_waitcnt lgkmcnt(0)\n" \
    "ds_write_b128 v115, v[64:67] offset:16384\n" \
    "ds_write_b128 v115, v[68:71] offset:17408\n" \
    "ds_write_b128 v115, v[72:75] offset:18432\n" \
    "ds_write_b128 v115, v[76:79] offset:19456\n" \
    "ds_write_b128 v115, v[80:83] offset:20480\n" \
    "ds_write_b128 v115, v[84:87] offset:21504\n" \
    "ds_write_b128 v115, v[88:91] offset:22528\n" \
    "ds_write_b128 v115, v[92:95] offset:23552\n" \
    "s_waitcnt lgkmcnt(0)\n" \
    "s_waitcnt vmcnt(0)\n" \
    "ds_read_b64_tr_b16 v[16:17], v104 offset:24576\n" \
    "ds_read_b64_tr_b16 v[18:19], v104 offset:26624\n" \
    "ds_read_b64_tr_b16 v[20:21], v104 offset:28672\n" \
    "ds_read_b64_tr_b16 v[22:23], v104 offset:30720\n" \
    "ds_read_b64_tr_b16 v[248:249], v105 offset:24576\n" \
    "ds_read_b64_tr_b16 v[250:251], v105 offset:26624\n" \
    "ds_read_b64_tr_b16 v[252:253], v105 offset:28672\n" \
    "ds_read_b64_tr_b16 v[254:255], v105 offset:30720\n" \
    "s_waitcnt lgkmcnt(4)\n" \
    "ds_read_b64_tr_b16 a[240:241], v106 offset:24576\n" \
    "ds_read_b64_tr_b16 a[242:243], v106 offset:26624\n" \
    "ds_read_b64_tr_b16 a[244:245], v106 offset:28672\n" \
    "ds_read_b64_tr_b16 a[246:247], v106 offset:30720\n" \
    "ds_read_b64_tr_b16 a[248:249], v107 offset:24576\n" \
    "ds_read_b64_tr_b16 a[250:251], v107 offset:26624\n" \
    "ds_read_b64_tr_b16 a[252:253], v107 offset:28672\n" \
    "ds_read_b64_tr_b16 a[254:255], v107 offset:30720\n" \
    "s_waitcnt lgkmcnt(0)\n" \
    "ds_write_b128 v115, v[96:99] offset:24576\n" \
    "ds_write_b128 v115, v[100:103] offset:25600\n" \
    "ds_write_b128 v115, v[0:3] offset:26624\n" \
    "ds_write_b128 v115, v[4:7] offset:27648\n" \
    "ds_write_b128 v115, v[8:11] offset:28672\n" \
    "ds_write_b128 v115, v[12:15] offset:29696\n" \
    "ds_write_b128 v115, v[16:19] offset:30720\n" \
    "ds_write_b128 v115, v[20:23] offset:31744\n" \
    "s_waitcnt lgkmcnt(0)\n" \
    "s_barrier\n" \
    "Lqk_t0:\n" \
    "s_cmp_eq_u32 s90, 0\n" \
    "s_cbranch_scc1 Lqk_t0_x\n" \
    "s_sub_u32 s90, s90, 1\n" \
    "buffer_load_dwordx2 v[62:63], off, s[40:43], s56 sc1\n" \
    "s_waitcnt vmcnt(0)\n" \
    "v_min_u32 v62, v62, v63\n" \
    "v_cmp_gt_u32 vcc, 2, v62\n" \
    "s_cbranch_vccnz Lqk_t0\n" \
    "Lqk_t0_x:\n" \
    "s_mov_b32 s62, 12582912\n" \
    "s_mov_b32 s63, 29360128\n" \
    "buffer_load_dwordx4 v[64:67], v117, s[40:43], s62 offen sc1\n" \
    "buffer_load_dwordx4 v[68:71], v117, s[40:43], s62 offen offset:1024 sc1\n" \
    "buffer_load_dwordx4 v[16:19], v118, s[40:43], s63 offen sc1\n" \
    "buffer_load_dwordx4 v[20:23], v118, s[40:43], s63 offen offset:64 sc1\n" \
    "s_waitcnt vmcnt(3)\n" \
    "ds_write_b128 v125, v[64:67]\n" \
    "s_waitcnt vmcnt(2)\n" \
    "ds_write_b128 v125, v[68:71] offset:1024\n" \
    "s_mov_b32 s62, 33024\n" \
    "Lqk_s:\n" \
    "buffer_load_dword v1, off, s[40:43], s62 sc1\n" \
    "s_cmp_eq_u32 s90, 0\n" \
    "s_cbranch_scc1 Lqk_s_x\n" \
    "s_sub_u32 s90, s90, 1\n" \
    "s_waitcnt vmcnt(0)\n" \
    "v_cmp_gt_u32 vcc, 1, v1\n" \
    "s_cbranch_vccnz Lqk_s\n" \
    "Lqk_s_x:\n" \
    "s_waitcnt vmcnt(0) lgkmcnt(0)\n" \
    "s_barrier\n" \
    "v_readfirstlane_b32 s10, v1\n" \
    "s_and_b32 s21, s10, 0x7000\n" \
    "s_lshr_b32 s22, s10, 16\n" \
    "s_and_b32 s22, s22, 0x7000\n" \
    "s_add_u32 s22, s22, 256\n" \
    "s_and_b32 s52, s10, 0x2000\n" \
    "s_lshl_b32 s7, s10, 13\n" \
    "s_and_b32 s7, s7, 0x4000\n" \
    "s_bfe_u32 s6, s10, 0x10001\n" \
    "s_add_u32 s6, s6, 4\n" \
    "s_mov_b32 s57, 0\n" \
    "s_mov_b32 s62, 0\n" \
    "s_mov_b32 s63, 0\n" \
    "s_mov_b32 s64, 0\n" \
    "s_mov_b32 s65, 0\n" \
    "s_mov_b32 s66, 0\n" \
    "s_mov_b32 s67, 0\n" \
    "s_mov_b32 s68, 0\n" \
    "s_mov_b32 s69, 0\n" \
    "s_mov_b32 s70, 0\n" \
    "s_mov_b32 s71, 0\n" \
    "s_mov_b32 s72, 0\n" \
    "s_mov_b32 s73, 0\n" \
    "s_mov_b32 s74, 0\n" \
    "s_mov_b32 s75, 0\n" \
    "s_mov_b32 s76, 0\n" \
    "s_mov_b32 s77, 0\n" \
    "s_mov_b32 s78, 0\n" \
    "s_mov_b32 s79, 0\n" \
    "s_mov_b32 s80, 0\n" \
    "s_mov_b32 s81, 0\n" \
    "s_mov_b32 s82, 0\n" \
    "s_mov_b32 s83, 0\n" \
    "s_mov_b32 s84, 0\n" \
    "s_mov_b32 s85, 0\n" \
    "s_mov_b32 s86, 0\n" \
    "s_mov_b32 s87, 0\n" \
    "s_mov_b32 s88, 0\n" \
    "s_mov_b32 s89, 0\n" \
    "s_mov_b32 s92, 0\n" \
    "s_mov_b32 s93, 0\n" \
    "s_mov_b32 s94, 0\n" \
    "s_mov_b32 s95, 0\n" \
    "s_sub_u32 s16, s56, 40960\n" \
    "s_lshl_b32 s16, s16, 7\n" \
    "s_add_u32 s16, s16, 2097152\n" \
    "s_mov_b32 s13, s46\n" \
    "s_mov_b32 s46, 0\n" \
    "s_xor_b32 s62, s21, 0x1000\n" \
    "s_add_u32 s55, s55, s62\n" \
    "v_mbcnt_lo_u32_b32 v25, -1, 0\n" \
    "v_cmp_gt_u32 vcc, 16, v25\n" \
    "v_lshlrev_b32 v25, 7, v25\n" \
    "v_add_u32 v25, 256, v25\n" \
    "v_cndmask_b32 v25, 0, v25, vcc\n" \
    "v_mov_b32 v26, -1\n" \
    "v_bfrev_b32 v27, 1\n" \
    "v_cndmask_b32 v26, v26, v27, vcc\n" \
    "s_lshl_b32 s15, s54, 11\n" \
    "s_add_u32 s15, s15, 147456\n" \
    "s_lshl_b32 s17, s54, 10\n" \
    "s_add_u32 s17, s17, 131072\n" \
    "s_mov_b32 s26, -1\n" \
    "s_mov_b32 s27, 0\n" \
    "s_mov_b32 s28, 0x3e0293ee\n" \
    "s_mov_b32 s29, 0x3e0293ee\n" \
    "v_and_b32 v6, 0x7ff, v119\n" \
    "v_mov_b32 v7, 0xffffff00\n" \
    "v_cndmask_b32 v125, v7, v6, s[26:27]\n" \
    "Lqk_step:\n" \
    "v_mov_b64 v[56:57], -1\n" \
    "v_mov_b64 v[58:59], -1\n" \
    "v_mov_b32 v24, s57\n" \
    "v_mov_b64 v[0:1], 0\n" \
    "v_mov_b64 v[2:3], 0\n" \
    "v_mov_b64 v[4:5], 0\n" \
    "v_mov_b64 v[6:7], 0\n" \
    "v_mov_b64 v[8:9], 0\n" \
    "v_mov_b64 v[10:11], 0\n" \
    "v_mov_b64 v[12:13], 0\n" \
    "v_mov_b64 v[14:15], 0\n" \
    "ds_read_b128 v[40:43], v115 offset:0\n" \
    "ds_read_b128 v[44:47], v115 offset:1024\n" \
    "ds_read_b128 v[48:51], v115 offset:2048\n" \
    "ds_read_b128 v[52:55], v115 offset:3072\n" \
    "ds_read_b128 v[80:83], v114 offset:16384\n" \
    "ds_read_b128 v[84:87], v114 offset:17408\n" \
    "ds_read_b128 v[88:91], v114 offset:18432\n" \
    "ds_read_b128 v[92:95], v114 offset:19456\n" \
    "ds_read_b128 v[96:99], v114 offset:20480\n" \
    "ds_read_b128 v[100:103], v114 offset:21504\n" \
    "ds_read_b128 v[104:107], v114 offset:22528\n" \
    "ds_read_b128 v[108:111], v114 offset:23552\n" \
    "s_lshl_b32 s58, s6, 15\n" \
    "s_add_u32 s58, s58, s52\n" \
    "s_add_u32 s58, s58, 65536\n" \
    "s_add_u32 s58, s58, s7\n" \
    "s_add_u32 s6, s57, 1\n" \
    "s_and_b32 s6, s6, 3\n" \
    "s_and_b32 s62, s57, 3\n" \
    "s_add_u32 s59, s58, 4096\n" \
    "s_xor_b32 s60, s58, 0x4000\n" \
    "s_xor_b32 s61, s59, 0x4000\n" \
    "s_lshl_b32 s99, s62, 17\n" \
    "s_add_u32 s99, s99, s52\n" \
    "s_add_u32 s99, s99, 524288\n" \
    "s_add_u32 s20, s99, 0x80000\n" \
    "s_xor_b32 s98, s99, 0x40000\n" \
    "s_sub_u32 s62, s57, 1\n" \
    "s_min_u32 s62, s62, 64\n" \
    "s_lshl_b32 s62, s62, 16\n" \
    "s_add_u32 s23, s62, 7340032\n" \
    "s_add_u32 s24, s62, s16\n" \
    "s_add_u32 s62, s57, 1\n" \
    "s_min_u32 s62, s62, 63\n" \
    "s_lshr_b32 s25, s62, 3\n" \
    "s_lshl_b32 s25, s25, 9\n" \
    "s_add_u32 s25, s25, s56\n" \
    "s_lshl_b32 s62, s62, 18\n" \
    "s_add_u32 s96, s62, 12582912\n" \
    "s_add_u32 s97, s62, 29360128\n" \
    "s_mov_b32 m0, s17\n" \
    "s_mov_b64 s[18:19], -1\n" \
    "s_waitcnt lgkmcnt(0)\n" \
    "s_cmp_lg_u32 s54, 1\n" \
    "s_cbranch_scc1 Lqk_t_x\n" \
    "s_and_b32 s62, s57, 7\n" \
    "s_cmp_lg_u32 s62, 7\n" \
    "s_cbranch_scc1 Lqk_t_x\n" \
    "Lqk_t:\n" \
    "s_cmp_eq_u32 s90, 0\n" \
    "s_cbranch_scc1 Lqk_t_x\n" \
    "s_sub_u32 s90, s90, 1\n" \
    "buffer_load_dwordx2 v[62:63], off, s[40:43], s25 sc1\n" \
    "s_waitcnt vmcnt(0)\n" \
    "v_min_u32 v62, v62, v63\n" \
    "v_cmp_gt_u32 vcc, 2, v62\n" \
    "s_cbranch_vccnz Lqk_t\n" \
    "Lqk_t_x:\n" \
    "s_cmp_lg_u32 s54, 0\n" \
    "s_cbranch_scc1 Lqk_c_x\n" \
    "v_add_u32 v27, s57, v26\n" \
    "Lqk_c:\n" \
    "buffer_load_dword v38, v25, s[40:43], s22 offen sc1\n" \
    "s_cmp_eq_u32 s90, 0\n" \
    "s_cbranch_scc1 Lqk_c_x\n" \
    "s_sub_u32 s90, s90, 1\n" \
    "s_waitcnt vmcnt(0)\n" \
    "v_cmp_eq_u32 vcc, v27, v38\n" \
    "s_cbranch_vccnz Lqk_c\n" \
    "s_mov_b32 s62, s53\n" \
    "Lqk_c_d:\n" \
    "s_sub_u32 s62, s62, 1\n" \
    "s_cbranch_scc0 Lqk_c_d\n" \
    "Lqk_c_x:\n" \
    "s_barrier\n" \
    "buffer_load_dwordx4 v112, s[40:43], s58 offen sc1 lds\n" \
    "s_add_u32 m0, m0, 4096\n" \
    "s_nop 11\n" \
    "buffer_load_dwordx4 v112, s[40:43], s59 offen sc1 lds\n" \
    "s_add_u32 m0, m0, 4096\n" \
    "s_nop 11\n" \
    "buffer_load_dwordx4 v112, s[40:43], s60 offen sc1 lds\n" \
    "s_add_u32 m0, m0, 4096\n" \
    "s_nop 11\n" \
    "buffer_load_dwordx4 v112, s[40:43], s61 offen sc1 lds\n" \
    "s_mov_b32 m0, s15\n" \
    "s_nop 12\n" \
    "buffer_store_dwordx4 v[28:31], v123, s[40:43], s23 offen sc1\n" \
    "buffer_store_dwordx4 v[32:35], v125, s[40:43], s24 offen sc1\n" \
    "buffer_store_dwordx4 v[56:59], v116, s[40:43], s98 offen sc1\n" \
    "s_waitcnt vmcnt(6)\n" \
    "Lqk_go:\n" \
    "s_barrier\n" \
    "ds_read_b128 v[64:67], v114 offset:0\n" \
    "ds_read_b128 v[68:71], v114 offset:1024\n" \
    "ds_read_b128 v[72:75], v114 offset:2048\n" \
    "ds_read_b128 v[76:79], v114 offset:3072\n" \
    "s_waitcnt lgkmcnt(3)\n" \
    "v_smfmac_f32_16x16x64_bf16 v[4:7], v[64:67], v[128:135], v122\n" \
    "v_smfmac_f32_16x16x64_bf16 v[12:15], v[64:67], a[8:15], v122\n" \
    "ds_read_b128 v[56:59], v115 offset:4096\n" \
    "v_smfmac_f32_16x16x64_bf16 v[8:11], v[64:67], a[0:7], v122\n" \
    "ds_read_b128 v[60:63], v115 offset:5120\n" \
    "v_smfmac_f32_16x16x64_bf16 v[0:3], v[64:67], v[40:47], v122\n" \
    "s_waitcnt lgkmcnt(4)\n" \
    "v_smfmac_f32_16x16x64_bf16 v[4:7], v[68:71], v[136:143], v122\n" \
    "v_smfmac_f32_16x16x64_bf16 v[12:15], v[68:71], a[24:31], v122\n" \
    "ds_read_b128 v[40:43], v115 offset:6144\n" \
    "v_smfmac_f32_16x16x64_bf16 v[8:11], v[68:71], a[16:23], v122\n" \
    "ds_read_b128 v[44:47], v115 offset:7168\n" \
    "v_smfmac_f32_16x16x64_bf16 v[0:3], v[68:71], v[48:55], v122\n" \
    "s_waitcnt lgkmcnt(5)\n" \
    "v_smfmac_f32_16x16x64_bf16 v[4:7], v[72:75], v[144:151], v122\n" \
    "s_waitcnt vmcnt(5)\n" \
    "s_barrier\n" \
    "v_smfmac_f32_16x16x64_bf16 v[12:15], v[72:75], a[40:47], v122\n" \
    "ds_read_b128 v[64:67], v114 offset:4096\n" \
    "ds_read_b128 v[48:51], v115 offset:8192\n" \
    "v_smfmac_f32_16x16x64_bf16 v[8:11], v[72:75], a[32:39], v122\n" \
    "ds_read_b128 v[52:55], v115 offset:9216\n" \
    "s_waitcnt lgkmcnt(5)\n" \
    "v_smfmac_f32_16x16x64_bf16 v[0:3], v[72:75], v[56:63], v122\n" \
    "v_smfmac_f32_16x16x64_bf16 v[4:7], v[76:79], v[152:159], v122\n" \
    "ds_read_b128 v[68:71], v114 offset:5120\n" \
    "v_smfmac_f32_16x16x64_bf16 v[12:15], v[76:79], a[56:63], v122\n" \
    "ds_read_b128 v[56:59], v115 offset:10240\n" \
    "v_smfmac_f32_16x16x64_bf16 v[8:11], v[76:79], a[48:55], v122\n" \
    "ds_read_b128 v[60:63], v115 offset:11264\n" \
    "s_waitcnt lgkmcnt(6)\n" \
    "v_smfmac_f32_16x16x64_bf16 v[0:3], v[76:79], v[40:47], v122\n" \
    "s_waitcnt lgkmcnt(5)\n" \
    "v_smfmac_f32_16x16x64_bf16 v[4:7], v[64:67], v[160:167], v122\n" \
    "s_waitcnt vmcnt(3)\n" \
    "s_barrier\n" \
    "v_smfmac_f32_16x16x64_bf16 v[12:15], v[64:67], a[72:79], v122\n" \
    "ds_read_b128 v[72:75], v114 offset:6144\n" \
    "ds_read_b128 v[40:43], v115 offset:12288\n" \
    "v_smfmac_f32_16x16x64_bf16 v[8:11], v[64:67], a[64:71], v122\n" \
    "ds_read_b128 v[44:47], v115 offset:13312\n" \
    "s_waitcnt lgkmcnt(6)\n" \
    "v_smfmac_f32_16x16x64_bf16 v[0:3], v[64:67], v[48:55], v122\n" \
    "ds_read2_b32 v[36:37], v124 offset1:32\n" \
    "s_waitcnt lgkmcnt(6)\n" \
    "v_smfmac_f32_16x16x64_bf16 v[4:7], v[68:71], v[168:175], v122\n" \
    "ds_read_b128 v[76:79], v114 offset:7168\n" \
    "v_smfmac_f32_16x16x64_bf16 v[12:15], v[68:71], a[88:95], v122\n" \
    "ds_read_b128 v[48:51], v115 offset:14336\n" \
    "v_smfmac_f32_16x16x64_bf16 v[8:11], v[68:71], a[80:87], v122\n" \
    "ds_read_b128 v[52:55], v115 offset:15360\n" \
    "s_waitcnt lgkmcnt(7)\n" \
    "v_smfmac_f32_16x16x64_bf16 v[0:3], v[68:71], v[56:63], v122\n" \
    "s_waitcnt lgkmcnt(3)\n" \
    "v_max_u32 v36, v36, v37\n" \
    "v_smfmac_f32_16x16x64_bf16 v[4:7], v[72:75], v[176:183], v122\n" \
    "ds_read_b128 v[64:67], v114 offset:8192\n" \
    "v_smfmac_f32_16x16x64_bf16 v[12:15], v[72:75], a[104:111], v122\n" \
    "ds_read_b128 v[56:59], v115 offset:16384\n" \
    "v_smfmac_f32_16x16x64_bf16 v[8:11], v[72:75], a[96:103], v122\n" \
    "ds_read_b128 v[60:63], v115 offset:17408\n" \
    "v_smfmac_f32_16x16x64_bf16 v[0:3], v[72:75], v[40:47], v122\n" \
    "s_waitcnt lgkmcnt(5)\n" \
    "v_cmp_eq_u32 s[30:31], -1, v36\n" \
    "v_smfmac_f32_16x16x64_bf16 v[4:7], v[76:79], v[184:191], v122\n" \
    "ds_read_b128 v[68:71], v114 offset:9216\n" \
    "v_smfmac_f32_16x16x64_bf16 v[12:15], v[76:79], a[120:127], v122\n" \
    "ds_read_b128 v[40:43], v115 offset:18432\n" \
    "v_smfmac_f32_16x16x64_bf16 v[8:11], v[76:79], a[112:119], v122\n" \
    "ds_read_b128 v[44:47], v115 offset:19456\n" \
    "s_waitcnt lgkmcnt(6)\n" \
    "v_smfmac_f32_16x16x64_bf16 v[0:3], v[76:79], v[48:55], v122\n" \
    "s_and_b64 s[30:31], s[30:31], s[18:19]\n" \
    "s_cbranch_scc1 Lqk_r0\n" \
    "s_waitcnt lgkmcnt(5)\n" \
    "v_smfmac_f32_16x16x64_bf16 v[4:7], v[64:67], v[192:199], v122\n" \
    "ds_read_b128 v[72:75], v114 offset:10240\n" \
    "v_smfmac_f32_16x16x64_bf16 v[12:15], v[64:67], a[136:143], v122\n" \
    "ds_read_b128 v[48:51], v115 offset:20480\n" \
    "v_smfmac_f32_16x16x64_bf16 v[8:11], v[64:67], a[128:135], v122\n" \
    "ds_read_b128 v[52:55], v115 offset:21504\n" \
    "s_waitcnt lgkmcnt(6)\n" \
    "v_smfmac_f32_16x16x64_bf16 v[0:3], v[64:67], v[56:63], v122\n" \
    "buffer_load_dwordx4 v117, s[40:43], s96 offen offset:0 sc1 lds\n" \
    "s_waitcnt lgkmcnt(5)\n" \
    "v_smfmac_f32_16x16x64_bf16 v[4:7], v[68:71], v[200:207], v122\n" \
    "ds_read_b128 v[76:79], v114 offset:11264\n" \
    "v_smfmac_f32_16x16x64_bf16 v[12:15], v[68:71], a[152:159], v122\n" \
    "ds_read_b128 v[56:59], v115 offset:22528\n" \
    "v_smfmac_f32_16x16x64_bf16 v[8:11], v[68:71], a[144:151], v122\n" \
    "ds_read_b128 v[60:63], v115 offset:23552\n" \
    "s_waitcnt lgkmcnt(6)\n" \
    "v_smfmac_f32_16x16x64_bf16 v[0:3], v[68:71], v[40:47], v122\n" \
    "buffer_load_dwordx4 v117, s[40:43], s96 offen offset:1024 sc1 lds\n" \
    "s_waitcnt lgkmcnt(5)\n" \
    "v_smfmac_f32_16x16x64_bf16 v[4:7], v[72:75], v[208:215], v122\n" \
    "ds_read_b128 v[64:67], v114 offset:12288\n" \
    "v_smfmac_f32_16x16x64_bf16 v[12:15], v[72:75], a[168:175], v122\n" \
    "ds_read_b128 v[40:43], v115 offset:24576\n" \
    "v_smfmac_f32_16x16x64_bf16 v[8:11], v[72:75], a[160:167], v122\n" \
    "ds_read_b128 v[44:47], v115 offset:25600\n" \
    "s_waitcnt lgkmcnt(6)\n" \
    "v_smfmac_f32_16x16x64_bf16 v[0:3], v[72:75], v[48:55], v122\n" \
    "s_waitcnt lgkmcnt(5)\n" \
    "v_smfmac_f32_16x16x64_bf16 v[4:7], v[76:79], v[216:223], v122\n" \
    "ds_read_b128 v[68:71], v114 offset:13312\n" \
    "v_smfmac_f32_16x16x64_bf16 v[12:15], v[76:79], a[184:191], v122\n" \
    "ds_read_b128 v[48:51], v115 offset:26624\n" \
    "v_smfmac_f32_16x16x64_bf16 v[8:11], v[76:79], a[176:183], v122\n" \
    "ds_read_b128 v[52:55], v115 offset:27648\n" \
    "s_waitcnt lgkmcnt(6)\n" \
    "v_smfmac_f32_16x16x64_bf16 v[0:3], v[76:79], v[56:63], v122\n" \
    "s_waitcnt lgkmcnt(5)\n" \
    "v_smfmac_f32_16x16x64_bf16 v[4:7], v[64:67], v[224:231], v122\n" \
    "ds_read_b128 v[72:75], v114 offset:14336\n" \
    "v_smfmac_f32_16x16x64_bf16 v[12:15], v[64:67], a[200:207], v122\n" \
    "ds_read_b128 v[56:59], v115 offset:28672\n" \
    "v_smfmac_f32_16x16x64_bf16 v[8:11], v[64:67], a[192:199], v122\n" \
    "ds_read_b128 v[60:63], v115 offset:29696\n" \
    "s_waitcnt lgkmcnt(6)\n" \
    "v_smfmac_f32_16x16x64_bf16 v[0:3], v[64:67], v[40:47], v122\n" \
    "s_waitcnt lgkmcnt(5)\n" \
    "v_smfmac_f32_16x16x64_bf16 v[4:7], v[68:71], v[232:239], v122\n" \
    "ds_read_b128 v[76:79], v114 offset:15360\n" \
    "v_smfmac_f32_16x16x64_bf16 v[12:15], v[68:71], a[216:223], v122\n" \
    "ds_read_b128 v[40:43], v115 offset:30720\n" \
    "v_smfmac_f32_16x16x64_bf16 v[8:11], v[68:71], a[208:215], v122\n" \
    "ds_read_b128 v[44:47], v115 offset:31744\n" \
    "s_waitcnt lgkmcnt(6)\n" \
    "v_smfmac_f32_16x16x64_bf16 v[0:3], v[68:71], v[48:55], v122\n" \
    "s_waitcnt lgkmcnt(5)\n" \
    "v_smfmac_f32_16x16x64_bf16 v[4:7], v[72:75], v[240:247], v122\n" \
    "v_smfmac_f32_16x16x64_bf16 v[12:15], v[72:75], a[232:239], v122\n" \
    "s_waitcnt lgkmcnt(2)\n" \
    "v_smfmac_f32_16x16x64_bf16 v[4:7], v[76:79], v[248:255], v122\n" \
    "v_smfmac_f32_16x16x64_bf16 v[12:15], v[76:79], a[248:255], v122\n" \
    "v_smfmac_f32_16x16x64_bf16 v[8:11], v[72:75], a[224:231], v122\n" \
    "v_smfmac_f32_16x16x64_bf16 v[0:3], v[72:75], v[56:63], v122\n" \
    "s_waitcnt lgkmcnt(0)\n" \
    "v_smfmac_f32_16x16x64_bf16 v[8:11], v[76:79], a[240:247], v122\n" \
    "v_permlane16_swap_b32 v4, v12\n" \
    "v_smfmac_f32_16x16x64_bf16 v[0:3], v[76:79], v[40:47], v122\n" \
    "v_permlane16_swap_b32 v5, v13\n" \
    "v_permlane16_swap_b32 v6, v14\n" \
    "v_permlane16_swap_b32 v7, v15\n" \
    "v_pk_add_f32 v[4:5], v[4:5], v[12:13]\n" \
    "v_pk_add_f32 v[6:7], v[6:7], v[14:15]\n" \
    "v_permlane16_swap_b32 v0, v8\n" \
    "v_permlane16_swap_b32 v1, v9\n" \
    "v_cvt_pk_bf16_f32 v4, v4, v5\n" \
    "v_cvt_pk_bf16_f32 v5, v6, v7\n" \
    "ds_write_b64 v120, v[4:5] offset:512\n" \
    "v_permlane16_swap_b32 v2, v10\n" \
    "v_permlane16_swap_b32 v3, v11\n" \
    "v_pk_add_f32 v[0:1], v[0:1], v[8:9]\n" \
    "v_pk_add_f32 v[2:3], v[2:3], v[10:11]\n" \
    "v_cvt_pk_bf16_f32 v0, v0, v1\n" \
    "v_cvt_pk_bf16_f32 v1, v2, v3\n" \
    "ds_write_b64 v120, v[0:1]\n" \
    "v_mov_b32 v6, 0xff800000\n" \
    "v_mov_b32 v76, 0x3f803f80\n" \
    "v_mov_b32 v77, 0x3f803f80\n" \
    "v_mov_b32 v78, 0x3f803f80\n" \
    "v_mov_b32 v79, 0x3f803f80\n" \
    "v_mov_b32 v8, s57\n" \
    "v_add_u32 v8, 1, v8\n" \
    "s_waitcnt lgkmcnt(0)\n" \
    "s_barrier\n" \
    "ds_read_b64_tr_b16 v[40:41], v121 offset:0\n" \
    "ds_read_b64_tr_b16 v[42:43], v121 offset:512\n" \
    "ds_read_b64_tr_b16 v[44:45], v121 offset:1024\n" \
    "ds_read_b64_tr_b16 v[46:47], v121 offset:1536\n" \
    "ds_read_b64_tr_b16 v[48:49], v121 offset:2048\n" \
    "ds_read_b64_tr_b16 v[50:51], v121 offset:2560\n" \
    "ds_read_b64_tr_b16 v[52:53], v121 offset:3072\n" \
    "ds_read_b64_tr_b16 v[54:55], v121 offset:3584\n" \
    "buffer_load_dwordx4 v[64:67], v119, s[40:43], s20 offen sc1\n" \
    "s_waitcnt lgkmcnt(6)\n" \
    "v_mfma_f32_16x16x32_bf16 v[60:63], v[40:43], v[40:43], 0\n" \
    "v_cmp_u_f32 s[30:31], v40, v40\n" \
    "v_mfma_f32_16x16x32_bf16 v[68:71], v[80:83], v[40:43], 0\n" \
    "v_mfma_f32_16x16x32_bf16 v[72:75], v[96:99], v[40:43], 0\n" \
    "s_waitcnt lgkmcnt(4)\n" \
    "v_mfma_f32_16x16x32_bf16 v[60:63], v[44:47], v[44:47], v[60:63]\n" \
    "s_and_b64 s[30:31], s[30:31], s[18:19]\n" \
    "s_cbranch_scc1 Lqk_r0\n" \
    "v_mfma_f32_16x16x32_bf16 v[68:71], v[84:87], v[44:47], v[68:71]\n" \
    "buffer_store_dword v8, v126, s[44:47], s21 offen sc1\n" \
    "v_mfma_f32_16x16x32_bf16 v[72:75], v[100:103], v[44:47], v[72:75]\n" \
    "s_cmp_eq_u32 s57, 63\n" \
    "s_cbranch_scc1 Lqk_end\n" \
    "s_waitcnt lgkmcnt(2)\n" \
    "v_mfma_f32_16x16x32_bf16 v[60:63], v[48:51], v[48:51], v[60:63]\n" \
    "ds_read_b64_tr_b16 v[56:57], v127 offset:0\n" \
    "v_mfma_f32_16x16x32_bf16 v[68:71], v[88:91], v[48:51], v[68:71]\n" \
    "ds_read_b64_tr_b16 v[58:59], v127 offset:512\n" \
    "v_mfma_f32_16x16x32_bf16 v[72:75], v[104:107], v[48:51], v[72:75]\n" \
    "s_waitcnt lgkmcnt(2)\n" \
    "v_mfma_f32_16x16x32_bf16 v[60:63], v[52:55], v[52:55], v[60:63]\n" \
    "v_mfma_f32_16x16x32_bf16 v[68:71], v[92:95], v[52:55], v[68:71]\n" \
    "v_mfma_f32_16x16x32_bf16 v[72:75], v[108:111], v[52:55], v[72:75]\n" \
    "v_mov_b32 v3, v3\n" \
    "v_cndmask_b32 v60, v60, v6, s[26:27]\n" \
    "v_cndmask_b32 v61, v61, v6, s[26:27]\n" \
    "v_cndmask_b32 v62, v62, v6, s[26:27]\n" \
    "v_cndmask_b32 v63, v63, v6, s[26:27]\n" \
    "v_max3_f32 v7, v60, v61, v62\n" \
    "v_max3_f32 v7, v7, v63, v68\n" \
    "v_max3_f32 v7, v7, v69, v70\n" \
    "v_max_f32 v7, v7, v71\n" \
    "v_max3_f32 v7, v7, v72, v73\n" \
    "v_max3_f32 v5, v7, v74, v75\n" \
    "v_max3_f32 v7, v7, v74, v75\n" \
    "s_nop 1\n" \
    "v_permlane32_swap_b32 v7, v5\n" \
    "v_max_f32 v2, v7, v5\n" \
    "v_max_f32 v7, v7, v5\n" \
    "s_nop 1\n" \
    "v_permlane16_swap_b32 v7, v2\n" \
    "v_max_f32 v7, v7, v2\n" \
    "v_mul_f32 v6, 0xbe0293ee, v7\n" \
    "v_pk_fma_f32 v[68:69], v[68:69], s[28:29], v[6:7] op_sel_hi:[1,0,0]\n" \
    "v_pk_fma_f32 v[70:71], v[70:71], s[28:29], v[6:7] op_sel_hi:[1,0,0]\n" \
    "v_pk_fma_f32 v[72:73], v[72:73], s[28:29], v[6:7] op_sel_hi:[1,0,0]\n" \
    "v_pk_fma_f32 v[74:75], v[74:75], s[28:29], v[6:7] op_sel_hi:[1,0,0]\n" \
    "v_exp_f32 v68, v68\n" \
    "v_exp_f32 v69, v69\n" \
    "v_exp_f32 v70, v70\n" \
    "v_exp_f32 v71, v71\n" \
    "v_exp_f32 v72, v72\n" \
    "v_cvt_pk_bf16_f32 v0, v68, v69\n" \
    "v_exp_f32 v73, v73\n" \
    "v_cvt_pk_bf16_f32 v1, v70, v71\n" \
    "v_exp_f32 v74, v74\n" \
    "v_exp_f32 v75, v75\n" \
    "v_cvt_pk_bf16_f32 v2, v72, v73\n" \
    "v_cvt_pk_bf16_f32 v3, v74, v75\n" \
    "v_pk_fma_f32 v[60:61], v[60:61], s[28:29], v[6:7] op_sel_hi:[1,0,0]\n" \
    "v_pk_fma_f32 v[62:63], v[62:63], s[28:29], v[6:7] op_sel_hi:[1,0,0]\n" \
    "v_mfma_f32_16x16x32_bf16 v[8:11], v[76:79], v[0:3], 0\n" \
    "v_exp_f32 v60, v60\n" \
    "v_mfma_f32_16x16x32_bf16 v[12:15], v[16:19], v[0:3], 0\n" \
    "v_exp_f32 v61, v61\n" \
    "v_mfma_f32_16x16x32_bf16 v[40:43], v[20:23], v[0:3], 0\n" \
    "v_exp_f32 v62, v62\n" \
    "v_exp_f32 v63, v63\n" \
    "v_cvt_pk_bf16_f32 v4, v60, v61\n" \
    "v_cvt_pk_bf16_f32 v5, v62, v63\n" \
    "s_waitcnt vmcnt(1)\n" \
    "v_max_u32 v49, v64, v65\n" \
    "v_max3_u32 v49, v49, v66, v67\n" \
    "v_mfma_f32_16x16x16_bf16 v[8:11], v[76:77], v[4:5], v[8:11]\n" \
    "v_cmp_eq_u32 s[30:31], -1, v49\n" \
    "Lqk_pv:\n" \
    "v_mfma_f32_16x16x16_bf16 v[12:15], v[64:65], v[4:5], v[12:15]\n" \
    "v_mfma_f32_16x16x16_bf16 v[40:43], v[66:67], v[4:5], v[40:43]\n" \
    "v_rcp_f32 v6, v8\n" \
    "s_cmp_lg_u64 s[30:31], 0\n" \
    "v_mov_b32 v48, v48\n" \
    "v_mov_b32 v48, v48\n" \
    "v_pk_mul_f32 v[12:13], v[12:13], v[6:7] op_sel_hi:[1,0]\n" \
    "v_pk_mul_f32 v[14:15], v[14:15], v[6:7] op_sel_hi:[1,0]\n" \
    "v_cvt_pk_bf16_f32 v44, v12, v13\n" \
    "v_cvt_pk_bf16_f32 v45, v14, v15\n" \
    "s_cbranch_scc1 Lqk_r2\n" \
    "v_pk_mul_f32 v[40:41], v[40:41], v[6:7] op_sel_hi:[1,0]\n" \
    "v_pk_mul_f32 v[42:43], v[42:43], v[6:7] op_sel_hi:[1,0]\n" \
    "v_cvt_pk_bf16_f32 v46, v40, v41\n" \
    "v_cvt_pk_bf16_f32 v47, v42, v43\n" \
    "buffer_store_dwordx4 v[44:47], v116, s[40:43], s99 offen sc1\n" \
    "s_xor_b32 s46, s46, s13\n" \
    "v_add_u32 v24, 1, v24\n" \
    "buffer_store_dword v24, v126, s[44:47], s21 offen sc1\n" \
    "s_mov_b32 s46, s13\n" \
    "s_waitcnt vmcnt(0)\n" \
    "s_barrier\n" \
    "s_nop 0\n" \
    "buffer_load_dwordx4 v[16:19], v118, s[40:43], s97 offen offset:0 sc1\n" \
    "buffer_load_dwordx4 v[20:23], v118, s[40:43], s97 offen offset:64 sc1\n" \
    "v_add_u32 v24, -1, v24\n" \
    "buffer_store_dword v24, v126, s[48:51], s55 offen sc1\n" \
    "s_waitcnt lgkmcnt(1)\n" \
    "v_mov_b64 v[28:29], v[56:57]\n" \
    "v_mov_b64 v[32:33], v[64:65]\n" \
    "s_waitcnt lgkmcnt(0)\n" \
    "v_mov_b64 v[30:31], v[58:59]\n" \
    "v_mov_b64 v[34:35], v[66:67]\n" \
    "s_add_u32 s57, s57, 1\n" \
    "s_branch Lqk_step\n" \
    "Lqk_r0:\n" \
    "s_waitcnt vmcnt(0) lgkmcnt(0)\n" \
    "s_barrier\n" \
    "Lqk_r1:\n" \
    "buffer_load_dwordx4 v[64:67], v112, s[40:43], s58 offen sc1\n" \
    "buffer_load_dwordx4 v[68:71], v112, s[40:43], s59 offen sc1\n" \
    "buffer_load_dwordx4 v[72:75], v112, s[40:43], s60 offen sc1\n" \
    "buffer_load_dwordx4 v[76:79], v112, s[40:43], s61 offen sc1\n" \
    "s_waitcnt vmcnt(3)\n" \
    "v_max_u32 v61, v64, v65\n" \
    "v_max3_u32 v61, v61, v66, v67\n" \
    "s_waitcnt vmcnt(2)\n" \
    "v_max3_u32 v61, v61, v68, v69\n" \
    "v_max3_u32 v61, v61, v70, v71\n" \
    "s_waitcnt vmcnt(1)\n" \
    "v_max3_u32 v61, v61, v72, v73\n" \
    "v_max3_u32 v61, v61, v74, v75\n" \
    "s_waitcnt vmcnt(0)\n" \
    "v_max3_u32 v61, v61, v76, v77\n" \
    "v_max3_u32 v61, v61, v78, v79\n" \
    "v_cmp_eq_u32 vcc, -1, v61\n" \
    "s_cbranch_vccz Lqk_r1_x\n" \
    "s_cmp_eq_u32 s90, 0\n" \
    "s_cbranch_scc1 Lqk_r1_x\n" \
    "s_sub_u32 s90, s90, 1\n" \
    "s_branch Lqk_r1\n" \
    "Lqk_r1_x:\n" \
    "ds_write_b128 v113, v[64:67] offset:0\n" \
    "ds_write_b128 v113, v[68:71] offset:4096\n" \
    "ds_write_b128 v113, v[72:75] offset:8192\n" \
    "ds_write_b128 v113, v[76:79] offset:12288\n" \
    "s_cmp_eq_u32 0, 0\n" \
    "s_cselect_b32 s62, 1, 0\n" \
    "s_add_u32 s53, s53, s62\n" \
    "s_min_u32 s53, s53, 40\n" \
    "s_mov_b32 s46, 0\n" \
    "v_mov_b64 v[0:1], 0\n" \
    "v_mov_b64 v[2:3], 0\n" \
    "v_mov_b64 v[4:5], 0\n" \
    "v_mov_b64 v[6:7], 0\n" \
    "v_mov_b64 v[8:9], 0\n" \
    "v_mov_b64 v[10:11], 0\n" \
    "v_mov_b64 v[12:13], 0\n" \
    "v_mov_b64 v[14:15], 0\n" \
    "ds_read_b128 v[40:43], v115 offset:0\n" \
    "ds_read_b128 v[44:47], v115 offset:1024\n" \
    "ds_read_b128 v[48:51], v115 offset:2048\n" \
    "ds_read_b128 v[52:55], v115 offset:3072\n" \
    "s_mov_b64 s[18:19], 0\n" \
    "s_waitcnt lgkmcnt(0)\n" \
    "s_branch Lqk_go\n" \
    "Lqk_r2:\n" \
    "s_cmp_eq_u32 s90, 0\n" \
    "s_cbranch_scc1 Lqk_r2_y\n" \
    "s_sub_u32 s90, s90, 1\n" \
    "s_waitcnt lgkmcnt(0)\n" \
    "buffer_load_dwordx4 v[64:67], v119, s[40:43], s20 offen sc1\n" \
    "s_waitcnt vmcnt(0)\n" \
    "v_max_u32 v49, v64, v65\n" \
    "v_max3_u32 v49, v49, v66, v67\n" \
    "v_cmp_eq_u32 vcc, -1, v49\n" \
    "s_cbranch_vccnz Lqk_r2\n" \
    "Lqk_r2_y:\n" \
    "s_nop 9\n" \
    "s_mov_b64 s[30:31], 0\n" \
    "v_mfma_f32_16x16x32_bf16 v[12:15], v[16:19], v[0:3], 0\n" \
    "v_mfma_f32_16x16x32_bf16 v[40:43], v[20:23], v[0:3], 0\n" \
    "s_nop 9\n" \
    "s_branch Lqk_pv\n" \
    "Lqk_end:\n" \
    "ds_read_b64_tr_b16 v[28:29], v127 offset:0\n" \
    "ds_read_b64_tr_b16 v[30:31], v127 offset:512\n" \
    "s_add_u32 s23, s23, 65536\n" \
    "s_add_u32 s24, s24, 65536\n" \
    "s_waitcnt vmcnt(2)\n" \
    "s_barrier\n" \
    "buffer_store_dword v24, v126, s[48:51], s55 offen sc1\n" \
    "s_waitcnt lgkmcnt(0)\n" \
    "buffer_store_dwordx4 v[28:31], v123, s[40:43], s23 offen sc1\n" \
    "s_waitcnt vmcnt(3)\n" \
    "s_branch Lqk_end_k\n" \
    "Lqk_end_l:\n" \
    "s_cmp_eq_u32 s90, 0\n" \
    "s_cbranch_scc1 Lqk_end_x\n" \
    "s_sub_u32 s90, s90, 1\n" \
    "buffer_load_dwordx4 v[64:67], v119, s[40:43], s20 offen sc1\n" \
    "s_waitcnt vmcnt(0)\n" \
    "Lqk_end_k:\n" \
    "v_max_u32 v49, v64, v65\n" \
    "v_max3_u32 v49, v49, v66, v67\n" \
    "v_cmp_eq_u32 vcc, -1, v49\n" \
    "s_cbranch_vccnz Lqk_end_l\n" \
    "Lqk_end_x:\n" \
    "buffer_store_dwordx4 v[64:67], v125, s[40:43], s24 offen sc1\n" \
    "s_waitcnt vmcnt(0) lgkmcnt(0)\n" \
    "s_barrier\n" \
    "v_mov_b32 v0, 64\n" \
    "buffer_store_dword v0, v126, s[48:51], s55 offen sc1\n" \
    "s_cmp_lg_u32 s90, 0\n" \
    "s_cbranch_scc1 7f\n" \
    "v_mov_b32 v0, 1\n" \
    "s_mov_b32 s62, 32768\n" \
    "buffer_store_dword v0, v126, s[40:43], s62 offen sc1\n" \
    "7:\n" \
    "s_endpgm\n"
#define VO_LOOP_ASM \
    "s_nop 4\n" \
    "s_movk_i32 s72, 1024\n" \
    "s_mul_i32 s73, s54, 40960\n" \
    "v_xor_b32 v0, 0, v124\n" \
    "v_xor_b32 v1, 32, v124\n" \
    "v_xor_b32 v2, 64, v124\n" \
    "v_xor_b32 v3, 96, v124\n" \
    "s_mov_b32 s62, 0\n" \
    "buffer_load_dword v4, v126, s[68:71], s62 offen\n" \
    "s_mov_b32 s62, 131072\n" \
    "buffer_load_dword v5, v126, s[68:71], s62 offen\n" \
    "s_mov_b32 s62, 262144\n" \
    "buffer_load_dword v6, v126, s[68:71], s62 offen\n" \
    "s_mov_b32 s62, 393216\n" \
    "buffer_load_dword v7, v126, s[68:71], s62 offen\n" \
    "s_mov_b32 s74, 57344\n" \
    "buffer_load_dword v108, off, s[40:43], s74 sc1\n" \
    "s_add_u32 m0, s73, 0\n" \
    "s_mov_b32 s62, 0\n" \
    "buffer_load_dwordx4 v122, s[64:67], s62 offen lds\n" \
    "s_add_u32 m0, m0, s72\n" \
    "s_mov_b32 s62, 131072\n" \
    "buffer_load_dwordx4 v122, s[64:67], s62 offen lds\n" \
    "s_add_u32 m0, m0, s72\n" \
    "s_mov_b32 s62, 8192\n" \
    "buffer_load_dwordx4 v122, s[64:67], s62 offen lds\n" \
    "s_add_u32 m0, m0, s72\n" \
    "s_mov_b32 s62, 139264\n" \
    "buffer_load_dwordx4 v122, s[64:67], s62 offen lds\n" \
    "s_add_u32 m0, m0, s72\n" \
    "s_mov_b32 s62, 262144\n" \
    "buffer_load_dwordx4 v122, s[64:67], s62 offen lds\n" \
    "s_add_u32 m0, m0, s72\n" \
    "s_mov_b32 s62, 393216\n" \
    "buffer_load_dwordx4 v122, s[64:67], s62 offen lds\n" \
    "s_add_u32 m0, m0, s72\n" \
    "s_mov_b32 s62, 270336\n" \
    "buffer_load_dwordx4 v122, s[64:67], s62 offen lds\n" \
    "s_add_u32 m0, m0, s72\n" \
    "s_mov_b32 s62, 401408\n" \
    "buffer_load_dwordx4 v122, s[64:67], s62 offen lds\n" \
    "s_add_u32 m0, s73, 8192\n" \
    "s_mov_b32 s62, 524288\n" \
    "buffer_load_dwordx4 v122, s[64:67], s62 offen lds\n" \
    "s_add_u32 m0, m0, s72\n" \
    "s_mov_b32 s62, 655360\n" \
    "buffer_load_dwordx4 v122, s[64:67], s62 offen lds\n" \
    "s_add_u32 m0, m0, s72\n" \
    "s_mov_b32 s62, 532480\n" \
    "buffer_load_dwordx4 v122, s[64:67], s62 offen lds\n" \
    "s_add_u32 m0, m0, s72\n" \
    "s_mov_b32 s62, 663552\n" \
    "buffer_load_dwordx4 v122, s[64:67], s62 offen lds\n" \
    "s_add_u32 m0, m0, s72\n" \
    "s_mov_b32 s62, 786432\n" \
    "buffer_load_dwordx4 v122, s[64:67], s62 offen lds\n" \
    "s_add_u32 m0, m0, s72\n" \
    "s_mov_b32 s62, 917504\n" \
    "buffer_load_dwordx4 v122, s[64:67], s62 offen lds\n" \
    "s_add_u32 m0, m0, s72\n" \
    "s_mov_b32 s62, 794624\n" \
    "buffer_load_dwordx4 v122, s[64:67], s62 offen lds\n" \
    "s_add_u32 m0, m0, s72\n" \
    "s_mov_b32 s62, 925696\n" \
    "buffer_load_dwordx4 v122, s[64:67], s62 offen lds\n" \
    "s_add_u32 m0, s73, 16384\n" \
    "s_mov_b32 s62, 1048576\n" \
    "buffer_load_dwordx4 v122, s[64:67], s62 offen lds\n" \
    "s_add_u32 m0, m0, s72\n" \
    "s_mov_b32 s62, 1179648\n" \
    "buffer_load_dwordx4 v122, s[64:67], s62 offen lds\n" \
    "s_add_u32 m0, m0, s72\n" \
    "s_mov_b32 s62, 1056768\n" \
    "buffer_load_dwordx4 v122, s[64:67], s62 offen lds\n" \
    "s_add_u32 m0, m0, s72\n" \
    "s_mov_b32 s62, 1187840\n" \
    "buffer_load_dwordx4 v122, s[64:67], s62 offen lds\n" \
    "s_add_u32 m0, m0, s72\n" \
    "s_mov_b32 s62, 1310720\n" \
    "buffer_load_dwordx4 v122, s[64:67], s62 offen lds\n" \
    "s_add_u32 m0, m0, s72\n" \
    "s_mov_b32 s62, 1441792\n" \
    "buffer_load_dwordx4 v122, s[64:67], s62 offen lds\n" \
    "s_add_u32 m0, m0, s72\n" \
    "s_mov_b32 s62, 1318912\n" \
    "buffer_load_dwordx4 v122, s[64:67], s62 offen lds\n" \
    "s_add_u32 m0, m0, s72\n" \
    "s_mov_b32 s62, 1449984\n" \
    "buffer_load_dwordx4 v122, s[64:67], s62 offen lds\n" \
    "s_add_u32 m0, s73, 24576\n" \
    "s_mov_b32 s62, 1572864\n" \
    "buffer_load_dwordx4 v122, s[64:67], s62 offen lds\n" \
    "s_add_u32 m0, m0, s72\n" \
    "s_mov_b32 s62, 1703936\n" \
    "buffer_load_dwordx4 v122, s[64:67], s62 offen lds\n" \
    "s_add_u32 m0, m0, s72\n" \
    "s_mov_b32 s62, 1581056\n" \
    "buffer_load_dwordx4 v122, s[64:67], s62 offen lds\n" \
    "s_add_u32 m0, m0, s72\n" \
    "s_mov_b32 s62, 1712128\n" \
    "buffer_load_dwordx4 v122, s[64:67], s62 offen lds\n" \
    "s_add_u32 m0, m0, s72\n" \
    "s_mov_b32 s62, 1835008\n" \
    "buffer_load_dwordx4 v122, s[64:67], s62 offen lds\n" \
    "s_add_u32 m0, m0, s72\n" \
    "s_mov_b32 s62, 1966080\n" \
    "buffer_load_dwordx4 v122, s[64:67], s62 offen lds\n" \
    "s_add_u32 m0, m0, s72\n" \
    "s_mov_b32 s62, 1843200\n" \
    "buffer_load_dwordx4 v122, s[64:67], s62 offen lds\n" \
    "s_add_u32 m0, m0, s72\n" \
    "s_mov_b32 s62, 1974272\n" \
    "buffer_load_dwordx4 v122, s[64:67], s62 offen lds\n" \
    "s_add_u32 m0, s73, 32768\n" \
    "s_mov_b32 s62, 0\n" \
    "buffer_load_dwordx4 v123, s[68:71], s62 offen lds\n" \
    "s_add_u32 m0, m0, s72\n" \
    "s_mov_b32 s62, 4096\n" \
    "buffer_load_dwordx4 v123, s[68:71], s62 offen lds\n" \
    "s_add_u32 m0, m0, s72\n" \
    "s_mov_b32 s62, 131072\n" \
    "buffer_load_dwordx4 v123, s[68:71], s62 offen lds\n" \
    "s_add_u32 m0, m0, s72\n" \
    "s_mov_b32 s62, 135168\n" \
    "buffer_load_dwordx4 v123, s[68:71], s62 offen lds\n" \
    "s_add_u32 m0, m0, s72\n" \
    "s_mov_b32 s62, 16384\n" \
    "buffer_load_dwordx4 v123, s[68:71], s62 offen lds\n" \
    "s_add_u32 m0, m0, s72\n" \
    "s_mov_b32 s62, 20480\n" \
    "buffer_load_dwordx4 v123, s[68:71], s62 offen lds\n" \
    "s_add_u32 m0, m0, s72\n" \
    "s_mov_b32 s62, 147456\n" \
    "buffer_load_dwordx4 v123, s[68:71], s62 offen lds\n" \
    "s_add_u32 m0, m0, s72\n" \
    "s_mov_b32 s62, 151552\n" \
    "buffer_load_dwordx4 v123, s[68:71], s62 offen lds\n" \
    "s_waitcnt vmcnt(32)\n" \
    "s_barrier\n" \
    "s_cmp_lg_u32 s54, 0\n" \
    "s_cbranch_scc1 Lvo_i_y\n" \
    "Lvo_i:\n" \
    "s_cmp_eq_u32 s90, 0\n" \
    "s_cbranch_scc1 Lvo_i_x\n" \
    "s_sub_u32 s90, s90, 1\n" \
    "buffer_load_dword v109, off, s[40:43], s74 sc1\n" \
    "s_waitcnt vmcnt(0)\n" \
    "v_cmp_eq_u32 vcc, v108, v109\n" \
    "s_cbranch_vccnz Lvo_i\n" \
    "Lvo_i_x:\n" \
    "v_mov_b32 v109, 0x10001\n" \
    "s_mov_b32 s62, 32896\n" \
    "buffer_atomic_add v109, v117, s[40:43], s62 offen\n" \
    "Lvo_i_y:\n" \
    "ds_read_b64_tr_b16 v[128:129], v0 offset:0\n" \
    "ds_read_b64_tr_b16 v[130:131], v0 offset:2048\n" \
    "ds_read_b64_tr_b16 v[132:133], v0 offset:4096\n" \
    "ds_read_b64_tr_b16 v[134:135], v0 offset:6144\n" \
    "ds_read_b64_tr_b16 v[136:137], v1 offset:0\n" \
    "ds_read_b64_tr_b16 v[138:139], v1 offset:2048\n" \
    "ds_read_b64_tr_b16 v[140:141], v1 offset:4096\n" \
    "ds_read_b64_tr_b16 v[142:143], v1 offset:6144\n" \
    "s_waitcnt lgkmcnt(4)\n" \
    "ds_read_b64_tr_b16 v[144:145], v2 offset:0\n" \
    "ds_read_b64_tr_b16 v[146:147], v2 offset:2048\n" \
    "ds_read_b64_tr_b16 v[148:149], v2 offset:4096\n" \
    "ds_read_b64_tr_b16 v[150:151], v2 offset:6144\n" \
    "ds_read_b64_tr_b16 v[152:153], v3 offset:0\n" \
    "ds_read_b64_tr_b16 v[154:155], v3 offset:2048\n" \
    "ds_read_b64_tr_b16 v[156:157], v3 offset:4096\n" \
    "ds_read_b64_tr_b16 v[158:159], v3 offset:6144\n" \
    "s_waitcnt lgkmcnt(0)\n" \
    "s_add_u32 m0, s73, 0\n" \
    "s_mov_b32 s62, 262144\n" \
    "buffer_load_dwordx4 v123, s[68:71], s62 offen lds\n" \
    "s_add_u32 m0, m0, s72\n" \
    "s_mov_b32 s62, 266240\n" \
    "buffer_load_dwordx4 v123, s[68:71], s62 offen lds\n" \
    "s_add_u32 m0, m0, s72\n" \
    "s_mov_b32 s62, 393216\n" \
    "buffer_load_dwordx4 v123, s[68:71], s62 offen lds\n" \
    "s_add_u32 m0, m0, s72\n" \
    "s_mov_b32 s62, 397312\n" \
    "buffer_load_dwordx4 v123, s[68:71], s62 offen lds\n" \
    "s_add_u32 m0, m0, s72\n" \
    "s_mov_b32 s62, 278528\n" \
    "buffer_load_dwordx4 v123, s[68:71], s62 offen lds\n" \
    "s_add_u32 m0, m0, s72\n" \
    "s_mov_b32 s62, 282624\n" \
    "buffer_load_dwordx4 v123, s[68:71], s62 offen lds\n" \
    "s_add_u32 m0, m0, s72\n" \
    "s_mov_b32 s62, 409600\n" \
    "buffer_load_dwordx4 v123, s[68:71], s62 offen lds\n" \
    "s_add_u32 m0, m0, s72\n" \
    "s_mov_b32 s62, 413696\n" \
    "buffer_load_dwordx4 v123, s[68:71], s62 offen lds\n" \
    "s_waitcnt vmcnt(32)\n" \
    "ds_read_b64_tr_b16 v[160:161], v0 offset:8192\n" \
    "ds_read_b64_tr_b16 v[162:163], v0 offset:10240\n" \
    "ds_read_b64_tr_b16 v[164:165], v0 offset:12288\n" \
    "ds_read_b64_tr_b16 v[166:167], v0 offset:14336\n" \
    "ds_read_b64_tr_b16 v[168:169], v1 offset:8192\n" \
    "ds_read_b64_tr_b16 v[170:171], v1 offset:10240\n" \
    "ds_read_b64_tr_b16 v[172:173], v1 offset:12288\n" \
    "ds_read_b64_tr_b16 v[174:175], v1 offset:14336\n" \
    "s_waitcnt lgkmcnt(4)\n" \
    "ds_read_b64_tr_b16 v[176:177], v2 offset:8192\n" \
    "ds_read_b64_tr_b16 v[178:179], v2 offset:10240\n" \
    "ds_read_b64_tr_b16 v[180:181], v2 offset:12288\n" \
    "ds_read_b64_tr_b16 v[182:183], v2 offset:14336\n" \
    "ds_read_b64_tr_b16 v[184:185], v3 offset:8192\n" \
    "ds_read_b64_tr_b16 v[186:187], v3 offset:10240\n" \
    "ds_read_b64_tr_b16 v[188:189], v3 offset:12288\n" \
    "ds_read_b64_tr_b16 v[190:191], v3 offset:14336\n" \
    "s_waitcnt lgkmcnt(0)\n" \
    "s_add_u32 m0, s73, 8192\n" \
    "s_mov_b32 s62, 524288\n" \
    "buffer_load_dwordx4 v123, s[68:71], s62 offen lds\n" \
    "s_add_u32 m0, m0, s72\n" \
    "s_mov_b32 s62, 528384\n" \
    "buffer_load_dwordx4 v123, s[68:71], s62 offen lds\n" \
    "s_add_u32 m0, m0, s72\n" \
    "s_mov_b32 s62, 655360\n" \
    "buffer_load_dwordx4 v123, s[68:71], s62 offen lds\n" \
    "s_add_u32 m0, m0, s72\n" \
    "s_mov_b32 s62, 659456\n" \
    "buffer_load_dwordx4 v123, s[68:71], s62 offen lds\n" \
    "s_add_u32 m0, m0, s72\n" \
    "s_mov_b32 s62, 540672\n" \
    "buffer_load_dwordx4 v123, s[68:71], s62 offen lds\n" \
    "s_add_u32 m0, m0, s72\n" \
    "s_mov_b32 s62, 544768\n" \
    "buffer_load_dwordx4 v123, s[68:71], s62 offen lds\n" \
    "s_add_u32 m0, m0, s72\n" \
    "s_mov_b32 s62, 671744\n" \
    "buffer_load_dwordx4 v123, s[68:71], s62 offen lds\n" \
    "s_add_u32 m0, m0, s72\n" \
    "s_mov_b32 s62, 675840\n" \
    "buffer_load_dwordx4 v123, s[68:71], s62 offen lds\n" \
    "s_waitcnt vmcnt(32)\n" \
    "ds_read_b64_tr_b16 v[192:193], v0 offset:16384\n" \
    "ds_read_b64_tr_b16 v[194:195], v0 offset:18432\n" \
    "ds_read_b64_tr_b16 v[196:197], v0 offset:20480\n" \
    "ds_read_b64_tr_b16 v[198:199], v0 offset:22528\n" \
    "ds_read_b64_tr_b16 v[200:201], v1 offset:16384\n" \
    "ds_read_b64_tr_b16 v[202:203], v1 offset:18432\n" \
    "ds_read_b64_tr_b16 v[204:205], v1 offset:20480\n" \
    "ds_read_b64_tr_b16 v[206:207], v1 offset:22528\n" \
    "s_waitcnt lgkmcnt(4)\n" \
    "ds_read_b64_tr_b16 v[208:209], v2 offset:16384\n" \
    "ds_read_b64_tr_b16 v[210:211], v2 offset:18432\n" \
    "ds_read_b64_tr_b16 v[212:213], v2 offset:20480\n" \
    "ds_read_b64_tr_b16 v[214:215], v2 offset:22528\n" \
    "ds_read_b64_tr_b16 v[216:217], v3 offset:16384\n" \
    "ds_read_b64_tr_b16 v[218:219], v3 offset:18432\n" \
    "ds_read_b64_tr_b16 v[220:221], v3 offset:20480\n" \
    "ds_read_b64_tr_b16 v[222:223], v3 offset:22528\n" \
    "s_waitcnt lgkmcnt(0)\n" \
    "s_add_u32 m0, s73, 16384\n" \
    "s_mov_b32 s62, 786432\n" \
    "buffer_load_dwordx4 v123, s[68:71], s62 offen lds\n" \
    "s_add_u32 m0, m0, s72\n" \
    "s_mov_b32 s62, 790528\n" \
    "buffer_load_dwordx4 v123, s[68:71], s62 offen lds\n" \
    "s_add_u32 m0, m0, s72\n" \
    "s_mov_b32 s62, 917504\n" \
    "buffer_load_dwordx4 v123, s[68:71], s62 offen lds\n" \
    "s_add_u32 m0, m0, s72\n" \
    "s_mov_b32 s62, 921600\n" \
    "buffer_load_dwordx4 v123, s[68:71], s62 offen lds\n" \
    "s_add_u32 m0, m0, s72\n" \
    "s_mov_b32 s62, 802816\n" \
    "buffer_load_dwordx4 v123, s[68:71], s62 offen lds\n" \
    "s_add_u32 m0, m0, s72\n" \
    "s_mov_b32 s62, 806912\n" \
    "buffer_load_dwordx4 v123, s[68:71], s62 offen lds\n" \
    "s_add_u32 m0, m0, s72\n" \
    "s_mov_b32 s62, 933888\n" \
    "buffer_load_dwordx4 v123, s[68:71], s62 offen lds\n" \
    "s_add_u32 m0, m0, s72\n" \
    "s_mov_b32 s62, 937984\n" \
    "buffer_load_dwordx4 v123, s[68:71], s62 offen lds\n" \
    "s_waitcnt vmcnt(32)\n" \
    "ds_read_b64_tr_b16 v[224:225], v0 offset:24576\n" \
    "ds_read_b64_tr_b16 v[226:227], v0 offset:26624\n" \
    "ds_read_b64_tr_b16 v[228:229], v0 offset:28672\n" \
    "ds_read_b64_tr_b16 v[230:231], v0 offset:30720\n" \
    "ds_read_b64_tr_b16 v[232:233], v1 offset:24576\n" \
    "ds_read_b64_tr_b16 v[234:235], v1 offset:26624\n" \
    "ds_read_b64_tr_b16 v[236:237], v1 offset:28672\n" \
    "ds_read_b64_tr_b16 v[238:239], v1 offset:30720\n" \
    "s_waitcnt lgkmcnt(4)\n" \
    "ds_read_b64_tr_b16 v[240:241], v2 offset:24576\n" \
    "ds_read_b64_tr_b16 v[242:243], v2 offset:26624\n" \
    "ds_read_b64_tr_b16 v[244:245], v2 offset:28672\n" \
    "ds_read_b64_tr_b16 v[246:247], v2 offset:30720\n" \
    "ds_read_b64_tr_b16 v[248:249], v3 offset:24576\n" \
    "ds_read_b64_tr_b16 v[250:251], v3 offset:26624\n" \
    "ds_read_b64_tr_b16 v[252:253], v3 offset:28672\n" \
    "ds_read_b64_tr_b16 v[254:255], v3 offset:30720\n" \
    "s_waitcnt lgkmcnt(0)\n" \
    "s_add_u32 m0, s73, 24576\n" \
    "s_mov_b32 s62, 1048576\n" \
    "buffer_load_dwordx4 v123, s[68:71], s62 offen lds\n" \
    "s_add_u32 m0, m0, s72\n" \
    "s_mov_b32 s62, 1052672\n" \
    "buffer_load_dwordx4 v123, s[68:71], s62 offen lds\n" \
    "s_add_u32 m0, m0, s72\n" \
    "s_mov_b32 s62, 1179648\n" \
    "buffer_load_dwordx4 v123, s[68:71], s62 offen lds\n" \
    "s_add_u32 m0, m0, s72\n" \
    "s_mov_b32 s62, 1183744\n" \
    "buffer_load_dwordx4 v123, s[68:71], s62 offen lds\n" \
    "s_add_u32 m0, m0, s72\n" \
    "s_mov_b32 s62, 1064960\n" \
    "buffer_load_dwordx4 v123, s[68:71], s62 offen lds\n" \
    "s_add_u32 m0, m0, s72\n" \
    "s_mov_b32 s62, 1069056\n" \
    "buffer_load_dwordx4 v123, s[68:71], s62 offen lds\n" \
    "s_add_u32 m0, m0, s72\n" \
    "s_mov_b32 s62, 1196032\n" \
    "buffer_load_dwordx4 v123, s[68:71], s62 offen lds\n" \
    "s_add_u32 m0, m0, s72\n" \
    "s_mov_b32 s62, 1200128\n" \
    "buffer_load_dwordx4 v123, s[68:71], s62 offen lds\n" \
    "s_waitcnt vmcnt(32)\n" \
    "ds_read_b64_tr_b16 a[0:1], v125 offset:32768\n" \
    "ds_read_b64_tr_b16 a[2:3], v125 offset:33792\n" \
    "ds_read_b64_tr_b16 a[4:5], v125 offset:34816\n" \
    "ds_read_b64_tr_b16 a[6:7], v125 offset:35840\n" \
    "ds_read_b64_tr_b16 a[8:9], v125 offset:32776\n" \
    "ds_read_b64_tr_b16 a[10:11], v125 offset:33800\n" \
    "ds_read_b64_tr_b16 a[12:13], v125 offset:34824\n" \
    "ds_read_b64_tr_b16 a[14:15], v125 offset:35848\n" \
    "s_waitcnt lgkmcnt(4)\n" \
    "ds_read_b64_tr_b16 a[16:17], v125 offset:36864\n" \
    "ds_read_b64_tr_b16 a[18:19], v125 offset:37888\n" \
    "ds_read_b64_tr_b16 a[20:21], v125 offset:38912\n" \
    "ds_read_b64_tr_b16 a[22:23], v125 offset:39936\n" \
    "ds_read_b64_tr_b16 a[24:25], v125 offset:36872\n" \
    "ds_read_b64_tr_b16 a[26:27], v125 offset:37896\n" \
    "ds_read_b64_tr_b16 a[28:29], v125 offset:38920\n" \
    "ds_read_b64_tr_b16 a[30:31], v125 offset:39944\n" \
    "s_waitcnt lgkmcnt(0)\n" \
    "s_add_u32 m0, s73, 32768\n" \
    "s_mov_b32 s62, 1310720\n" \
    "buffer_load_dwordx4 v123, s[68:71], s62 offen lds\n" \
    "s_add_u32 m0, m0, s72\n" \
    "s_mov_b32 s62, 1314816\n" \
    "buffer_load_dwordx4 v123, s[68:71], s62 offen lds\n" \
    "s_add_u32 m0, m0, s72\n" \
    "s_mov_b32 s62, 1441792\n" \
    "buffer_load_dwordx4 v123, s[68:71], s62 offen lds\n" \
    "s_add_u32 m0, m0, s72\n" \
    "s_mov_b32 s62, 1445888\n" \
    "buffer_load_dwordx4 v123, s[68:71], s62 offen lds\n" \
    "s_add_u32 m0, m0, s72\n" \
    "s_mov_b32 s62, 1327104\n" \
    "buffer_load_dwordx4 v123, s[68:71], s62 offen lds\n" \
    "s_add_u32 m0, m0, s72\n" \
    "s_mov_b32 s62, 1331200\n" \
    "buffer_load_dwordx4 v123, s[68:71], s62 offen lds\n" \
    "s_add_u32 m0, m0, s72\n" \
    "s_mov_b32 s62, 1458176\n" \
    "buffer_load_dwordx4 v123, s[68:71], s62 offen lds\n" \
    "s_add_u32 m0, m0, s72\n" \
    "s_mov_b32 s62, 1462272\n" \
    "buffer_load_dwordx4 v123, s[68:71], s62 offen lds\n" \
    "s_waitcnt vmcnt(32)\n" \
    "ds_read_b64_tr_b16 a[32:33], v125 offset:0\n" \
    "ds_read_b64_tr_b16 a[34:35], v125 offset:1024\n" \
    "ds_read_b64_tr_b16 a[36:37], v125 offset:2048\n" \
    "ds_read_b64_tr_b16 a[38:39], v125 offset:3072\n" \
    "ds_read_b64_tr_b16 a[40:41], v125 offset:8\n" \
    "ds_read_b64_tr_b16 a[42:43], v125 offset:1032\n" \
    "ds_read_b64_tr_b16 a[44:45], v125 offset:2056\n" \
    "ds_read_b64_tr_b16 a[46:47], v125 offset:3080\n" \
    "s_waitcnt lgkmcnt(4)\n" \
    "ds_read_b64_tr_b16 a[48:49], v125 offset:4096\n" \
    "ds_read_b64_tr_b16 a[50:51], v125 offset:5120\n" \
    "ds_read_b64_tr_b16 a[52:53], v125 offset:6144\n" \
    "ds_read_b64_tr_b16 a[54:55], v125 offset:7168\n" \
    "ds_read_b64_tr_b16 a[56:57], v125 offset:4104\n" \
    "ds_read_b64_tr_b16 a[58:59], v125 offset:5128\n" \
    "ds_read_b64_tr_b16 a[60:61], v125 offset:6152\n" \
    "ds_read_b64_tr_b16 a[62:63], v125 offset:7176\n" \
    "s_waitcnt lgkmcnt(0)\n" \
    "s_add_u32 m0, s73, 0\n" \
    "s_mov_b32 s62, 1572864\n" \
    "buffer_load_dwordx4 v123, s[68:71], s62 offen lds\n" \
    "s_add_u32 m0, m0, s72\n" \
    "s_mov_b32 s62, 1576960\n" \
    "buffer_load_dwordx4 v123, s[68:71], s62 offen lds\n" \
    "s_add_u32 m0, m0, s72\n" \
    "s_mov_b32 s62, 1703936\n" \
    "buffer_load_dwordx4 v123, s[68:71], s62 offen lds\n" \
    "s_add_u32 m0, m0, s72\n" \
    "s_mov_b32 s62, 1708032\n" \
    "buffer_load_dwordx4 v123, s[68:71], s62 offen lds\n" \
    "s_add_u32 m0, m0, s72\n" \
    "s_mov_b32 s62, 1589248\n" \
    "buffer_load_dwordx4 v123, s[68:71], s62 offen lds\n" \
    "s_add_u32 m0, m0, s72\n" \
    "s_mov_b32 s62, 1593344\n" \
    "buffer_load_dwordx4 v123, s[68:71], s62 offen lds\n" \
    "s_add_u32 m0, m0, s72\n" \
    "s_mov_b32 s62, 1720320\n" \
    "buffer_load_dwordx4 v123, s[68:71], s62 offen lds\n" \
    "s_add_u32 m0, m0, s72\n" \
    "s_mov_b32 s62, 1724416\n" \
    "buffer_load_dwordx4 v123, s[68:71], s62 offen lds\n" \
    "s_waitcnt vmcnt(32)\n" \
    "ds_read_b64_tr_b16 a[64:65], v125 offset:8192\n" \
    "ds_read_b64_tr_b16 a[66:67], v125 offset:9216\n" \
    "ds_read_b64_tr_b16 a[68:69], v125 offset:10240\n" \
    "ds_read_b64_tr_b16 a[70:71], v125 offset:11264\n" \
    "ds_read_b64_tr_b16 a[72:73], v125 offset:8200\n" \
    "ds_read_b64_tr_b16 a[74:75], v125 offset:9224\n" \
    "ds_read_b64_tr_b16 a[76:77], v125 offset:10248\n" \
    "ds_read_b64_tr_b16 a[78:79], v125 offset:11272\n" \
    "s_waitcnt lgkmcnt(4)\n" \
    "ds_read_b64_tr_b16 a[80:81], v125 offset:12288\n" \
    "ds_read_b64_tr_b16 a[82:83], v125 offset:13312\n" \
    "ds_read_b64_tr_b16 a[84:85], v125 offset:14336\n" \
    "ds_read_b64_tr_b16 a[86:87], v125 offset:15360\n" \
    "ds_read_b64_tr_b16 a[88:89], v125 offset:12296\n" \
    "ds_read_b64_tr_b16 a[90:91], v125 offset:13320\n" \
    "ds_read_b64_tr_b16 a[92:93], v125 offset:14344\n" \
    "ds_read_b64_tr_b16 a[94:95], v125 offset:15368\n" \
    "s_waitcnt lgkmcnt(0)\n" \
    "s_add_u32 m0, s73, 8192\n" \
    "s_mov_b32 s62, 1835008\n" \
    "buffer_load_dwordx4 v123, s[68:71], s62 offen lds\n" \
    "s_add_u32 m0, m0, s72\n" \
    "s_mov_b32 s62, 1839104\n" \
    "buffer_load_dwordx4 v123, s[68:71], s62 offen lds\n" \
    "s_add_u32 m0, m0, s72\n" \
    "s_mov_b32 s62, 1966080\n" \
    "buffer_load_dwordx4 v123, s[68:71], s62 offen lds\n" \
    "s_add_u32 m0, m0, s72\n" \
    "s_mov_b32 s62, 1970176\n" \
    "buffer_load_dwordx4 v123, s[68:71], s62 offen lds\n" \
    "s_add_u32 m0, m0, s72\n" \
    "s_mov_b32 s62, 1851392\n" \
    "buffer_load_dwordx4 v123, s[68:71], s62 offen lds\n" \
    "s_add_u32 m0, m0, s72\n" \
    "s_mov_b32 s62, 1855488\n" \
    "buffer_load_dwordx4 v123, s[68:71], s62 offen lds\n" \
    "s_add_u32 m0, m0, s72\n" \
    "s_mov_b32 s62, 1982464\n" \
    "buffer_load_dwordx4 v123, s[68:71], s62 offen lds\n" \
    "s_add_u32 m0, m0, s72\n" \
    "s_mov_b32 s62, 1986560\n" \
    "buffer_load_dwordx4 v123, s[68:71], s62 offen lds\n" \
    "s_waitcnt vmcnt(32)\n" \
    "ds_read_b64_tr_b16 a[96:97], v125 offset:16384\n" \
    "ds_read_b64_tr_b16 a[98:99], v125 offset:17408\n" \
    "ds_read_b64_tr_b16 a[100:101], v125 offset:18432\n" \
    "ds_read_b64_tr_b16 a[102:103], v125 offset:19456\n" \
    "ds_read_b64_tr_b16 a[104:105], v125 offset:16392\n" \
    "ds_read_b64_tr_b16 a[106:107], v125 offset:17416\n" \
    "ds_read_b64_tr_b16 a[108:109], v125 offset:18440\n" \
    "ds_read_b64_tr_b16 a[110:111], v125 offset:19464\n" \
    "s_waitcnt lgkmcnt(4)\n" \
    "ds_read_b64_tr_b16 a[112:113], v125 offset:20480\n" \
    "ds_read_b64_tr_b16 a[114:115], v125 offset:21504\n" \
    "ds_read_b64_tr_b16 a[116:117], v125 offset:22528\n" \
    "ds_read_b64_tr_b16 a[118:119], v125 offset:23552\n" \
    "ds_read_b64_tr_b16 a[120:121], v125 offset:20488\n" \
    "ds_read_b64_tr_b16 a[122:123], v125 offset:21512\n" \
    "ds_read_b64_tr_b16 a[124:125], v125 offset:22536\n" \
    "ds_read_b64_tr_b16 a[126:127], v125 offset:23560\n" \
    "s_waitcnt vmcnt(24)\n" \
    "ds_read_b64_tr_b16 a[128:129], v125 offset:24576\n" \
    "ds_read_b64_tr_b16 a[130:131], v125 offset:25600\n" \
    "ds_read_b64_tr_b16 a[132:133], v125 offset:26624\n" \
    "ds_read_b64_tr_b16 a[134:135], v125 offset:27648\n" \
    "ds_read_b64_tr_b16 a[136:137], v125 offset:24584\n" \
    "ds_read_b64_tr_b16 a[138:139], v125 offset:25608\n" \
    "ds_read_b64_tr_b16 a[140:141], v125 offset:26632\n" \
    "ds_read_b64_tr_b16 a[142:143], v125 offset:27656\n" \
    "s_waitcnt lgkmcnt(4)\n" \
    "ds_read_b64_tr_b16 a[144:145], v125 offset:28672\n" \
    "ds_read_b64_tr_b16 a[146:147], v125 offset:29696\n" \
    "ds_read_b64_tr_b16 a[148:149], v125 offset:30720\n" \
    "ds_read_b64_tr_b16 a[150:151], v125 offset:31744\n" \
    "ds_read_b64_tr_b16 a[152:153], v125 offset:28680\n" \
    "ds_read_b64_tr_b16 a[154:155], v125 offset:29704\n" \
    "ds_read_b64_tr_b16 a[156:157], v125 offset:30728\n" \
    "ds_read_b64_tr_b16 a[158:159], v125 offset:31752\n" \
    "s_waitcnt vmcnt(16)\n" \
    "ds_read_b64_tr_b16 a[160:161], v125 offset:32768\n" \
    "ds_read_b64_tr_b16 a[162:163], v125 offset:33792\n" \
    "ds_read_b64_tr_b16 a[164:165], v125 offset:34816\n" \
    "ds_read_b64_tr_b16 a[166:167], v125 offset:35840\n" \
    "ds_read_b64_tr_b16 a[168:169], v125 offset:32776\n" \
    "ds_read_b64_tr_b16 a[170:171], v125 offset:33800\n" \
    "ds_read_b64_tr_b16 a[172:173], v125 offset:34824\n" \
    "ds_read_b64_tr_b16 a[174:175], v125 offset:35848\n" \
    "s_waitcnt lgkmcnt(4)\n" \
    "ds_read_b64_tr_b16 a[176:177], v125 offset:36864\n" \
    "ds_read_b64_tr_b16 a[178:179], v125 offset:37888\n" \
    "ds_read_b64_tr_b16 a[180:181], v125 offset:38912\n" \
    "ds_read_b64_tr_b16 a[182:183], v125 offset:39936\n" \
    "ds_read_b64_tr_b16 a[184:185], v125 offset:36872\n" \
    "ds_read_b64_tr_b16 a[186:187], v125 offset:37896\n" \
    "ds_read_b64_tr_b16 a[188:189], v125 offset:38920\n" \
    "ds_read_b64_tr_b16 a[190:191], v125 offset:39944\n" \
    "s_waitcnt vmcnt(8)\n" \
    "ds_read_b64_tr_b16 a[192:193], v125 offset:0\n" \
    "ds_read_b64_tr_b16 a[194:195], v125 offset:1024\n" \
    "ds_read_b64_tr_b16 a[196:197], v125 offset:2048\n" \
    "ds_read_b64_tr_b16 a[198:199], v125 offset:3072\n" \
    "ds_read_b64_tr_b16 a[200:201], v125 offset:8\n" \
    "ds_read_b64_tr_b16 a[202:203], v125 offset:1032\n" \
    "ds_read_b64_tr_b16 a[204:205], v125 offset:2056\n" \
    "ds_read_b64_tr_b16 a[206:207], v125 offset:3080\n" \
    "s_waitcnt lgkmcnt(4)\n" \
    "ds_read_b64_tr_b16 a[208:209], v125 offset:4096\n" \
    "ds_read_b64_tr_b16 a[210:211], v125 offset:5120\n" \
    "ds_read_b64_tr_b16 a[212:213], v125 offset:6144\n" \
    "ds_read_b64_tr_b16 a[214:215], v125 offset:7168\n" \
    "ds_read_b64_tr_b16 a[216:217], v125 offset:4104\n" \
    "ds_read_b64_tr_b16 a[218:219], v125 offset:5128\n" \
    "ds_read_b64_tr_b16 a[220:221], v125 offset:6152\n" \
    "ds_read_b64_tr_b16 a[222:223], v125 offset:7176\n" \
    "s_waitcnt vmcnt(0)\n" \
    "ds_read_b64_tr_b16 a[224:225], v125 offset:8192\n" \
    "ds_read_b64_tr_b16 a[226:227], v125 offset:9216\n" \
    "ds_read_b64_tr_b16 a[228:229], v125 offset:10240\n" \
    "ds_read_b64_tr_b16 a[230:231], v125 offset:11264\n" \
    "ds_read_b64_tr_b16 a[232:233], v125 offset:8200\n" \
    "ds_read_b64_tr_b16 a[234:235], v125 offset:9224\n" \
    "ds_read_b64_tr_b16 a[236:237], v125 offset:10248\n" \
    "ds_read_b64_tr_b16 a[238:239], v125 offset:11272\n" \
    "s_waitcnt lgkmcnt(4)\n" \
    "ds_read_b64_tr_b16 a[240:241], v125 offset:12288\n" \
    "ds_read_b64_tr_b16 a[242:243], v125 offset:13312\n" \
    "ds_read_b64_tr_b16 a[244:245], v125 offset:14336\n" \
    "ds_read_b64_tr_b16 a[246:247], v125 offset:15360\n" \
    "ds_read_b64_tr_b16 a[248:249], v125 offset:12296\n" \
    "ds_read_b64_tr_b16 a[250:251], v125 offset:13320\n" \
    "ds_read_b64_tr_b16 a[252:253], v125 offset:14344\n" \
    "ds_read_b64_tr_b16 a[254:255], v125 offset:15368\n" \
    "s_barrier\n" \
    "s_waitcnt lgkmcnt(0)\n" \
    "s_mov_b32 s62, 33024\n" \
    "Lvo_s:\n" \
    "buffer_load_dword v1, off, s[40:43], s62 sc1\n" \
    "s_cmp_eq_u32 s90, 0\n" \
    "s_cbranch_scc1 Lvo_s_x\n" \
    "s_sub_u32 s90, s90, 1\n" \
    "s_waitcnt vmcnt(0)\n" \
    "v_cmp_gt_u32 vcc, 1, v1\n" \
    "s_cbranch_vccnz Lvo_s\n" \
    "Lvo_s_x:\n" \
    "s_waitcnt vmcnt(0) lgkmcnt(0)\n" \
    "s_barrier\n" \
    "v_readfirstlane_b32 s10, v1\n" \
    "s_and_b32 s21, s10, 0x7000\n" \
    "s_lshr_b32 s22, s10, 16\n" \
    "s_and_b32 s22, s22, 0x7000\n" \
    "s_add_u32 s22, s22, 256\n" \
    "s_and_b32 s52, s10, 0x2000\n" \
    "s_lshl_b32 s7, s10, 13\n" \
    "s_and_b32 s7, s7, 0x4000\n" \
    "s_bfe_u32 s6, s10, 0x10001\n" \
    "s_add_u32 s6, s6, 4\n" \
    "s_mov_b32 s57, 0\n" \
    "s_mov_b32 s62, 0\n" \
    "s_mov_b32 s63, 0\n" \
    "s_mov_b32 s64, 0\n" \
    "s_mov_b32 s65, 0\n" \
    "s_mov_b32 s66, 0\n" \
    "s_mov_b32 s67, 0\n" \
    "s_mov_b32 s68, 0\n" \
    "s_mov_b32 s69, 0\n" \
    "s_mov_b32 s70, 0\n" \
    "s_mov_b32 s71, 0\n" \
    "s_mov_b32 s72, 0\n" \
    "s_mov_b32 s73, 0\n" \
    "s_mov_b32 s74, 0\n" \
    "s_mov_b32 s75, 0\n" \
    "s_mov_b32 s76, 0\n" \
    "s_mov_b32 s77, 0\n" \
    "s_mov_b32 s78, 0\n" \
    "s_mov_b32 s79, 0\n" \
    "s_mov_b32 s80, 0\n" \
    "s_mov_b32 s81, 0\n" \
    "s_mov_b32 s82, 0\n" \
    "s_mov_b32 s83, 0\n" \
    "s_mov_b32 s84, 0\n" \
    "s_mov_b32 s85, 0\n" \
    "s_mov_b32 s86, 0\n" \
    "s_mov_b32 s87, 0\n" \
    "s_mov_b32 s88, 0\n" \
    "s_mov_b32 s89, 0\n" \
    "s_mov_b32 s92, 0\n" \
    "s_mov_b32 s93, 0\n" \
    "s_mov_b32 s94, 0\n" \
    "s_mov_b32 s95, 0\n" \
    "v_mov_b64 v[64:65], -1\n" \
    "v_mov_b64 v[66:67], -1\n" \
    "v_xor_b32 v124, s7, v110\n" \
    "v_mov_b32 v125, v111\n" \
    "s_lshl_b32 s98, s6, 15\n" \
    "s_add_u32 s98, s98, s52\n" \
    "s_add_u32 s98, s98, 65536\n" \
    "v_xor_b32 v112, s7, v112\n" \
    "v_xor_b32 v114, s7, v114\n" \
    "s_mov_b32 s17, 0\n" \
    "s_mov_b32 s12, 0\n" \
    "s_cmp_lg_u32 s56, 0\n" \
    "s_cselect_b32 s50, s42, 0\n" \
    "s_add_u32 s14, s56, s22\n" \
    "s_mov_b64 s[8:9], s[40:41]\n" \
    "s_mov_b32 s10, 0\n" \
    "s_mov_b32 s11, s43\n" \
    "v_mov_b32 v75, 0x3000\n" \
    "v_mov_b32 v74, 0\n" \
    "v_mov_b32 v73, 0\n" \
    "ds_write_b32 v75, v74\n" \
    "Lvo_step:\n" \
    "s_mov_b32 s58, s98\n" \
    "v_mov_b64 v[0:1], 0\n" \
    "v_mov_b64 v[2:3], 0\n" \
    "v_mov_b64 v[4:5], 0\n" \
    "v_mov_b64 v[6:7], 0\n" \
    "v_mov_b64 v[8:9], 0\n" \
    "v_mov_b64 v[10:11], 0\n" \
    "v_mov_b64 v[12:13], 0\n" \
    "v_mov_b64 v[14:15], 0\n" \
    "s_and_b32 s62, s55, s12\n" \
    "s_mov_b32 s12, -1\n" \
    "Lvo_d:\n" \
    "s_sub_u32 s62, s62, 1\n" \
    "s_cbranch_scc0 Lvo_d\n" \
    "buffer_load_dwordx4 v[16:19], v112, s[40:43], s58 offen offset:0 sc1\n" \
    "s_nop 12\n" \
    "buffer_load_dwordx4 v[20:23], v112, s[40:43], s58 offen offset:1024 sc1\n" \
    "s_nop 12\n" \
    "buffer_load_dwordx4 v[24:27], v112, s[40:43], s58 offen offset:2048 sc1\n" \
    "s_nop 12\n" \
    "buffer_load_dwordx4 v[28:31], v112, s[40:43], s58 offen offset:3072 sc1\n" \
    "s_mov_b64 s[18:19], -1\n" \
    "s_add_u32 s23, s57, 1\n" \
    "s_and_b32 s62, s57, 3\n" \
    "s_lshl_b32 s96, s62, 17\n" \
    "s_add_u32 s96, s96, s52\n" \
    "s_add_u32 s96, s96, 524288\n" \
    "s_add_u32 s97, s96, 4096\n" \
    "s_add_u32 s60, s96, 0x80000\n" \
    "s_add_u32 s62, s57, 3\n" \
    "s_and_b32 s62, s62, 3\n" \
    "s_lshl_b32 s59, s62, 15\n" \
    "s_add_u32 s59, s59, s52\n" \
    "s_add_u32 s59, s59, 65536\n" \
    "s_lshl_b32 s61, s62, 17\n" \
    "s_add_u32 s61, s61, s52\n" \
    "s_add_u32 s61, s61, 524288\n" \
    "s_add_u32 s61, s61, 0x80000\n" \
    "s_and_b32 s62, s23, 3\n" \
    "s_lshl_b32 s98, s62, 15\n" \
    "s_add_u32 s98, s98, s52\n" \
    "s_add_u32 s98, s98, 65536\n" \
    "v_mov_b32 v69, s23\n" \
    "Lvo_r0:\n" \
    "s_waitcnt vmcnt(3)\n" \
    "v_smfmac_f32_16x16x64_bf16 v[0:3], v[16:19], v[128:135], v121\n" \
    "v_smfmac_f32_16x16x64_bf16 v[4:7], v[16:19], v[136:143], v121\n" \
    "v_smfmac_f32_16x16x64_bf16 v[8:11], v[16:19], v[144:151], v121\n" \
    "v_smfmac_f32_16x16x64_bf16 v[12:15], v[16:19], v[152:159], v121\n" \
    "s_waitcnt vmcnt(2)\n" \
    "v_smfmac_f32_16x16x64_bf16 v[0:3], v[20:23], v[160:167], v121\n" \
    "v_smfmac_f32_16x16x64_bf16 v[4:7], v[20:23], v[168:175], v121\n" \
    "v_smfmac_f32_16x16x64_bf16 v[8:11], v[20:23], v[176:183], v121\n" \
    "v_smfmac_f32_16x16x64_bf16 v[12:15], v[20:23], v[184:191], v121\n" \
    "s_waitcnt vmcnt(1)\n" \
    "v_smfmac_f32_16x16x64_bf16 v[0:3], v[24:27], v[192:199], v121\n" \
    "v_smfmac_f32_16x16x64_bf16 v[4:7], v[24:27], v[200:207], v121\n" \
    "v_smfmac_f32_16x16x64_bf16 v[8:11], v[24:27], v[208:215], v121\n" \
    "v_smfmac_f32_16x16x64_bf16 v[12:15], v[24:27], v[216:223], v121\n" \
    "s_waitcnt vmcnt(0)\n" \
    "v_smfmac_f32_16x16x64_bf16 v[0:3], v[28:31], v[224:231], v121\n" \
    "v_smfmac_f32_16x16x64_bf16 v[4:7], v[28:31], v[232:239], v121\n" \
    "v_smfmac_f32_16x16x64_bf16 v[8:11], v[28:31], v[240:247], v121\n" \
    "v_smfmac_f32_16x16x64_bf16 v[12:15], v[28:31], v[248:255], v121\n" \
    "v_mov_b32 v70, v70\n" \
    "v_permlane16_swap_b32 v0, v4\n" \
    "v_permlane16_swap_b32 v1, v5\n" \
    "v_permlane16_swap_b32 v2, v6\n" \
    "v_permlane16_swap_b32 v3, v7\n" \
    "v_pk_add_f32 v[0:1], v[0:1], v[4:5]\n" \
    "v_pk_add_f32 v[2:3], v[2:3], v[6:7]\n" \
    "ds_write_b128 v118, v[0:3]\n" \
    "v_permlane16_swap_b32 v8, v12\n" \
    "v_permlane16_swap_b32 v9, v13\n" \
    "v_permlane16_swap_b32 v10, v14\n" \
    "v_permlane16_swap_b32 v11, v15\n" \
    "v_pk_add_f32 v[8:9], v[8:9], v[12:13]\n" \
    "v_pk_add_f32 v[10:11], v[10:11], v[14:15]\n" \
    "ds_write_b128 v118, v[8:11] offset:1088\n" \
    "v_cmp_u_f32 s[30:31], v0, v1\n" \
    "v_cmp_u_f32 vcc, v2, v3\n" \
    "s_or_b64 s[30:31], s[30:31], vcc\n" \
    "s_and_b64 s[30:31], s[30:31], s[18:19]\n" \
    "s_cbranch_scc1 Lvo_r0c\n" \
    "s_cmp_lg_u32 s54, 1\n" \
    "s_waitcnt lgkmcnt(0)\n" \
    "s_barrier\n" \
    "s_cbranch_scc1 Lvo_v_x\n" \
    "ds_read_b128 v[32:35], v120 offset:0\n" \
    "ds_read_b128 v[36:39], v120 offset:64\n" \
    "ds_read_b128 v[40:43], v120 offset:2304\n" \
    "ds_read_b128 v[44:47], v120 offset:2368\n" \
    "ds_read_b128 v[48:51], v120 offset:4608\n" \
    "ds_read_b128 v[52:55], v120 offset:4672\n" \
    "ds_read_b128 v[56:59], v120 offset:6912\n" \
    "ds_read_b128 v[60:63], v120 offset:6976\n" \
    "s_waitcnt lgkmcnt(5)\n" \
    "v_pk_add_f32 v[32:33], v[32:33], v[40:41]\n" \
    "v_pk_add_f32 v[34:35], v[34:35], v[42:43]\n" \
    "s_waitcnt lgkmcnt(4)\n" \
    "v_pk_add_f32 v[36:37], v[36:37], v[44:45]\n" \
    "v_pk_add_f32 v[38:39], v[38:39], v[46:47]\n" \
    "s_waitcnt lgkmcnt(3)\n" \
    "v_pk_add_f32 v[32:33], v[32:33], v[48:49]\n" \
    "v_pk_add_f32 v[34:35], v[34:35], v[50:51]\n" \
    "s_waitcnt lgkmcnt(2)\n" \
    "v_pk_add_f32 v[36:37], v[36:37], v[52:53]\n" \
    "v_pk_add_f32 v[38:39], v[38:39], v[54:55]\n" \
    "s_waitcnt lgkmcnt(1)\n" \
    "v_pk_add_f32 v[32:33], v[32:33], v[56:57]\n" \
    "v_pk_add_f32 v[34:35], v[34:35], v[58:59]\n" \
    "s_waitcnt lgkmcnt(0)\n" \
    "v_pk_add_f32 v[36:37], v[36:37], v[60:61]\n" \
    "v_pk_add_f32 v[38:39], v[38:39], v[62:63]\n" \
    "v_cvt_pk_bf16_f32 v16, v32, v33\n" \
    "v_cvt_pk_bf16_f32 v17, v34, v35\n" \
    "v_cvt_pk_bf16_f32 v18, v36, v37\n" \
    "v_cvt_pk_bf16_f32 v19, v38, v39\n" \
    "buffer_store_dwordx4 v[16:19], v115, s[40:43], s60 offen sc1\n" \
    "Lvo_v_x:\n" \
    "s_cmp_lg_u32 s54, 0\n" \
    "s_cbranch_scc1 Lvo_e_x\n" \
    "s_sleep 3\n" \
    "Lvo_e_x:\n" \
    "s_sleep 0\n" \
    "buffer_store_dwordx4 v[64:67], v115, s[40:43], s61 offen sc1\n" \
    "buffer_store_dwordx4 v[64:67], v114, s[40:43], s59 offen sc1\n" \
    "s_cmp_eq_u32 s57, 63\n" \
    "s_cbranch_scc1 Lvo_end\n" \
    "v_mov_b64 v[0:1], 0\n" \
    "v_mov_b64 v[2:3], 0\n" \
    "v_mov_b64 v[4:5], 0\n" \
    "v_mov_b64 v[6:7], 0\n" \
    "v_mov_b64 v[8:9], 0\n" \
    "v_mov_b64 v[10:11], 0\n" \
    "v_mov_b64 v[12:13], 0\n" \
    "v_mov_b64 v[14:15], 0\n" \
    "s_mov_b64 s[18:19], -1\n" \
    "s_waitcnt vmcnt(0) lgkmcnt(0)\n" \
    "s_cmp_lg_u32 s54, 2\n" \
    "s_cbranch_scc1 Lvo_c_x\n" \
    "v_mov_b32 v75, 0x3000\n" \
    "ds_read_b32 v76, v75\n" \
    "s_waitcnt lgkmcnt(0)\n" \
    "v_readfirstlane_b32 s63, v76\n" \
    "s_cmp_lg_u32 s63, s17\n" \
    "s_cselect_b32 s62, 1, 0\n" \
    "s_add_u32 s53, s53, s62\n" \
    "s_min_u32 s53, s53, 40\n" \
    "s_mov_b32 s17, s63\n" \
    "Lvo_c:\n" \
    "buffer_load_dword v71, off, s[40:43], s21 sc1\n" \
    "s_cmp_eq_u32 s90, 0\n" \
    "s_cbranch_scc1 Lvo_c_x\n" \
    "s_sub_u32 s90, s90, 1\n" \
    "s_waitcnt vmcnt(0)\n" \
    "v_cmp_gt_u32 vcc, s23, v71\n" \
    "s_cbranch_vccnz Lvo_c\n" \
    "s_mov_b32 s62, s53\n" \
    "Lvo_c_d:\n" \
    "s_sub_u32 s62, s62, 1\n" \
    "s_cbranch_scc0 Lvo_c_d\n" \
    "Lvo_c_x:\n" \
    "s_barrier\n" \
    "buffer_load_dwordx4 v[32:35], v113, s[40:43], s96 offen offset:0 sc1\n" \
    "s_nop 12\n" \
    "buffer_load_dwordx4 v[36:39], v113, s[40:43], s96 offen offset:1024 sc1\n" \
    "s_nop 12\n" \
    "buffer_load_dwordx4 v[40:43], v113, s[40:43], s96 offen offset:2048 sc1\n" \
    "s_nop 12\n" \
    "buffer_load_dwordx4 v[44:47], v113, s[40:43], s96 offen offset:3072 sc1\n" \
    "s_nop 12\n" \
    "buffer_load_dwordx4 v[48:51], v113, s[40:43], s97 offen offset:0 sc1\n" \
    "s_nop 12\n" \
    "buffer_load_dwordx4 v[52:55], v113, s[40:43], s97 offen offset:1024 sc1\n" \
    "s_nop 12\n" \
    "buffer_load_dwordx4 v[56:59], v113, s[40:43], s97 offen offset:2048 sc1\n" \
    "s_nop 12\n" \
    "buffer_load_dwordx4 v[60:63], v113, s[40:43], s97 offen offset:3072 sc1\n" \
    "Lvo_r1:\n" \
    "s_waitcnt vmcnt(7)\n" \
    "v_smfmac_f32_16x16x64_bf16 v[0:3], v[32:35], a[0:7], v116\n" \
    "v_cmp_eq_u32 s[30:31], -1, v32\n" \
    "v_smfmac_f32_16x16x64_bf16 v[8:11], v[32:35], a[16:23], v116\n" \
    "v_smfmac_f32_16x16x64_bf16 v[4:7], v[32:35], a[8:15], v116\n" \
    "v_smfmac_f32_16x16x64_bf16 v[12:15], v[32:35], a[24:31], v116\n" \
    "s_and_b64 s[30:31], s[30:31], s[18:19]\n" \
    "s_cbranch_scc1 Lvo_e0\n" \
    "s_waitcnt vmcnt(6)\n" \
    "v_smfmac_f32_16x16x64_bf16 v[0:3], v[36:39], a[32:39], v116\n" \
    "v_cmp_eq_u32 s[30:31], -1, v36\n" \
    "v_smfmac_f32_16x16x64_bf16 v[8:11], v[36:39], a[48:55], v116\n" \
    "v_smfmac_f32_16x16x64_bf16 v[4:7], v[36:39], a[40:47], v116\n" \
    "v_smfmac_f32_16x16x64_bf16 v[12:15], v[36:39], a[56:63], v116\n" \
    "s_and_b64 s[30:31], s[30:31], s[18:19]\n" \
    "s_cbranch_scc1 Lvo_e1\n" \
    "s_waitcnt vmcnt(5)\n" \
    "v_smfmac_f32_16x16x64_bf16 v[0:3], v[40:43], a[64:71], v116\n" \
    "v_cmp_eq_u32 s[30:31], -1, v40\n" \
    "v_smfmac_f32_16x16x64_bf16 v[8:11], v[40:43], a[80:87], v116\n" \
    "v_smfmac_f32_16x16x64_bf16 v[4:7], v[40:43], a[72:79], v116\n" \
    "v_smfmac_f32_16x16x64_bf16 v[12:15], v[40:43], a[88:95], v116\n" \
    "s_and_b64 s[30:31], s[30:31], s[18:19]\n" \
    "s_cbranch_scc1 Lvo_e2\n" \
    "s_waitcnt vmcnt(4)\n" \
    "v_smfmac_f32_16x16x64_bf16 v[0:3], v[44:47], a[96:103], v116\n" \
    "v_cmp_eq_u32 s[30:31], -1, v44\n" \
    "v_smfmac_f32_16x16x64_bf16 v[8:11], v[44:47], a[112:119], v116\n" \
    "v_smfmac_f32_16x16x64_bf16 v[4:7], v[44:47], a[104:111], v116\n" \
    "v_smfmac_f32_16x16x64_bf16 v[12:15], v[44:47], a[120:127], v116\n" \
    "s_and_b64 s[30:31], s[30:31], s[18:19]\n" \
    "s_cbranch_scc1 Lvo_e3\n" \
    "s_waitcnt vmcnt(3)\n" \
    "v_smfmac_f32_16x16x64_bf16 v[0:3], v[48:51], a[128:135], v116\n" \
    "v_smfmac_f32_16x16x64_bf16 v[8:11], v[48:51], a[144:151], v116\n" \
    "v_smfmac_f32_16x16x64_bf16 v[4:7], v[48:51], a[136:143], v116\n" \
    "v_smfmac_f32_16x16x64_bf16 v[12:15], v[48:51], a[152:159], v116\n" \
    "s_waitcnt vmcnt(2)\n" \
    "v_smfmac_f32_16x16x64_bf16 v[0:3], v[52:55], a[160:167], v116\n" \
    "v_smfmac_f32_16x16x64_bf16 v[8:11], v[52:55], a[176:183], v116\n" \
    "buffer_store_dword v69, v117, s[44:47], s22 offen sc1\n" \
    "v_smfmac_f32_16x16x64_bf16 v[4:7], v[52:55], a[168:175], v116\n" \
    "v_smfmac_f32_16x16x64_bf16 v[12:15], v[52:55], a[184:191], v116\n" \
    "s_waitcnt vmcnt(2)\n" \
    "v_smfmac_f32_16x16x64_bf16 v[0:3], v[56:59], a[192:199], v116\n" \
    "v_smfmac_f32_16x16x64_bf16 v[8:11], v[56:59], a[208:215], v116\n" \
    "v_smfmac_f32_16x16x64_bf16 v[4:7], v[56:59], a[200:207], v116\n" \
    "v_smfmac_f32_16x16x64_bf16 v[12:15], v[56:59], a[216:223], v116\n" \
    "s_waitcnt vmcnt(1)\n" \
    "v_smfmac_f32_16x16x64_bf16 v[0:3], v[60:63], a[224:231], v116\n" \
    "v_smfmac_f32_16x16x64_bf16 v[8:11], v[60:63], a[240:247], v116\n" \
    "v_smfmac_f32_16x16x64_bf16 v[4:7], v[60:63], a[232:239], v116\n" \
    "v_smfmac_f32_16x16x64_bf16 v[12:15], v[60:63], a[248:255], v116\n" \
    "v_permlane16_swap_b32 v0, v1\n" \
    "v_permlane16_swap_b32 v2, v3\n" \
    "v_permlane16_swap_b32 v8, v9\n" \
    "v_permlane16_swap_b32 v10, v11\n" \
    "v_add_f32 v0, v0, v9\n" \
    "v_add_f32 v2, v2, v11\n" \
    "v_permlane16_swap_b32 v4, v5\n" \
    "v_permlane16_swap_b32 v6, v7\n" \
    "v_permlane16_swap_b32 v12, v13\n" \
    "v_permlane16_swap_b32 v14, v15\n" \
    "v_add_f32 v1, v4, v13\n" \
    "v_add_f32 v3, v6, v15\n" \
    "v_permlane32_swap_b32 v0, v2\n" \
    "v_mov_b32 v70, v70\n" \
    "v_permlane32_swap_b32 v1, v3\n" \
    "v_pk_add_f32 v[0:1], v[0:1], v[2:3]\n" \
    "ds_write_b64 v119, v[0:1]\n" \
    "v_cmp_u_f32 s[30:31], v0, v1\n" \
    "buffer_store_dword v69, v117, s[8:11], s14 offen sc1\n" \
    "s_mov_b32 s10, 0\n" \
    "s_and_b64 s[30:31], s[30:31], s[18:19]\n" \
    "s_cbranch_scc1 Lvo_r1c\n" \
    "s_cmp_lg_u32 s54, 0\n" \
    "s_waitcnt lgkmcnt(0)\n" \
    "s_barrier\n" \
    "s_cbranch_scc1 Lvo_h\n" \
    "ds_read2st64_b64 v[0:3], v125 offset0:0 offset1:1\n" \
    "ds_read2st64_b64 v[4:7], v125 offset0:2 offset1:3\n" \
    "buffer_load_dword v73, v127, s[40:43], s22 offen offset:256 sc1\n" \
    "s_waitcnt lgkmcnt(1)\n" \
    "v_pk_add_f32 v[0:1], v[0:1], v[2:3]\n" \
    "s_waitcnt lgkmcnt(0)\n" \
    "v_pk_add_f32 v[4:5], v[4:5], v[6:7]\n" \
    "v_pk_add_f32 v[0:1], v[0:1], v[4:5]\n" \
    "v_cvt_pk_bf16_f32 v16, v0, v1\n" \
    "buffer_store_dword v16, v124, s[40:43], s98 offen sc1\n" \
    "s_waitcnt vmcnt(1)\n" \
    "s_branch Lvo_h_k\n" \
    "Lvo_h0:\n" \
    "s_branch Lvo_h\n" \
    "Lvo_h2:\n" \
    "s_mov_b32 s12, 0\n" \
    "Lvo_h:\n" \
    "buffer_load_dword v73, v127, s[40:43], s22 offen offset:256 sc1\n" \
    "s_cmp_eq_u32 s90, 0\n" \
    "s_cbranch_scc1 Lvo_h_x\n" \
    "s_sub_u32 s90, s90, 1\n" \
    "s_waitcnt vmcnt(0)\n" \
    "Lvo_h_k:\n" \
    "s_or_b32 s62, s23, 0x80000000\n" \
    "v_cmp_eq_u32 vcc, s62, v73\n" \
    "s_cbranch_vccnz Lvo_h2\n" \
    "Lvo_h_x:\n" \
    "s_mov_b32 s57, s23\n" \
    "s_branch Lvo_step\n" \
    "Lvo_e0:\n" \
    "v_bfrev_b32 v74, 1\n" \
    "v_add_u32 v74, s23, v74\n" \
    "buffer_store_dword v74, v117, s[48:51], s14 offen sc1\n" \
    "s_mov_b32 s10, s50\n" \
    "buffer_load_dwordx4 v[32:35], v113, s[40:43], s96 offen offset:0 sc1\n" \
    "v_mov_b32 v75, 12288\n" \
    "s_waitcnt vmcnt(0)\n" \
    "s_branch Lvo_r1c_k\n" \
    "Lvo_e1:\n" \
    "v_bfrev_b32 v74, 1\n" \
    "v_add_u32 v74, s23, v74\n" \
    "buffer_store_dword v74, v117, s[48:51], s14 offen sc1\n" \
    "s_mov_b32 s10, s50\n" \
    "buffer_load_dwordx4 v[36:39], v113, s[40:43], s96 offen offset:1024 sc1\n" \
    "v_mov_b32 v75, 12288\n" \
    "s_waitcnt vmcnt(0)\n" \
    "s_branch Lvo_r1c_k\n" \
    "Lvo_e2:\n" \
    "v_bfrev_b32 v74, 1\n" \
    "v_add_u32 v74, s23, v74\n" \
    "buffer_store_dword v74, v117, s[48:51], s14 offen sc1\n" \
    "s_mov_b32 s10, s50\n" \
    "buffer_load_dwordx4 v[40:43], v113, s[40:43], s96 offen offset:2048 sc1\n" \
    "v_mov_b32 v75, 12292\n" \
    "s_waitcnt vmcnt(0)\n" \
    "s_branch Lvo_r1c_k\n" \
    "Lvo_e3:\n" \
    "v_bfrev_b32 v74, 1\n" \
    "v_add_u32 v74, s23, v74\n" \
    "buffer_store_dword v74, v117, s[48:51], s14 offen sc1\n" \
    "s_mov_b32 s10, s50\n" \
    "buffer_load_dwordx4 v[44:47], v113, s[40:43], s96 offen offset:3072 sc1\n" \
    "v_mov_b32 v75, 12292\n" \
    "s_waitcnt vmcnt(0)\n" \
    "s_branch Lvo_r1c_k\n" \
    "Lvo_r0c:\n" \
    "buffer_load_dwordx4 v[16:19], v112, s[40:43], s58 offen offset:0 sc1\n" \
    "buffer_load_dwordx4 v[20:23], v112, s[40:43], s58 offen offset:1024 sc1\n" \
    "buffer_load_dwordx4 v[24:27], v112, s[40:43], s58 offen offset:2048 sc1\n" \
    "buffer_load_dwordx4 v[28:31], v112, s[40:43], s58 offen offset:3072 sc1\n" \
    "Lvo_r0c_k:\n" \
    "s_waitcnt vmcnt(3)\n" \
    "v_max_u32 v68, v16, v17\n" \
    "v_max3_u32 v68, v68, v18, v19\n" \
    "s_waitcnt vmcnt(2)\n" \
    "v_max3_u32 v68, v68, v20, v21\n" \
    "v_max3_u32 v68, v68, v22, v23\n" \
    "s_waitcnt vmcnt(1)\n" \
    "v_max3_u32 v68, v68, v24, v25\n" \
    "v_max3_u32 v68, v68, v26, v27\n" \
    "s_waitcnt vmcnt(0)\n" \
    "v_max3_u32 v68, v68, v28, v29\n" \
    "v_max3_u32 v68, v68, v30, v31\n" \
    "v_cmp_eq_u32 vcc, -1, v68\n" \
    "s_cbranch_vccz Lvo_r0c_x\n" \
    "s_cmp_eq_u32 s90, 0\n" \
    "s_cbranch_scc1 Lvo_r0c_x\n" \
    "s_sub_u32 s90, s90, 1\n" \
    "s_branch Lvo_r0c\n" \
    "Lvo_r0c_x:\n" \
    "v_cmp_eq_u32 vcc, s57, v73\n" \
    "s_cmp_eq_u64 vcc, 0\n" \
    "s_cselect_b32 s62, 1, 0\n" \
    "s_add_u32 s55, s55, s62\n" \
    "s_min_u32 s55, s55, 40\n" \
    "v_mov_b64 v[0:1], 0\n" \
    "v_mov_b64 v[2:3], 0\n" \
    "v_mov_b64 v[4:5], 0\n" \
    "v_mov_b64 v[6:7], 0\n" \
    "v_mov_b64 v[8:9], 0\n" \
    "v_mov_b64 v[10:11], 0\n" \
    "v_mov_b64 v[12:13], 0\n" \
    "v_mov_b64 v[14:15], 0\n" \
    "s_mov_b64 s[18:19], 0\n" \
    "s_waitcnt lgkmcnt(0)\n" \
    "s_branch Lvo_r0\n" \
    "Lvo_r1c:\n" \
    "v_bfrev_b32 v74, 1\n" \
    "v_add_u32 v74, s23, v74\n" \
    "buffer_store_dword v74, v117, s[48:51], s14 offen sc1\n" \
    "s_mov_b32 s10, s50\n" \
    "Lvo_r1c_l:\n" \
    "v_mov_b32 v75, 0x3004\n" \
    "buffer_load_dwordx4 v[32:35], v113, s[40:43], s96 offen offset:0 sc1\n" \
    "buffer_load_dwordx4 v[36:39], v113, s[40:43], s96 offen offset:1024 sc1\n" \
    "buffer_load_dwordx4 v[40:43], v113, s[40:43], s96 offen offset:2048 sc1\n" \
    "buffer_load_dwordx4 v[44:47], v113, s[40:43], s96 offen offset:3072 sc1\n" \
    "buffer_load_dwordx4 v[48:51], v113, s[40:43], s97 offen offset:0 sc1\n" \
    "buffer_load_dwordx4 v[52:55], v113, s[40:43], s97 offen offset:1024 sc1\n" \
    "buffer_load_dwordx4 v[56:59], v113, s[40:43], s97 offen offset:2048 sc1\n" \
    "buffer_load_dwordx4 v[60:63], v113, s[40:43], s97 offen offset:3072 sc1\n" \
    "Lvo_r1c_k:\n" \
    "s_waitcnt vmcnt(7)\n" \
    "v_max_u32 v68, v32, v33\n" \
    "v_max3_u32 v68, v68, v34, v35\n" \
    "s_waitcnt vmcnt(6)\n" \
    "v_max3_u32 v68, v68, v36, v37\n" \
    "v_max3_u32 v68, v68, v38, v39\n" \
    "s_waitcnt vmcnt(5)\n" \
    "v_max3_u32 v68, v68, v40, v41\n" \
    "v_max3_u32 v68, v68, v42, v43\n" \
    "s_waitcnt vmcnt(4)\n" \
    "v_max3_u32 v68, v68, v44, v45\n" \
    "v_max3_u32 v68, v68, v46, v47\n" \
    "s_waitcnt vmcnt(3)\n" \
    "v_max3_u32 v68, v68, v48, v49\n" \
    "v_max3_u32 v68, v68, v50, v51\n" \
    "s_waitcnt vmcnt(2)\n" \
    "v_max3_u32 v68, v68, v52, v53\n" \
    "v_max3_u32 v68, v68, v54, v55\n" \
    "s_waitcnt vmcnt(1)\n" \
    "v_max3_u32 v68, v68, v56, v57\n" \
    "v_max3_u32 v68, v68, v58, v59\n" \
    "s_waitcnt vmcnt(0)\n" \
    "v_max3_u32 v68, v68, v60, v61\n" \
    "v_max3_u32 v68, v68, v62, v63\n" \
    "v_cmp_eq_u32 vcc, -1, v68\n" \
    "s_cbranch_vccz Lvo_r1c_x\n" \
    "s_cmp_eq_u32 s90, 0\n" \
    "s_cbranch_scc1 Lvo_r1c_x\n" \
    "s_sub_u32 s90, s90, 1\n" \
    "s_branch Lvo_r1c_l\n" \
    "Lvo_r1c_x:\n" \
    "ds_write_b32 v75, v69\n" \
    "v_mov_b64 v[0:1], 0\n" \
    "v_mov_b64 v[2:3], 0\n" \
    "v_mov_b64 v[4:5], 0\n" \
    "v_mov_b64 v[6:7], 0\n" \
    "v_mov_b64 v[8:9], 0\n" \
    "v_mov_b64 v[10:11], 0\n" \
    "v_mov_b64 v[12:13], 0\n" \
    "v_mov_b64 v[14:15], 0\n" \
    "s_mov_b64 s[18:19], 0\n" \
    "s_waitcnt lgkmcnt(0)\n" \
    "s_branch Lvo_r1\n" \
    "Lvo_end:\n" \
    "s_waitcnt vmcnt(0) lgkmcnt(0)\n" \
    "s_cmp_lg_u32 s90, 0\n" \
    "s_cbranch_scc1 7f\n" \
    "v_mov_b32 v0, 1\n" \
    "s_mov_b32 s62, 32768\n" \
    "buffer_store_dword v0, v117, s[40:43], s62 offen sc1\n" \
    "7:\n" \
    "s_endpgm\n"
#define GEMM8_ASM \
    "ds_read_b32 v254, v253\n" \
    "s_mov_b32 s77, -98304\n" \
    "s_mov_b32 s81, 136\n" \
    "s_lshl_b32 s78, s60, 10\n" \
    "s_lshl_b32 s79, s60, 12\n" \
    "s_add_u32 s79, s79, 32768\n" \
    "s_mov_b32 s43, 0x20000\n" \
    "s_mov_b32 s47, 0x20000\n" \
    "s_mov_b32 s51, 0x20000\n" \
    "s_mov_b32 s59, 0x20000\n" \
    "s_mov_b64 s[48:49], s[52:53]\n" \
    "s_mov_b32 s50, 0\n" \
    "s_mov_b64 s[56:57], s[52:53]\n" \
    "s_mov_b32 s58, 0\n" \
    "s_mov_b32 s63, 0\n" \
    "s_mov_b32 s64, 0\n" \
    "s_mov_b32 s65, 0\n" \
    "s_mov_b32 s66, 0\n" \
    "s_mov_b32 s67, 0\n" \
    "s_mov_b32 s74, 0\n" \
    "v_mov_b32 v15, v249\n" \
    "s_waitcnt lgkmcnt(0)\n" \
    "v_readlane_b32 s40, v254, 0\n" \
    "v_readlane_b32 s41, v254, 1\n" \
    "v_readlane_b32 s42, v254, 2\n" \
    "v_readlane_b32 s44, v254, 3\n" \
    "v_readlane_b32 s45, v254, 4\n" \
    "v_readlane_b32 s46, v254, 5\n" \
    "v_readlane_b32 s61, v254, 6\n" \
    "v_readlane_b32 s73, v254, 7\n" \
    "s_lshr_b32 s73, s73, 16\n" \
    "s_lshl_b32 s68, s61, 4\n" \
    "s_mul_i32 s69, s61, 12\n" \
    "s_lshr_b32 s70, s61, 1\n" \
    "v_mad_u32_u24 v14, v247, s61, v248\n" \
    "s_nop 0\n" \
    "s_add_u32 m0, s74, s79\n" \
    "s_mul_i32 s72, s73, s70\n" \
    "s_nop 0\n" \
    "buffer_load_dwordx4 v14, s[44:47], s72 offen lds\n" \
    "s_add_u32 m0, m0, 1024\n" \
    "s_add_u32 s72, s72, s68\n" \
    "s_nop 0\n" \
    "buffer_load_dwordx4 v14, s[44:47], s72 offen lds\n" \
    "s_add_u32 m0, m0, 1024\n" \
    "s_sub_u32 s72, s72, s69\n" \
    "s_nop 0\n" \
    "buffer_load_dwordx4 v14, s[44:47], s72 offen lds\n" \
    "s_add_u32 m0, m0, 1024\n" \
    "s_add_u32 s72, s72, s68\n" \
    "s_nop 0\n" \
    "buffer_load_dwordx4 v14, s[44:47], s72 offen lds\n" \
    "s_add_u32 m0, s74, s78\n" \
    "s_mov_b32 s71, s73\n" \
    "s_nop 0\n" \
    "buffer_load_dwordx4 v246, s[40:43], s71 offen sc1 lds\n" \
    "s_add_u32 s73, s73, 128\n" \
    "s_add_u32 m0, m0, 4096\n" \
    "s_add_u32 s71, s71, 65536\n" \
    "s_nop 0\n" \
    "buffer_load_dwordx4 v246, s[40:43], s71 offen sc1 lds\n" \
    "s_and_b32 s73, s73, 2047\n" \
    "s_add_u32 m0, m0, 4096\n" \
    "s_add_u32 s71, s71, 65536\n" \
    "s_nop 0\n" \
    "buffer_load_dwordx4 v246, s[40:43], s71 offen sc1 lds\n" \
    "s_add_u32 m0, m0, 4096\n" \
    "s_add_u32 s71, s71, 65536\n" \
    "s_nop 0\n" \
    "buffer_load_dwordx4 v246, s[40:43], s71 offen sc1 lds\n" \
    "s_add_u32 m0, m0, 4096\n" \
    "s_add_u32 s71, s71, 65536\n" \
    "s_nop 0\n" \
    "buffer_load_dwordx4 v246, s[40:43], s71 offen sc1 lds\n" \
    "s_add_u32 m0, m0, 4096\n" \
    "s_add_u32 s71, s71, 65536\n" \
    "s_nop 0\n" \
    "buffer_load_dwordx4 v246, s[40:43], s71 offen sc1 lds\n" \
    "s_add_u32 m0, m0, 4096\n" \
    "s_add_u32 s71, s71, 65536\n" \
    "s_nop 0\n" \
    "buffer_load_dwordx4 v246, s[40:43], s71 offen sc1 lds\n" \
    "s_add_u32 m0, m0, 4096\n" \
    "s_add_u32 s71, s71, 65536\n" \
    "s_nop 0\n" \
    "buffer_load_dwordx4 v246, s[40:43], s71 offen sc1 lds\n" \
    "s_add_u32 s74, s74, 49152\n" \
    "s_cmp_eq_u32 s74, 147456\n" \
    "s_cselect_b32 s74, 0, s74\n" \
    "s_add_u32 m0, s74, s79\n" \
    "s_mul_i32 s72, s73, s70\n" \
    "s_nop 0\n" \
    "buffer_load_dwordx4 v14, s[44:47], s72 offen lds\n" \
    "s_add_u32 m0, m0, 1024\n" \
    "s_add_u32 s72, s72, s68\n" \
    "s_nop 0\n" \
    "buffer_load_dwordx4 v14, s[44:47], s72 offen lds\n" \
    "s_add_u32 m0, m0, 1024\n" \
    "s_sub_u32 s72, s72, s69\n" \
    "s_nop 0\n" \
    "buffer_load_dwordx4 v14, s[44:47], s72 offen lds\n" \
    "s_add_u32 m0, m0, 1024\n" \
    "s_add_u32 s72, s72, s68\n" \
    "s_nop 0\n" \
    "buffer_load_dwordx4 v14, s[44:47], s72 offen lds\n" \
    "s_add_u32 m0, s74, s78\n" \
    "s_mov_b32 s71, s73\n" \
    "s_nop 0\n" \
    "buffer_load_dwordx4 v246, s[40:43], s71 offen sc1 lds\n" \
    "s_add_u32 s73, s73, 128\n" \
    "s_add_u32 m0, m0, 4096\n" \
    "s_add_u32 s71, s71, 65536\n" \
    "s_nop 0\n" \
    "buffer_load_dwordx4 v246, s[40:43], s71 offen sc1 lds\n" \
    "s_and_b32 s73, s73, 2047\n" \
    "s_add_u32 m0, m0, 4096\n" \
    "s_add_u32 s71, s71, 65536\n" \
    "s_nop 0\n" \
    "buffer_load_dwordx4 v246, s[40:43], s71 offen sc1 lds\n" \
    "s_add_u32 m0, m0, 4096\n" \
    "s_add_u32 s71, s71, 65536\n" \
    "s_nop 0\n" \
    "buffer_load_dwordx4 v246, s[40:43], s71 offen sc1 lds\n" \
    "s_add_u32 m0, m0, 4096\n" \
    "s_add_u32 s71, s71, 65536\n" \
    "s_nop 0\n" \
    "buffer_load_dwordx4 v246, s[40:43], s71 offen sc1 lds\n" \
    "s_add_u32 m0, m0, 4096\n" \
    "s_add_u32 s71, s71, 65536\n" \
    "s_nop 0\n" \
    "buffer_load_dwordx4 v246, s[40:43], s71 offen sc1 lds\n" \
    "s_add_u32 m0, m0, 4096\n" \
    "s_add_u32 s71, s71, 65536\n" \
    "s_nop 0\n" \
    "buffer_load_dwordx4 v246, s[40:43], s71 offen sc1 lds\n" \
    "s_add_u32 m0, m0, 4096\n" \
    "s_add_u32 s71, s71, 65536\n" \
    "s_nop 0\n" \
    "buffer_load_dwordx4 v246, s[40:43], s71 offen sc1 lds\n" \
    "s_add_u32 s74, s74, 49152\n" \
    "s_cmp_eq_u32 s74, 147456\n" \
    "s_cselect_b32 s74, 0, s74\n" \
    "s_add_u32 m0, s74, s79\n" \
    "s_mul_i32 s72, s73, s70\n" \
    "s_nop 0\n" \
    "buffer_load_dwordx4 v14, s[44:47], s72 offen lds\n" \
    "s_add_u32 m0, m0, 1024\n" \
    "s_add_u32 s72, s72, s68\n" \
    "s_nop 0\n" \
    "buffer_load_dwordx4 v14, s[44:47], s72 offen lds\n" \
    "s_add_u32 m0, m0, 1024\n" \
    "s_sub_u32 s72, s72, s69\n" \
    "s_nop 0\n" \
    "buffer_load_dwordx4 v14, s[44:47], s72 offen lds\n" \
    "s_add_u32 m0, m0, 1024\n" \
    "s_add_u32 s72, s72, s68\n" \
    "s_nop 0\n" \
    "buffer_load_dwordx4 v14, s[44:47], s72 offen lds\n" \
    "s_add_u32 m0, s74, s78\n" \
    "s_mov_b32 s71, s73\n" \
    "s_nop 0\n" \
    "buffer_load_dwordx4 v246, s[40:43], s71 offen sc1 lds\n" \
    "s_add_u32 s73, s73, 128\n" \
    "s_add_u32 m0, m0, 4096\n" \
    "s_add_u32 s71, s71, 65536\n" \
    "s_nop 0\n" \
    "buffer_load_dwordx4 v246, s[40:43], s71 offen sc1 lds\n" \
    "s_and_b32 s73, s73, 2047\n" \
    "s_add_u32 s74, s74, 49152\n" \
    "s_cmp_eq_u32 s74, 147456\n" \
    "s_cselect_b32 s74, 0, s74\n" \
    "s_waitcnt vmcnt(18)\n" \
    "s_barrier\n" \
    "ds_read_b64_tr_b16 v[176:177], v242 offset:0\n" \
    "ds_read_b64_tr_b16 v[178:179], v242 offset:2048\n" \
    "ds_read_b64_tr_b16 v[180:181], v243 offset:0\n" \
    "ds_read_b64_tr_b16 v[182:183], v243 offset:2048\n" \
    "ds_read_b64_tr_b16 v[184:185], v244 offset:0\n" \
    "ds_read_b64_tr_b16 v[186:187], v244 offset:2048\n" \
    "ds_read_b64_tr_b16 v[188:189], v245 offset:0\n" \
    "ds_read_b64_tr_b16 v[190:191], v245 offset:2048\n" \
    "ds_read_b128 v[144:147], v240 offset:0\n" \
    "ds_read_b128 v[148:151], v240 offset:2048\n" \
    "ds_read_b128 v[152:155], v240 offset:4096\n" \
    "ds_read_b128 v[156:159], v240 offset:6144\n" \
    "ds_read_b128 v[160:163], v240 offset:8192\n" \
    "ds_read_b128 v[164:167], v240 offset:10240\n" \
    "ds_read_b128 v[168:171], v240 offset:12288\n" \
    "ds_read_b128 v[172:175], v240 offset:14336\n" \
    "Lg8_tile%=:\n" \
    "v_readlane_b32 s62, v254, 7\n" \
    "ds_read_b32 v255, v253 offset:256\n" \
    "v_add_u32 v253, 256, v253\n" \
    "s_mov_b32 s75, 16\n" \
    "s_bitcmp1_b32 s62, 0\n" \
    "s_cbranch_scc1 Lg81_in%=\n" \
    "Lg80_in%=:\n" \
    "s_waitcnt lgkmcnt(0)\n" \
    "v_mfma_f32_16x16x32_bf16 v[16:19], v[176:179], v[144:147], 0\n" \
    "ds_read_b64_tr_b16 v[224:225], v242 offset:4096\n" \
    "v_mfma_f32_16x16x32_bf16 v[20:23], v[180:183], v[144:147], 0\n" \
    "s_add_u32 m0, m0, 4096\n" \
    "s_add_u32 s71, s71, 65536\n" \
    "v_mfma_f32_16x16x32_bf16 v[24:27], v[184:187], v[144:147], 0\n" \
    "ds_read_b64_tr_b16 v[226:227], v242 offset:6144\n" \
    "buffer_load_dwordx4 v246, s[40:43], s71 offen sc1 lds\n" \
    "v_mfma_f32_16x16x32_bf16 v[28:31], v[188:191], v[144:147], 0\n" \
    "ds_read_b64_tr_b16 v[228:229], v243 offset:4096\n" \
    "v_mfma_f32_16x16x32_bf16 v[32:35], v[176:179], v[148:151], 0\n" \
    "ds_read_b64_tr_b16 v[230:231], v243 offset:6144\n" \
    "v_mfma_f32_16x16x32_bf16 v[36:39], v[180:183], v[148:151], 0\n" \
    "ds_read_b64_tr_b16 v[232:233], v244 offset:4096\n" \
    "v_mfma_f32_16x16x32_bf16 v[40:43], v[184:187], v[148:151], 0\n" \
    "s_add_u32 m0, m0, 4096\n" \
    "s_add_u32 s71, s71, 65536\n" \
    "v_mfma_f32_16x16x32_bf16 v[44:47], v[188:191], v[148:151], 0\n" \
    "ds_read_b64_tr_b16 v[234:235], v244 offset:6144\n" \
    "buffer_load_dwordx4 v246, s[40:43], s71 offen sc1 lds\n" \
    "v_mfma_f32_16x16x32_bf16 v[48:51], v[176:179], v[152:155], 0\n" \
    "ds_read_b64_tr_b16 v[236:237], v245 offset:4096\n" \
    "v_mfma_f32_16x16x32_bf16 v[52:55], v[180:183], v[152:155], 0\n" \
    "ds_read_b64_tr_b16 v[238:239], v245 offset:6144\n" \
    "v_mfma_f32_16x16x32_bf16 v[56:59], v[184:187], v[152:155], 0\n" \
    "ds_read_b128 v[192:195], v241 offset:0\n" \
    "v_mfma_f32_16x16x32_bf16 v[60:63], v[188:191], v[152:155], 0\n" \
    "s_add_u32 m0, m0, 4096\n" \
    "s_add_u32 s71, s71, 65536\n" \
    "v_mfma_f32_16x16x32_bf16 v[64:67], v[176:179], v[156:159], 0\n" \
    "ds_read_b128 v[196:199], v241 offset:2048\n" \
    "buffer_load_dwordx4 v246, s[40:43], s71 offen sc1 lds\n" \
    "v_mfma_f32_16x16x32_bf16 v[68:71], v[180:183], v[156:159], 0\n" \
    "ds_read_b128 v[200:203], v241 offset:4096\n" \
    "v_mfma_f32_16x16x32_bf16 v[72:75], v[184:187], v[156:159], 0\n" \
    "ds_read_b128 v[204:207], v241 offset:6144\n" \
    "v_mfma_f32_16x16x32_bf16 v[76:79], v[188:191], v[156:159], 0\n" \
    "ds_read_b128 v[208:211], v241 offset:8192\n" \
    "v_mfma_f32_16x16x32_bf16 v[80:83], v[176:179], v[160:163], 0\n" \
    "s_add_u32 m0, m0, 4096\n" \
    "s_add_u32 s71, s71, 65536\n" \
    "v_mfma_f32_16x16x32_bf16 v[84:87], v[180:183], v[160:163], 0\n" \
    "ds_read_b128 v[212:215], v241 offset:10240\n" \
    "buffer_load_dwordx4 v246, s[40:43], s71 offen sc1 lds\n" \
    "v_mfma_f32_16x16x32_bf16 v[88:91], v[184:187], v[160:163], 0\n" \
    "ds_read_b128 v[216:219], v241 offset:12288\n" \
    "v_mfma_f32_16x16x32_bf16 v[92:95], v[188:191], v[160:163], 0\n" \
    "ds_read_b128 v[220:223], v241 offset:14336\n" \
    "v_mfma_f32_16x16x32_bf16 v[96:99], v[176:179], v[164:167], 0\n" \
    "s_cmp_eq_u32 s74, 98304\n" \
    "s_cselect_b32 s76, s77, 49152\n" \
    "v_mfma_f32_16x16x32_bf16 v[100:103], v[180:183], v[164:167], 0\n" \
    "s_add_u32 m0, m0, 4096\n" \
    "s_add_u32 s71, s71, 65536\n" \
    "v_mfma_f32_16x16x32_bf16 v[104:107], v[184:187], v[164:167], 0\n" \
    "buffer_load_dwordx4 v246, s[40:43], s71 offen sc1 lds\n" \
    "v_mfma_f32_16x16x32_bf16 v[108:111], v[188:191], v[164:167], 0\n" \
    "v_add_u32 v242, s76, v242\n" \
    "v_mfma_f32_16x16x32_bf16 v[112:115], v[176:179], v[168:171], 0\n" \
    "v_add_u32 v243, s76, v243\n" \
    "v_mfma_f32_16x16x32_bf16 v[116:119], v[180:183], v[168:171], 0\n" \
    "v_add_u32 v244, s76, v244\n" \
    "v_mfma_f32_16x16x32_bf16 v[120:123], v[184:187], v[168:171], 0\n" \
    "s_add_u32 m0, m0, 4096\n" \
    "s_add_u32 s71, s71, 65536\n" \
    "v_mfma_f32_16x16x32_bf16 v[124:127], v[188:191], v[168:171], 0\n" \
    "buffer_load_dwordx4 v246, s[40:43], s71 offen sc1 lds\n" \
    "v_mfma_f32_16x16x32_bf16 v[128:131], v[176:179], v[172:175], 0\n" \
    "v_add_u32 v245, s76, v245\n" \
    "v_mfma_f32_16x16x32_bf16 v[132:135], v[180:183], v[172:175], 0\n" \
    "v_add_u32 v240, s76, v240\n" \
    "v_mfma_f32_16x16x32_bf16 v[136:139], v[184:187], v[172:175], 0\n" \
    "v_add_u32 v241, s76, v241\n" \
    "v_mfma_f32_16x16x32_bf16 v[140:143], v[188:191], v[172:175], 0\n" \
    "s_waitcnt vmcnt(12) lgkmcnt(0)\n" \
    "v_mfma_f32_16x16x32_bf16 v[16:19], v[224:227], v[192:195], v[16:19]\n" \
    "s_barrier\n" \
    "v_mfma_f32_16x16x32_bf16 v[20:23], v[228:231], v[192:195], v[20:23]\n" \
    "ds_read_b64_tr_b16 v[176:177], v242 offset:0\n" \
    "v_mfma_f32_16x16x32_bf16 v[24:27], v[232:235], v[192:195], v[24:27]\n" \
    "s_add_u32 m0, s74, s79\n" \
    "s_mul_i32 s72, s73, s70\n" \
    "v_mfma_f32_16x16x32_bf16 v[28:31], v[236:239], v[192:195], v[28:31]\n" \
    "ds_read_b64_tr_b16 v[178:179], v242 offset:2048\n" \
    "buffer_load_dwordx4 v14, s[44:47], s72 offen lds\n" \
    "v_mfma_f32_16x16x32_bf16 v[32:35], v[224:227], v[196:199], v[32:35]\n" \
    "ds_read_b64_tr_b16 v[180:181], v243 offset:0\n" \
    "v_mfma_f32_16x16x32_bf16 v[36:39], v[228:231], v[196:199], v[36:39]\n" \
    "ds_read_b64_tr_b16 v[182:183], v243 offset:2048\n" \
    "v_mfma_f32_16x16x32_bf16 v[40:43], v[232:235], v[196:199], v[40:43]\n" \
    "s_add_u32 m0, m0, 1024\n" \
    "s_add_u32 s72, s72, s68\n" \
    "v_mfma_f32_16x16x32_bf16 v[44:47], v[236:239], v[196:199], v[44:47]\n" \
    "ds_read_b64_tr_b16 v[184:185], v244 offset:0\n" \
    "buffer_load_dwordx4 v14, s[44:47], s72 offen lds\n" \
    "v_mfma_f32_16x16x32_bf16 v[48:51], v[224:227], v[200:203], v[48:51]\n" \
    "ds_read_b64_tr_b16 v[186:187], v244 offset:2048\n" \
    "v_mfma_f32_16x16x32_bf16 v[52:55], v[228:231], v[200:203], v[52:55]\n" \
    "ds_read_b64_tr_b16 v[188:189], v245 offset:0\n" \
    "v_mfma_f32_16x16x32_bf16 v[56:59], v[232:235], v[200:203], v[56:59]\n" \
    "ds_read_b64_tr_b16 v[190:191], v245 offset:2048\n" \
    "v_mfma_f32_16x16x32_bf16 v[60:63], v[236:239], v[200:203], v[60:63]\n" \
    "s_add_u32 m0, m0, 1024\n" \
    "s_sub_u32 s72, s72, s69\n" \
    "v_mfma_f32_16x16x32_bf16 v[64:67], v[224:227], v[204:207], v[64:67]\n" \
    "ds_read_b128 v[144:147], v240 offset:0\n" \
    "buffer_load_dwordx4 v14, s[44:47], s72 offen lds\n" \
    "v_mfma_f32_16x16x32_bf16 v[68:71], v[228:231], v[204:207], v[68:71]\n" \
    "ds_read_b128 v[148:151], v240 offset:2048\n" \
    "v_mfma_f32_16x16x32_bf16 v[72:75], v[232:235], v[204:207], v[72:75]\n" \
    "ds_read_b128 v[152:155], v240 offset:4096\n" \
    "v_mfma_f32_16x16x32_bf16 v[76:79], v[236:239], v[204:207], v[76:79]\n" \
    "ds_read_b128 v[156:159], v240 offset:6144\n" \
    "v_mfma_f32_16x16x32_bf16 v[80:83], v[224:227], v[208:211], v[80:83]\n" \
    "s_add_u32 m0, m0, 1024\n" \
    "s_add_u32 s72, s72, s68\n" \
    "v_mfma_f32_16x16x32_bf16 v[84:87], v[228:231], v[208:211], v[84:87]\n" \
    "ds_read_b128 v[160:163], v240 offset:8192\n" \
    "buffer_load_dwordx4 v14, s[44:47], s72 offen lds\n" \
    "v_mfma_f32_16x16x32_bf16 v[88:91], v[232:235], v[208:211], v[88:91]\n" \
    "ds_read_b128 v[164:167], v240 offset:10240\n" \
    "v_mfma_f32_16x16x32_bf16 v[92:95], v[236:239], v[208:211], v[92:95]\n" \
    "ds_read_b128 v[168:171], v240 offset:12288\n" \
    "v_mfma_f32_16x16x32_bf16 v[96:99], v[224:227], v[212:215], v[96:99]\n" \
    "ds_read_b128 v[172:175], v240 offset:14336\n" \
    "v_mfma_f32_16x16x32_bf16 v[100:103], v[228:231], v[212:215], v[100:103]\n" \
    "s_add_u32 m0, s74, s78\n" \
    "s_mov_b32 s71, s73\n" \
    "v_mfma_f32_16x16x32_bf16 v[104:107], v[232:235], v[212:215], v[104:107]\n" \
    "buffer_load_dwordx4 v246, s[40:43], s71 offen sc1 lds\n" \
    "s_add_u32 s73, s73, 128\n" \
    "v_mfma_f32_16x16x32_bf16 v[108:111], v[236:239], v[212:215], v[108:111]\n" \
    "buffer_store_dwordx4 a[192:195], v15, s[48:51], s66 offen sc1\n" \
    "s_add_u32 s80, s66, s65\n" \
    "v_mfma_f32_16x16x32_bf16 v[112:115], v[224:227], v[216:219], v[112:115]\n" \
    "v_mfma_f32_16x16x32_bf16 v[116:119], v[228:231], v[216:219], v[116:119]\n" \
    "v_mfma_f32_16x16x32_bf16 v[120:123], v[232:235], v[216:219], v[120:123]\n" \
    "s_add_u32 m0, m0, 4096\n" \
    "s_add_u32 s71, s71, 65536\n" \
    "v_mfma_f32_16x16x32_bf16 v[124:127], v[236:239], v[216:219], v[124:127]\n" \
    "buffer_load_dwordx4 v246, s[40:43], s71 offen sc1 lds\n" \
    "s_and_b32 s73, s73, 2047\n" \
    "v_mfma_f32_16x16x32_bf16 v[128:131], v[224:227], v[220:223], v[128:131]\n" \
    "buffer_store_dwordx4 a[196:199], v15, s[48:51], s80 offen sc1\n" \
    "s_add_u32 s80, s66, s64\n" \
    "v_mfma_f32_16x16x32_bf16 v[132:135], v[228:231], v[220:223], v[132:135]\n" \
    "s_add_u32 s74, s74, s76\n" \
    "s_sub_u32 s75, s75, 1\n" \
    "v_mfma_f32_16x16x32_bf16 v[136:139], v[232:235], v[220:223], v[136:139]\n" \
    "v_mfma_f32_16x16x32_bf16 v[140:143], v[236:239], v[220:223], v[140:143]\n" \
    "s_cmp_lg_u32 s75, 0\n" \
    "s_waitcnt lgkmcnt(0)\n" \
    "v_mfma_f32_16x16x32_bf16 v[16:19], v[176:179], v[144:147], v[16:19]\n" \
    "ds_read_b64_tr_b16 v[224:225], v242 offset:4096\n" \
    "v_mfma_f32_16x16x32_bf16 v[20:23], v[180:183], v[144:147], v[20:23]\n" \
    "s_add_u32 m0, m0, 4096\n" \
    "s_add_u32 s71, s71, 65536\n" \
    "v_mfma_f32_16x16x32_bf16 v[24:27], v[184:187], v[144:147], v[24:27]\n" \
    "ds_read_b64_tr_b16 v[226:227], v242 offset:6144\n" \
    "buffer_load_dwordx4 v246, s[40:43], s71 offen sc1 lds\n" \
    "v_mfma_f32_16x16x32_bf16 v[28:31], v[188:191], v[144:147], v[28:31]\n" \
    "ds_read_b64_tr_b16 v[228:229], v243 offset:4096\n" \
    "v_mfma_f32_16x16x32_bf16 v[32:35], v[176:179], v[148:151], v[32:35]\n" \
    "ds_read_b64_tr_b16 v[230:231], v243 offset:6144\n" \
    "v_mfma_f32_16x16x32_bf16 v[36:39], v[180:183], v[148:151], v[36:39]\n" \
    "ds_read_b64_tr_b16 v[232:233], v244 offset:4096\n" \
    "v_mfma_f32_16x16x32_bf16 v[40:43], v[184:187], v[148:151], v[40:43]\n" \
    "s_add_u32 m0, m0, 4096\n" \
    "s_add_u32 s71, s71, 65536\n" \
    "v_mfma_f32_16x16x32_bf16 v[44:47], v[188:191], v[148:151], v[44:47]\n" \
    "ds_read_b64_tr_b16 v[234:235], v244 offset:6144\n" \
    "buffer_load_dwordx4 v246, s[40:43], s71 offen sc1 lds\n" \
    "v_mfma_f32_16x16x32_bf16 v[48:51], v[176:179], v[152:155], v[48:51]\n" \
    "ds_read_b64_tr_b16 v[236:237], v245 offset:4096\n" \
    "v_mfma_f32_16x16x32_bf16 v[52:55], v[180:183], v[152:155], v[52:55]\n" \
    "ds_read_b64_tr_b16 v[238:239], v245 offset:6144\n" \
    "v_mfma_f32_16x16x32_bf16 v[56:59], v[184:187], v[152:155], v[56:59]\n" \
    "ds_read_b128 v[192:195], v241 offset:0\n" \
    "v_mfma_f32_16x16x32_bf16 v[60:63], v[188:191], v[152:155], v[60:63]\n" \
    "s_add_u32 m0, m0, 4096\n" \
    "s_add_u32 s71, s71, 65536\n" \
    "v_mfma_f32_16x16x32_bf16 v[64:67], v[176:179], v[156:159], v[64:67]\n" \
    "ds_read_b128 v[196:199], v241 offset:2048\n" \
    "buffer_load_dwordx4 v246, s[40:43], s71 offen sc1 lds\n" \
    "v_mfma_f32_16x16x32_bf16 v[68:71], v[180:183], v[156:159], v[68:71]\n" \
    "ds_read_b128 v[200:203], v241 offset:4096\n" \
    "v_mfma_f32_16x16x32_bf16 v[72:75], v[184:187], v[156:159], v[72:75]\n" \
    "ds_read_b128 v[204:207], v241 offset:6144\n" \
    "v_mfma_f32_16x16x32_bf16 v[76:79], v[188:191], v[156:159], v[76:79]\n" \
    "ds_read_b128 v[208:211], v241 offset:8192\n" \
    "v_mfma_f32_16x16x32_bf16 v[80:83], v[176:179], v[160:163], v[80:83]\n" \
    "s_add_u32 m0, m0, 4096\n" \
    "s_add_u32 s71, s71, 65536\n" \
    "v_mfma_f32_16x16x32_bf16 v[84:87], v[180:183], v[160:163], v[84:87]\n" \
    "ds_read_b128 v[212:215], v241 offset:10240\n" \
    "buffer_load_dwordx4 v246, s[40:43], s71 offen sc1 lds\n" \
    "v_mfma_f32_16x16x32_bf16 v[88:91], v[184:187], v[160:163], v[88:91]\n" \
    "ds_read_b128 v[216:219], v241 offset:12288\n" \
    "v_mfma_f32_16x16x32_bf16 v[92:95], v[188:191], v[160:163], v[92:95]\n" \
    "ds_read_b128 v[220:223], v241 offset:14336\n" \
    "v_mfma_f32_16x16x32_bf16 v[96:99], v[176:179], v[164:167], v[96:99]\n" \
    "s_cmp_eq_u32 s74, 98304\n" \
    "s_cselect_b32 s76, s77, 49152\n" \
    "v_mfma_f32_16x16x32_bf16 v[100:103], v[180:183], v[164:167], v[100:103]\n" \
    "s_add_u32 m0, m0, 4096\n" \
    "s_add_u32 s71, s71, 65536\n" \
    "v_mfma_f32_16x16x32_bf16 v[104:107], v[184:187], v[164:167], v[104:107]\n" \
    "buffer_load_dwordx4 v246, s[40:43], s71 offen sc1 lds\n" \
    "v_mfma_f32_16x16x32_bf16 v[108:111], v[188:191], v[164:167], v[108:111]\n" \
    "v_add_u32 v242, s76, v242\n" \
    "v_mfma_f32_16x16x32_bf16 v[112:115], v[176:179], v[168:171], v[112:115]\n" \
    "v_add_u32 v243, s76, v243\n" \
    "v_mfma_f32_16x16x32_bf16 v[116:119], v[180:183], v[168:171], v[116:119]\n" \
    "v_add_u32 v244, s76, v244\n" \
    "v_mfma_f32_16x16x32_bf16 v[120:123], v[184:187], v[168:171], v[120:123]\n" \
    "s_add_u32 m0, m0, 4096\n" \
    "s_add_u32 s71, s71, 65536\n" \
    "v_mfma_f32_16x16x32_bf16 v[124:127], v[188:191], v[168:171], v[124:127]\n" \
    "buffer_load_dwordx4 v246, s[40:43], s71 offen sc1 lds\n" \
    "v_mfma_f32_16x16x32_bf16 v[128:131], v[176:179], v[172:175], v[128:131]\n" \
    "v_add_u32 v245, s76, v245\n" \
    "v_mfma_f32_16x16x32_bf16 v[132:135], v[180:183], v[172:175], v[132:135]\n" \
    "v_add_u32 v240, s76, v240\n" \
    "v_mfma_f32_16x16x32_bf16 v[136:139], v[184:187], v[172:175], v[136:139]\n" \
    "v_add_u32 v241, s76, v241\n" \
    "v_mfma_f32_16x16x32_bf16 v[140:143], v[188:191], v[172:175], v[140:143]\n" \
    "s_waitcnt vmcnt(14) lgkmcnt(0)\n" \
    "v_mfma_f32_16x16x32_bf16 v[16:19], v[224:227], v[192:195], v[16:19]\n" \
    "s_barrier\n" \
    "v_mfma_f32_16x16x32_bf16 v[20:23], v[228:231], v[192:195], v[20:23]\n" \
    "ds_read_b64_tr_b16 v[176:177], v242 offset:0\n" \
    "v_mfma_f32_16x16x32_bf16 v[24:27], v[232:235], v[192:195], v[24:27]\n" \
    "s_add_u32 m0, s74, s79\n" \
    "s_mul_i32 s72, s73, s70\n" \
    "v_mfma_f32_16x16x32_bf16 v[28:31], v[236:239], v[192:195], v[28:31]\n" \
    "ds_read_b64_tr_b16 v[178:179], v242 offset:2048\n" \
    "buffer_load_dwordx4 v14, s[44:47], s72 offen lds\n" \
    "v_mfma_f32_16x16x32_bf16 v[32:35], v[224:227], v[196:199], v[32:35]\n" \
    "ds_read_b64_tr_b16 v[180:181], v243 offset:0\n" \
    "v_mfma_f32_16x16x32_bf16 v[36:39], v[228:231], v[196:199], v[36:39]\n" \
    "ds_read_b64_tr_b16 v[182:183], v243 offset:2048\n" \
    "v_mfma_f32_16x16x32_bf16 v[40:43], v[232:235], v[196:199], v[40:43]\n" \
    "s_add_u32 m0, m0, 1024\n" \
    "s_add_u32 s72, s72, s68\n" \
    "v_mfma_f32_16x16x32_bf16 v[44:47], v[236:239], v[196:199], v[44:47]\n" \
    "ds_read_b64_tr_b16 v[184:185], v244 offset:0\n" \
    "buffer_load_dwordx4 v14, s[44:47], s72 offen lds\n" \
    "v_mfma_f32_16x16x32_bf16 v[48:51], v[224:227], v[200:203], v[48:51]\n" \
    "ds_read_b64_tr_b16 v[186:187], v244 offset:2048\n" \
    "v_mfma_f32_16x16x32_bf16 v[52:55], v[228:231], v[200:203], v[52:55]\n" \
    "ds_read_b64_tr_b16 v[188:189], v245 offset:0\n" \
    "v_mfma_f32_16x16x32_bf16 v[56:59], v[232:235], v[200:203], v[56:59]\n" \
    "ds_read_b64_tr_b16 v[190:191], v245 offset:2048\n" \
    "v_mfma_f32_16x16x32_bf16 v[60:63], v[236:239], v[200:203], v[60:63]\n" \
    "s_add_u32 m0, m0, 1024\n" \
    "s_sub_u32 s72, s72, s69\n" \
    "v_mfma_f32_16x16x32_bf16 v[64:67], v[224:227], v[204:207], v[64:67]\n" \
    "ds_read_b128 v[144:147], v240 offset:0\n" \
    "buffer_load_dwordx4 v14, s[44:47], s72 offen lds\n" \
    "v_mfma_f32_16x16x32_bf16 v[68:71], v[228:231], v[204:207], v[68:71]\n" \
    "ds_read_b128 v[148:151], v240 offset:2048\n" \
    "v_mfma_f32_16x16x32_bf16 v[72:75], v[232:235], v[204:207], v[72:75]\n" \
    "ds_read_b128 v[152:155], v240 offset:4096\n" \
    "v_mfma_f32_16x16x32_bf16 v[76:79], v[236:239], v[204:207], v[76:79]\n" \
    "ds_read_b128 v[156:159], v240 offset:6144\n" \
    "v_mfma_f32_16x16x32_bf16 v[80:83], v[224:227], v[208:211], v[80:83]\n" \
    "s_add_u32 m0, m0, 1024\n" \
    "s_add_u32 s72, s72, s68\n" \
    "v_mfma_f32_16x16x32_bf16 v[84:87], v[228:231], v[208:211], v[84:87]\n" \
    "ds_read_b128 v[160:163], v240 offset:8192\n" \
    "buffer_load_dwordx4 v14, s[44:47], s72 offen lds\n" \
    "v_mfma_f32_16x16x32_bf16 v[88:91], v[232:235], v[208:211], v[88:91]\n" \
    "ds_read_b128 v[164:167], v240 offset:10240\n" \
    "v_mfma_f32_16x16x32_bf16 v[92:95], v[236:239], v[208:211], v[92:95]\n" \
    "ds_read_b128 v[168:171], v240 offset:12288\n" \
    "v_mfma_f32_16x16x32_bf16 v[96:99], v[224:227], v[212:215], v[96:99]\n" \
    "ds_read_b128 v[172:175], v240 offset:14336\n" \
    "v_mfma_f32_16x16x32_bf16 v[100:103], v[228:231], v[212:215], v[100:103]\n" \
    "s_add_u32 m0, s74, s78\n" \
    "s_mov_b32 s71, s73\n" \
    "v_mfma_f32_16x16x32_bf16 v[104:107], v[232:235], v[212:215], v[104:107]\n" \
    "buffer_load_dwordx4 v246, s[40:43], s71 offen sc1 lds\n" \
    "s_add_u32 s73, s73, 128\n" \
    "v_mfma_f32_16x16x32_bf16 v[108:111], v[236:239], v[212:215], v[108:111]\n" \
    "buffer_store_dwordx4 a[200:203], v15, s[48:51], s80 offen sc1\n" \
    "s_add_u32 s80, s80, s65\n" \
    "v_mfma_f32_16x16x32_bf16 v[112:115], v[224:227], v[216:219], v[112:115]\n" \
    "v_mfma_f32_16x16x32_bf16 v[116:119], v[228:231], v[216:219], v[116:119]\n" \
    "v_mfma_f32_16x16x32_bf16 v[120:123], v[232:235], v[216:219], v[120:123]\n" \
    "s_add_u32 m0, m0, 4096\n" \
    "s_add_u32 s71, s71, 65536\n" \
    "v_mfma_f32_16x16x32_bf16 v[124:127], v[236:239], v[216:219], v[124:127]\n" \
    "buffer_load_dwordx4 v246, s[40:43], s71 offen sc1 lds\n" \
    "s_and_b32 s73, s73, 2047\n" \
    "v_mfma_f32_16x16x32_bf16 v[128:131], v[224:227], v[220:223], v[128:131]\n" \
    "buffer_store_dwordx4 a[204:207], v15, s[48:51], s80 offen sc1\n" \
    "s_add_u32 s66, s66, s63\n" \
    "v_mfma_f32_16x16x32_bf16 v[132:135], v[228:231], v[220:223], v[132:135]\n" \
    "s_add_u32 s74, s74, s76\n" \
    "s_sub_u32 s75, s75, 1\n" \
    "v_mfma_f32_16x16x32_bf16 v[136:139], v[232:235], v[220:223], v[136:139]\n" \
    "v_mfma_f32_16x16x32_bf16 v[140:143], v[236:239], v[220:223], v[140:143]\n" \
    "s_cmp_lg_u32 s75, 0\n" \
    "s_waitcnt lgkmcnt(0)\n" \
    "v_mfma_f32_16x16x32_bf16 v[16:19], v[176:179], v[144:147], v[16:19]\n" \
    "ds_read_b64_tr_b16 v[224:225], v242 offset:4096\n" \
    "v_mfma_f32_16x16x32_bf16 v[20:23], v[180:183], v[144:147], v[20:23]\n" \
    "s_add_u32 m0, m0, 4096\n" \
    "s_add_u32 s71, s71, 65536\n" \
    "v_mfma_f32_16x16x32_bf16 v[24:27], v[184:187], v[144:147], v[24:27]\n" \
    "ds_read_b64_tr_b16 v[226:227], v242 offset:6144\n" \
    "buffer_load_dwordx4 v246, s[40:43], s71 offen sc1 lds\n" \
    "v_mfma_f32_16x16x32_bf16 v[28:31], v[188:191], v[144:147], v[28:31]\n" \
    "ds_read_b64_tr_b16 v[228:229], v243 offset:4096\n" \
    "v_mfma_f32_16x16x32_bf16 v[32:35], v[176:179], v[148:151], v[32:35]\n" \
    "ds_read_b64_tr_b16 v[230:231], v243 offset:6144\n" \
    "v_mfma_f32_16x16x32_bf16 v[36:39], v[180:183], v[148:151], v[36:39]\n" \
    "ds_read_b64_tr_b16 v[232:233], v244 offset:4096\n" \
    "v_mfma_f32_16x16x32_bf16 v[40:43], v[184:187], v[148:151], v[40:43]\n" \
    "s_add_u32 m0, m0, 4096\n" \
    "s_add_u32 s71, s71, 65536\n" \
    "v_mfma_f32_16x16x32_bf16 v[44:47], v[188:191], v[148:151], v[44:47]\n" \
    "ds_read_b64_tr_b16 v[234:235], v244 offset:6144\n" \
    "buffer_load_dwordx4 v246, s[40:43], s71 offen sc1 lds\n" \
    "v_mfma_f32_16x16x32_bf16 v[48:51], v[176:179], v[152:155], v[48:51]\n" \
    "ds_read_b64_tr_b16 v[236:237], v245 offset:4096\n" \
    "v_mfma_f32_16x16x32_bf16 v[52:55], v[180:183], v[152:155], v[52:55]\n" \
    "ds_read_b64_tr_b16 v[238:239], v245 offset:6144\n" \
    "v_mfma_f32_16x16x32_bf16 v[56:59], v[184:187], v[152:155], v[56:59]\n" \
    "ds_read_b128 v[192:195], v241 offset:0\n" \
    "v_mfma_f32_16x16x32_bf16 v[60:63], v[188:191], v[152:155], v[60:63]\n" \
    "s_add_u32 m0, m0, 4096\n" \
    "s_add_u32 s71, s71, 65536\n" \
    "v_mfma_f32_16x16x32_bf16 v[64:67], v[176:179], v[156:159], v[64:67]\n" \
    "ds_read_b128 v[196:199], v241 offset:2048\n" \
    "buffer_load_dwordx4 v246, s[40:43], s71 offen sc1 lds\n" \
    "v_mfma_f32_16x16x32_bf16 v[68:71], v[180:183], v[156:159], v[68:71]\n" \
    "ds_read_b128 v[200:203], v241 offset:4096\n" \
    "v_mfma_f32_16x16x32_bf16 v[72:75], v[184:187], v[156:159], v[72:75]\n" \
    "ds_read_b128 v[204:207], v241 offset:6144\n" \
    "v_mfma_f32_16x16x32_bf16 v[76:79], v[188:191], v[156:159], v[76:79]\n" \
    "ds_read_b128 v[208:211], v241 offset:8192\n" \
    "v_mfma_f32_16x16x32_bf16 v[80:83], v[176:179], v[160:163], v[80:83]\n" \
    "s_add_u32 m0, m0, 4096\n" \
    "s_add_u32 s71, s71, 65536\n" \
    "v_mfma_f32_16x16x32_bf16 v[84:87], v[180:183], v[160:163], v[84:87]\n" \
    "ds_read_b128 v[212:215], v241 offset:10240\n" \
    "buffer_load_dwordx4 v246, s[40:43], s71 offen sc1 lds\n" \
    "v_mfma_f32_16x16x32_bf16 v[88:91], v[184:187], v[160:163], v[88:91]\n" \
    "ds_read_b128 v[216:219], v241 offset:12288\n" \
    "v_mfma_f32_16x16x32_bf16 v[92:95], v[188:191], v[160:163], v[92:95]\n" \
    "ds_read_b128 v[220:223], v241 offset:14336\n" \
    "v_mfma_f32_16x16x32_bf16 v[96:99], v[176:179], v[164:167], v[96:99]\n" \
    "s_cmp_eq_u32 s74, 98304\n" \
    "s_cselect_b32 s76, s77, 49152\n" \
    "v_mfma_f32_16x16x32_bf16 v[100:103], v[180:183], v[164:167], v[100:103]\n" \
    "s_add_u32 m0, m0, 4096\n" \
    "s_add_u32 s71, s71, 65536\n" \
    "v_mfma_f32_16x16x32_bf16 v[104:107], v[184:187], v[164:167], v[104:107]\n" \
    "buffer_load_dwordx4 v246, s[40:43], s71 offen sc1 lds\n" \
    "v_mfma_f32_16x16x32_bf16 v[108:111], v[188:191], v[164:167], v[108:111]\n" \
    "v_add_u32 v242, s76, v242\n" \
    "v_mfma_f32_16x16x32_bf16 v[112:115], v[176:179], v[168:171], v[112:115]\n" \
    "v_add_u32 v243, s76, v243\n" \
    "v_mfma_f32_16x16x32_bf16 v[116:119], v[180:183], v[168:171], v[116:119]\n" \
    "v_add_u32 v244, s76, v244\n" \
    "v_mfma_f32_16x16x32_bf16 v[120:123], v[184:187], v[168:171], v[120:123]\n" \
    "s_add_u32 m0, m0, 4096\n" \
    "s_add_u32 s71, s71, 65536\n" \
    "v_mfma_f32_16x16x32_bf16 v[124:127], v[188:191], v[168:171], v[124:127]\n" \
    "buffer_load_dwordx4 v246, s[40:43], s71 offen sc1 lds\n" \
    "v_mfma_f32_16x16x32_bf16 v[128:131], v[176:179], v[172:175], v[128:131]\n" \
    "v_add_u32 v245, s76, v245\n" \
    "v_mfma_f32_16x16x32_bf16 v[132:135], v[180:183], v[172:175], v[132:135]\n" \
    "v_add_u32 v240, s76, v240\n" \
    "v_mfma_f32_16x16x32_bf16 v[136:139], v[184:187], v[172:175], v[136:139]\n" \
    "v_add_u32 v241, s76, v241\n" \
    "v_mfma_f32_16x16x32_bf16 v[140:143], v[188:191], v[172:175], v[140:143]\n" \
    "s_waitcnt vmcnt(14) lgkmcnt(0)\n" \
    "v_mfma_f32_16x16x32_bf16 v[16:19], v[224:227], v[192:195], v[16:19]\n" \
    "s_barrier\n" \
    "v_mfma_f32_16x16x32_bf16 v[20:23], v[228:231], v[192:195], v[20:23]\n" \
    "ds_read_b64_tr_b16 v[176:177], v242 offset:0\n" \
    "v_mfma_f32_16x16x32_bf16 v[24:27], v[232:235], v[192:195], v[24:27]\n" \
    "s_add_u32 m0, s74, s79\n" \
    "s_mul_i32 s72, s73, s70\n" \
    "v_mfma_f32_16x16x32_bf16 v[28:31], v[236:239], v[192:195], v[28:31]\n" \
    "ds_read_b64_tr_b16 v[178:179], v242 offset:2048\n" \
    "buffer_load_dwordx4 v14, s[44:47], s72 offen lds\n" \
    "v_mfma_f32_16x16x32_bf16 v[32:35], v[224:227], v[196:199], v[32:35]\n" \
    "ds_read_b64_tr_b16 v[180:181], v243 offset:0\n" \
    "v_mfma_f32_16x16x32_bf16 v[36:39], v[228:231], v[196:199], v[36:39]\n" \
    "ds_read_b64_tr_b16 v[182:183], v243 offset:2048\n" \
    "v_mfma_f32_16x16x32_bf16 v[40:43], v[232:235], v[196:199], v[40:43]\n" \
    "s_add_u32 m0, m0, 1024\n" \
    "s_add_u32 s72, s72, s68\n" \
    "v_mfma_f32_16x16x32_bf16 v[44:47], v[236:239], v[196:199], v[44:47]\n" \
    "ds_read_b64_tr_b16 v[184:185], v244 offset:0\n" \
    "buffer_load_dwordx4 v14, s[44:47], s72 offen lds\n" \
    "v_mfma_f32_16x16x32_bf16 v[48:51], v[224:227], v[200:203], v[48:51]\n" \
    "ds_read_b64_tr_b16 v[186:187], v244 offset:2048\n" \
    "v_mfma_f32_16x16x32_bf16 v[52:55], v[228:231], v[200:203], v[52:55]\n" \
    "ds_read_b64_tr_b16 v[188:189], v245 offset:0\n" \
    "v_mfma_f32_16x16x32_bf16 v[56:59], v[232:235], v[200:203], v[56:59]\n" \
    "ds_read_b64_tr_b16 v[190:191], v245 offset:2048\n" \
    "v_mfma_f32_16x16x32_bf16 v[60:63], v[236:239], v[200:203], v[60:63]\n" \
    "s_add_u32 m0, m0, 1024\n" \
    "s_sub_u32 s72, s72, s69\n" \
    "v_mfma_f32_16x16x32_bf16 v[64:67], v[224:227], v[204:207], v[64:67]\n" \
    "ds_read_b128 v[144:147], v240 offset:0\n" \
    "buffer_load_dwordx4 v14, s[44:47], s72 offen lds\n" \
    "v_mfma_f32_16x16x32_bf16 v[68:71], v[228:231], v[204:207], v[68:71]\n" \
    "ds_read_b128 v[148:151], v240 offset:2048\n" \
    "v_mfma_f32_16x16x32_bf16 v[72:75], v[232:235], v[204:207], v[72:75]\n" \
    "ds_read_b128 v[152:155], v240 offset:4096\n" \
    "v_mfma_f32_16x16x32_bf16 v[76:79], v[236:239], v[204:207], v[76:79]\n" \
    "ds_read_b128 v[156:159], v240 offset:6144\n" \
    "v_mfma_f32_16x16x32_bf16 v[80:83], v[224:227], v[208:211], v[80:83]\n" \
    "s_add_u32 m0, m0, 1024\n" \
    "s_add_u32 s72, s72, s68\n" \
    "v_mfma_f32_16x16x32_bf16 v[84:87], v[228:231], v[208:211], v[84:87]\n" \
    "ds_read_b128 v[160:163], v240 offset:8192\n" \
    "buffer_load_dwordx4 v14, s[44:47], s72 offen lds\n" \
    "v_mfma_f32_16x16x32_bf16 v[88:91], v[232:235], v[208:211], v[88:91]\n" \
    "ds_read_b128 v[164:167], v240 offset:10240\n" \
    "v_mfma_f32_16x16x32_bf16 v[92:95], v[236:239], v[208:211], v[92:95]\n" \
    "ds_read_b128 v[168:171], v240 offset:12288\n" \
    "v_mfma_f32_16x16x32_bf16 v[96:99], v[224:227], v[212:215], v[96:99]\n" \
    "ds_read_b128 v[172:175], v240 offset:14336\n" \
    "v_mfma_f32_16x16x32_bf16 v[100:103], v[228:231], v[212:215], v[100:103]\n" \
    "s_add_u32 m0, s74, s78\n" \
    "s_mov_b32 s71, s73\n" \
    "v_mfma_f32_16x16x32_bf16 v[104:107], v[232:235], v[212:215], v[104:107]\n" \
    "buffer_load_dwordx4 v246, s[40:43], s71 offen sc1 lds\n" \
    "s_add_u32 s73, s73, 128\n" \
    "v_mfma_f32_16x16x32_bf16 v[108:111], v[236:239], v[212:215], v[108:111]\n" \
    "buffer_store_dwordx4 a[208:211], v15, s[48:51], s66 offen sc1\n" \
    "s_add_u32 s80, s66, s65\n" \
    "v_mfma_f32_16x16x32_bf16 v[112:115], v[224:227], v[216:219], v[112:115]\n" \
    "v_mfma_f32_16x16x32_bf16 v[116:119], v[228:231], v[216:219], v[116:119]\n" \
    "v_mfma_f32_16x16x32_bf16 v[120:123], v[232:235], v[216:219], v[120:123]\n" \
    "s_add_u32 m0, m0, 4096\n" \
    "s_add_u32 s71, s71, 65536\n" \
    "v_mfma_f32_16x16x32_bf16 v[124:127], v[236:239], v[216:219], v[124:127]\n" \
    "buffer_load_dwordx4 v246, s[40:43], s71 offen sc1 lds\n" \
    "s_and_b32 s73, s73, 2047\n" \
    "v_mfma_f32_16x16x32_bf16 v[128:131], v[224:227], v[220:223], v[128:131]\n" \
    "buffer_store_dwordx4 a[212:215], v15, s[48:51], s80 offen sc1\n" \
    "s_add_u32 s80, s66, s64\n" \
    "v_mfma_f32_16x16x32_bf16 v[132:135], v[228:231], v[220:223], v[132:135]\n" \
    "s_add_u32 s74, s74, s76\n" \
    "s_sub_u32 s75, s75, 1\n" \
    "v_mfma_f32_16x16x32_bf16 v[136:139], v[232:235], v[220:223], v[136:139]\n" \
    "v_mfma_f32_16x16x32_bf16 v[140:143], v[236:239], v[220:223], v[140:143]\n" \
    "s_cmp_lg_u32 s75, 0\n" \
    "s_waitcnt lgkmcnt(0)\n" \
    "v_mfma_f32_16x16x32_bf16 v[16:19], v[176:179], v[144:147], v[16:19]\n" \
    "ds_read_b64_tr_b16 v[224:225], v242 offset:4096\n" \
    "v_mfma_f32_16x16x32_bf16 v[20:23], v[180:183], v[144:147], v[20:23]\n" \
    "s_add_u32 m0, m0, 4096\n" \
    "s_add_u32 s71, s71, 65536\n" \
    "v_mfma_f32_16x16x32_bf16 v[24:27], v[184:187], v[144:147], v[24:27]\n" \
    "ds_read_b64_tr_b16 v[226:227], v242 offset:6144\n" \
    "buffer_load_dwordx4 v246, s[40:43], s71 offen sc1 lds\n" \
    "v_mfma_f32_16x16x32_bf16 v[28:31], v[188:191], v[144:147], v[28:31]\n" \
    "ds_read_b64_tr_b16 v[228:229], v243 offset:4096\n" \
    "v_mfma_f32_16x16x32_bf16 v[32:35], v[176:179], v[148:151], v[32:35]\n" \
    "ds_read_b64_tr_b16 v[230:231], v243 offset:6144\n" \
    "v_mfma_f32_16x16x32_bf16 v[36:39], v[180:183], v[148:151], v[36:39]\n" \
    "ds_read_b64_tr_b16 v[232:233], v244 offset:4096\n" \
    "v_mfma_f32_16x16x32_bf16 v[40:43], v[184:187], v[148:151], v[40:43]\n" \
    "s_add_u32 m0, m0, 4096\n" \
    "s_add_u32 s71, s71, 65536\n" \
    "v_mfma_f32_16x16x32_bf16 v[44:47], v[188:191], v[148:151], v[44:47]\n" \
    "ds_read_b64_tr_b16 v[234:235], v244 offset:6144\n" \
    "buffer_load_dwordx4 v246, s[40:43], s71 offen sc1 lds\n" \
    "v_mfma_f32_16x16x32_bf16 v[48:51], v[176:179], v[152:155], v[48:51]\n" \
    "ds_read_b64_tr_b16 v[236:237], v245 offset:4096\n" \
    "v_mfma_f32_16x16x32_bf16 v[52:55], v[180:183], v[152:155], v[52:55]\n" \
    "ds_read_b64_tr_b16 v[238:239], v245 offset:6144\n" \
    "v_mfma_f32_16x16x32_bf16 v[56:59], v[184:187], v[152:155], v[56:59]\n" \
    "ds_read_b128 v[192:195], v241 offset:0\n" \
    "v_mfma_f32_16x16x32_bf16 v[60:63], v[188:191], v[152:155], v[60:63]\n" \
    "s_add_u32 m0, m0, 4096\n" \
    "s_add_u32 s71, s71, 65536\n" \
    "v_mfma_f32_16x16x32_bf16 v[64:67], v[176:179], v[156:159], v[64:67]\n" \
    "ds_read_b128 v[196:199], v241 offset:2048\n" \
    "buffer_load_dwordx4 v246, s[40:43], s71 offen sc1 lds\n" \
    "v_mfma_f32_16x16x32_bf16 v[68:71], v[180:183], v[156:159], v[68:71]\n" \
    "ds_read_b128 v[200:203], v241 offset:4096\n" \
    "v_mfma_f32_16x16x32_bf16 v[72:75], v[184:187], v[156:159], v[72:75]\n" \
    "ds_read_b128 v[204:207], v241 offset:6144\n" \
    "v_mfma_f32_16x16x32_bf16 v[76:79], v[188:191], v[156:159], v[76:79]\n" \
    "ds_read_b128 v[208:211], v241 offset:8192\n" \
    "v_mfma_f32_16x16x32_bf16 v[80:83], v[176:179], v[160:163], v[80:83]\n" \
    "s_add_u32 m0, m0, 4096\n" \
    "s_add_u32 s71, s71, 65536\n" \
    "v_mfma_f32_16x16x32_bf16 v[84:87], v[180:183], v[160:163], v[84:87]\n" \
    "ds_read_b128 v[212:215], v241 offset:10240\n" \
    "buffer_load_dwordx4 v246, s[40:43], s71 offen sc1 lds\n" \
    "v_mfma_f32_16x16x32_bf16 v[88:91], v[184:187], v[160:163], v[88:91]\n" \
    "ds_read_b128 v[216:219], v241 offset:12288\n" \
    "v_mfma_f32_16x16x32_bf16 v[92:95], v[188:191], v[160:163], v[92:95]\n" \
    "ds_read_b128 v[220:223], v241 offset:14336\n" \
    "v_mfma_f32_16x16x32_bf16 v[96:99], v[176:179], v[164:167], v[96:99]\n" \
    "s_cmp_eq_u32 s74, 98304\n" \
    "s_cselect_b32 s76, s77, 49152\n" \
    "v_mfma_f32_16x16x32_bf16 v[100:103], v[180:183], v[164:167], v[100:103]\n" \
    "s_add_u32 m0, m0, 4096\n" \
    "s_add_u32 s71, s71, 65536\n" \
    "v_mfma_f32_16x16x32_bf16 v[104:107], v[184:187], v[164:167], v[104:107]\n" \
    "buffer_load_dwordx4 v246, s[40:43], s71 offen sc1 lds\n" \
    "v_mfma_f32_16x16x32_bf16 v[108:111], v[188:191], v[164:167], v[108:111]\n" \
    "v_add_u32 v242, s76, v242\n" \
    "v_mfma_f32_16x16x32_bf16 v[112:115], v[176:179], v[168:171], v[112:115]\n" \
    "v_add_u32 v243, s76, v243\n" \
    "v_mfma_f32_16x16x32_bf16 v[116:119], v[180:183], v[168:171], v[116:119]\n" \
    "v_add_u32 v244, s76, v244\n" \
    "v_mfma_f32_16x16x32_bf16 v[120:123], v[184:187], v[168:171], v[120:123]\n" \
    "s_add_u32 m0, m0, 4096\n" \
    "s_add_u32 s71, s71, 65536\n" \
    "v_mfma_f32_16x16x32_bf16 v[124:127], v[188:191], v[168:171], v[124:127]\n" \
    "buffer_load_dwordx4 v246, s[40:43], s71 offen sc1 lds\n" \
    "v_mfma_f32_16x16x32_bf16 v[128:131], v[176:179], v[172:175], v[128:131]\n" \
    "v_add_u32 v245, s76, v245\n" \
    "v_mfma_f32_16x16x32_bf16 v[132:135], v[180:183], v[172:175], v[132:135]\n" \
    "v_add_u32 v240, s76, v240\n" \
    "v_mfma_f32_16x16x32_bf16 v[136:139], v[184:187], v[172:175], v[136:139]\n" \
    "v_add_u32 v241, s76, v241\n" \
    "v_mfma_f32_16x16x32_bf16 v[140:143], v[188:191], v[172:175], v[140:143]\n" \
    "s_waitcnt vmcnt(14) lgkmcnt(0)\n" \
    "v_mfma_f32_16x16x32_bf16 v[16:19], v[224:227], v[192:195], v[16:19]\n" \
    "s_barrier\n" \
    "v_mfma_f32_16x16x32_bf16 v[20:23], v[228:231], v[192:195], v[20:23]\n" \
    "ds_read_b64_tr_b16 v[176:177], v242 offset:0\n" \
    "v_mfma_f32_16x16x32_bf16 v[24:27], v[232:235], v[192:195], v[24:27]\n" \
    "s_add_u32 m0, s74, s79\n" \
    "s_mul_i32 s72, s73, s70\n" \
    "v_mfma_f32_16x16x32_bf16 v[28:31], v[236:239], v[192:195], v[28:31]\n" \
    "ds_read_b64_tr_b16 v[178:179], v242 offset:2048\n" \
    "buffer_load_dwordx4 v14, s[44:47], s72 offen lds\n" \
    "v_mfma_f32_16x16x32_bf16 v[32:35], v[224:227], v[196:199], v[32:35]\n" \
    "ds_read_b64_tr_b16 v[180:181], v243 offset:0\n" \
    "v_mfma_f32_16x16x32_bf16 v[36:39], v[228:231], v[196:199], v[36:39]\n" \
    "ds_read_b64_tr_b16 v[182:183], v243 offset:2048\n" \
    "v_mfma_f32_16x16x32_bf16 v[40:43], v[232:235], v[196:199], v[40:43]\n" \
    "s_add_u32 m0, m0, 1024\n" \
    "s_add_u32 s72, s72, s68\n" \
    "v_mfma_f32_16x16x32_bf16 v[44:47], v[236:239], v[196:199], v[44:47]\n" \
    "ds_read_b64_tr_b16 v[184:185], v244 offset:0\n" \
    "buffer_load_dwordx4 v14, s[44:47], s72 offen lds\n" \
    "v_mfma_f32_16x16x32_bf16 v[48:51], v[224:227], v[200:203], v[48:51]\n" \
    "ds_read_b64_tr_b16 v[186:187], v244 offset:2048\n" \
    "v_mfma_f32_16x16x32_bf16 v[52:55], v[228:231], v[200:203], v[52:55]\n" \
    "ds_read_b64_tr_b16 v[188:189], v245 offset:0\n" \
    "v_mfma_f32_16x16x32_bf16 v[56:59], v[232:235], v[200:203], v[56:59]\n" \
    "ds_read_b64_tr_b16 v[190:191], v245 offset:2048\n" \
    "v_mfma_f32_16x16x32_bf16 v[60:63], v[236:239], v[200:203], v[60:63]\n" \
    "s_add_u32 m0, m0, 1024\n" \
    "s_sub_u32 s72, s72, s69\n" \
    "v_mfma_f32_16x16x32_bf16 v[64:67], v[224:227], v[204:207], v[64:67]\n" \
    "ds_read_b128 v[144:147], v240 offset:0\n" \
    "buffer_load_dwordx4 v14, s[44:47], s72 offen lds\n" \
    "v_mfma_f32_16x16x32_bf16 v[68:71], v[228:231], v[204:207], v[68:71]\n" \
    "ds_read_b128 v[148:151], v240 offset:2048\n" \
    "v_mfma_f32_16x16x32_bf16 v[72:75], v[232:235], v[204:207], v[72:75]\n" \
    "ds_read_b128 v[152:155], v240 offset:4096\n" \
    "v_mfma_f32_16x16x32_bf16 v[76:79], v[236:239], v[204:207], v[76:79]\n" \
    "ds_read_b128 v[156:159], v240 offset:6144\n" \
    "v_mfma_f32_16x16x32_bf16 v[80:83], v[224:227], v[208:211], v[80:83]\n" \
    "s_add_u32 m0, m0, 1024\n" \
    "s_add_u32 s72, s72, s68\n" \
    "v_mfma_f32_16x16x32_bf16 v[84:87], v[228:231], v[208:211], v[84:87]\n" \
    "ds_read_b128 v[160:163], v240 offset:8192\n" \
    "buffer_load_dwordx4 v14, s[44:47], s72 offen lds\n" \
    "v_mfma_f32_16x16x32_bf16 v[88:91], v[232:235], v[208:211], v[88:91]\n" \
    "ds_read_b128 v[164:167], v240 offset:10240\n" \
    "v_mfma_f32_16x16x32_bf16 v[92:95], v[236:239], v[208:211], v[92:95]\n" \
    "ds_read_b128 v[168:171], v240 offset:12288\n" \
    "v_mfma_f32_16x16x32_bf16 v[96:99], v[224:227], v[212:215], v[96:99]\n" \
    "ds_read_b128 v[172:175], v240 offset:14336\n" \
    "v_mfma_f32_16x16x32_bf16 v[100:103], v[228:231], v[212:215], v[100:103]\n" \
    "s_add_u32 m0, s74, s78\n" \
    "s_mov_b32 s71, s73\n" \
    "v_mfma_f32_16x16x32_bf16 v[104:107], v[232:235], v[212:215], v[104:107]\n" \
    "buffer_load_dwordx4 v246, s[40:43], s71 offen sc1 lds\n" \
    "s_add_u32 s73, s73, 128\n" \
    "v_mfma_f32_16x16x32_bf16 v[108:111], v[236:239], v[212:215], v[108:111]\n" \
    "buffer_store_dwordx4 a[216:219], v15, s[48:51], s80 offen sc1\n" \
    "s_add_u32 s80, s80, s65\n" \
    "v_mfma_f32_16x16x32_bf16 v[112:115], v[224:227], v[216:219], v[112:115]\n" \
    "v_mfma_f32_16x16x32_bf16 v[116:119], v[228:231], v[216:219], v[116:119]\n" \
    "v_mfma_f32_16x16x32_bf16 v[120:123], v[232:235], v[216:219], v[120:123]\n" \
    "s_add_u32 m0, m0, 4096\n" \
    "s_add_u32 s71, s71, 65536\n" \
    "v_mfma_f32_16x16x32_bf16 v[124:127], v[236:239], v[216:219], v[124:127]\n" \
    "buffer_load_dwordx4 v246, s[40:43], s71 offen sc1 lds\n" \
    "s_and_b32 s73, s73, 2047\n" \
    "v_mfma_f32_16x16x32_bf16 v[128:131], v[224:227], v[220:223], v[128:131]\n" \
    "buffer_store_dwordx4 a[220:223], v15, s[48:51], s80 offen sc1\n" \
    "s_add_u32 s66, s66, s63\n" \
    "v_mfma_f32_16x16x32_bf16 v[132:135], v[228:231], v[220:223], v[132:135]\n" \
    "s_add_u32 s74, s74, s76\n" \
    "s_sub_u32 s75, s75, 1\n" \
    "v_mfma_f32_16x16x32_bf16 v[136:139], v[232:235], v[220:223], v[136:139]\n" \
    "v_mfma_f32_16x16x32_bf16 v[140:143], v[236:239], v[220:223], v[140:143]\n" \
    "s_cmp_lg_u32 s75, 0\n" \
    "s_waitcnt lgkmcnt(0)\n" \
    "v_mfma_f32_16x16x32_bf16 v[16:19], v[176:179], v[144:147], v[16:19]\n" \
    "ds_read_b64_tr_b16 v[224:225], v242 offset:4096\n" \
    "v_mfma_f32_16x16x32_bf16 v[20:23], v[180:183], v[144:147], v[20:23]\n" \
    "s_add_u32 m0, m0, 4096\n" \
    "s_add_u32 s71, s71, 65536\n" \
    "v_mfma_f32_16x16x32_bf16 v[24:27], v[184:187], v[144:147], v[24:27]\n" \
    "ds_read_b64_tr_b16 v[226:227], v242 offset:6144\n" \
    "buffer_load_dwordx4 v246, s[40:43], s71 offen sc1 lds\n" \
    "v_mfma_f32_16x16x32_bf16 v[28:31], v[188:191], v[144:147], v[28:31]\n" \
    "ds_read_b64_tr_b16 v[228:229], v243 offset:4096\n" \
    "v_mfma_f32_16x16x32_bf16 v[32:35], v[176:179], v[148:151], v[32:35]\n" \
    "ds_read_b64_tr_b16 v[230:231], v243 offset:6144\n" \
    "v_mfma_f32_16x16x32_bf16 v[36:39], v[180:183], v[148:151], v[36:39]\n" \
    "ds_read_b64_tr_b16 v[232:233], v244 offset:4096\n" \
    "v_mfma_f32_16x16x32_bf16 v[40:43], v[184:187], v[148:151], v[40:43]\n" \
    "s_add_u32 m0, m0, 4096\n" \
    "s_add_u32 s71, s71, 65536\n" \
    "v_mfma_f32_16x16x32_bf16 v[44:47], v[188:191], v[148:151], v[44:47]\n" \
    "ds_read_b64_tr_b16 v[234:235], v244 offset:6144\n" \
    "buffer_load_dwordx4 v246, s[40:43], s71 offen sc1 lds\n" \
    "v_mfma_f32_16x16x32_bf16 v[48:51], v[176:179], v[152:155], v[48:51]\n" \
    "ds_read_b64_tr_b16 v[236:237], v245 offset:4096\n" \
    "v_mfma_f32_16x16x32_bf16 v[52:55], v[180:183], v[152:155], v[52:55]\n" \
    "ds_read_b64_tr_b16 v[238:239], v245 offset:6144\n" \
    "v_mfma_f32_16x16x32_bf16 v[56:59], v[184:187], v[152:155], v[56:59]\n" \
    "ds_read_b128 v[192:195], v241 offset:0\n" \
    "v_mfma_f32_16x16x32_bf16 v[60:63], v[188:191], v[152:155], v[60:63]\n" \
    "s_add_u32 m0, m0, 4096\n" \
    "s_add_u32 s71, s71, 65536\n" \
    "v_mfma_f32_16x16x32_bf16 v[64:67], v[176:179], v[156:159], v[64:67]\n" \
    "ds_read_b128 v[196:199], v241 offset:2048\n" \
    "buffer_load_dwordx4 v246, s[40:43], s71 offen sc1 lds\n" \
    "v_mfma_f32_16x16x32_bf16 v[68:71], v[180:183], v[156:159], v[68:71]\n" \
    "ds_read_b128 v[200:203], v241 offset:4096\n" \
    "v_mfma_f32_16x16x32_bf16 v[72:75], v[184:187], v[156:159], v[72:75]\n" \
    "ds_read_b128 v[204:207], v241 offset:6144\n" \
    "v_mfma_f32_16x16x32_bf16 v[76:79], v[188:191], v[156:159], v[76:79]\n" \
    "ds_read_b128 v[208:211], v241 offset:8192\n" \
    "v_mfma_f32_16x16x32_bf16 v[80:83], v[176:179], v[160:163], v[80:83]\n" \
    "s_add_u32 m0, m0, 4096\n" \
    "s_add_u32 s71, s71, 65536\n" \
    "v_mfma_f32_16x16x32_bf16 v[84:87], v[180:183], v[160:163], v[84:87]\n" \
    "ds_read_b128 v[212:215], v241 offset:10240\n" \
    "buffer_load_dwordx4 v246, s[40:43], s71 offen sc1 lds\n" \
    "v_mfma_f32_16x16x32_bf16 v[88:91], v[184:187], v[160:163], v[88:91]\n" \
    "ds_read_b128 v[216:219], v241 offset:12288\n" \
    "v_mfma_f32_16x16x32_bf16 v[92:95], v[188:191], v[160:163], v[92:95]\n" \
    "ds_read_b128 v[220:223], v241 offset:14336\n" \
    "v_mfma_f32_16x16x32_bf16 v[96:99], v[176:179], v[164:167], v[96:99]\n" \
    "s_cmp_eq_u32 s74, 98304\n" \
    "s_cselect_b32 s76, s77, 49152\n" \
    "v_mfma_f32_16x16x32_bf16 v[100:103], v[180:183], v[164:167], v[100:103]\n" \
    "s_add_u32 m0, m0, 4096\n" \
    "s_add_u32 s71, s71, 65536\n" \
    "v_mfma_f32_16x16x32_bf16 v[104:107], v[184:187], v[164:167], v[104:107]\n" \
    "buffer_load_dwordx4 v246, s[40:43], s71 offen sc1 lds\n" \
    "v_mfma_f32_16x16x32_bf16 v[108:111], v[188:191], v[164:167], v[108:111]\n" \
    "v_add_u32 v242, s76, v242\n" \
    "v_mfma_f32_16x16x32_bf16 v[112:115], v[176:179], v[168:171], v[112:115]\n" \
    "v_add_u32 v243, s76, v243\n" \
    "v_mfma_f32_16x16x32_bf16 v[116:119], v[180:183], v[168:171], v[116:119]\n" \
    "v_add_u32 v244, s76, v244\n" \
    "v_mfma_f32_16x16x32_bf16 v[120:123], v[184:187], v[168:171], v[120:123]\n" \
    "s_add_u32 m0, m0, 4096\n" \
    "s_add_u32 s71, s71, 65536\n" \
    "v_mfma_f32_16x16x32_bf16 v[124:127], v[188:191], v[168:171], v[124:127]\n" \
    "buffer_load_dwordx4 v246, s[40:43], s71 offen sc1 lds\n" \
    "v_mfma_f32_16x16x32_bf16 v[128:131], v[176:179], v[172:175], v[128:131]\n" \
    "v_add_u32 v245, s76, v245\n" \
    "v_mfma_f32_16x16x32_bf16 v[132:135], v[180:183], v[172:175], v[132:135]\n" \
    "v_add_u32 v240, s76, v240\n" \
    "v_mfma_f32_16x16x32_bf16 v[136:139], v[184:187], v[172:175], v[136:139]\n" \
    "v_add_u32 v241, s76, v241\n" \
    "v_mfma_f32_16x16x32_bf16 v[140:143], v[188:191], v[172:175], v[140:143]\n" \
    "s_waitcnt vmcnt(14) lgkmcnt(0)\n" \
    "v_mfma_f32_16x16x32_bf16 v[16:19], v[224:227], v[192:195], v[16:19]\n" \
    "s_barrier\n" \
    "v_mfma_f32_16x16x32_bf16 v[20:23], v[228:231], v[192:195], v[20:23]\n" \
    "ds_read_b64_tr_b16 v[176:177], v242 offset:0\n" \
    "v_mfma_f32_16x16x32_bf16 v[24:27], v[232:235], v[192:195], v[24:27]\n" \
    "s_add_u32 m0, s74, s79\n" \
    "s_mul_i32 s72, s73, s70\n" \
    "v_mfma_f32_16x16x32_bf16 v[28:31], v[236:239], v[192:195], v[28:31]\n" \
    "ds_read_b64_tr_b16 v[178:179], v242 offset:2048\n" \
    "buffer_load_dwordx4 v14, s[44:47], s72 offen lds\n" \
    "v_mfma_f32_16x16x32_bf16 v[32:35], v[224:227], v[196:199], v[32:35]\n" \
    "ds_read_b64_tr_b16 v[180:181], v243 offset:0\n" \
    "v_mfma_f32_16x16x32_bf16 v[36:39], v[228:231], v[196:199], v[36:39]\n" \
    "ds_read_b64_tr_b16 v[182:183], v243 offset:2048\n" \
    "v_mfma_f32_16x16x32_bf16 v[40:43], v[232:235], v[196:199], v[40:43]\n" \
    "s_add_u32 m0, m0, 1024\n" \
    "s_add_u32 s72, s72, s68\n" \
    "v_mfma_f32_16x16x32_bf16 v[44:47], v[236:239], v[196:199], v[44:47]\n" \
    "ds_read_b64_tr_b16 v[184:185], v244 offset:0\n" \
    "buffer_load_dwordx4 v14, s[44:47], s72 offen lds\n" \
    "v_mfma_f32_16x16x32_bf16 v[48:51], v[224:227], v[200:203], v[48:51]\n" \
    "ds_read_b64_tr_b16 v[186:187], v244 offset:2048\n" \
    "v_mfma_f32_16x16x32_bf16 v[52:55], v[228:231], v[200:203], v[52:55]\n" \
    "ds_read_b64_tr_b16 v[188:189], v245 offset:0\n" \
    "v_mfma_f32_16x16x32_bf16 v[56:59], v[232:235], v[200:203], v[56:59]\n" \
    "ds_read_b64_tr_b16 v[190:191], v245 offset:2048\n" \
    "v_mfma_f32_16x16x32_bf16 v[60:63], v[236:239], v[200:203], v[60:63]\n" \
    "s_add_u32 m0, m0, 1024\n" \
    "s_sub_u32 s72, s72, s69\n" \
    "v_mfma_f32_16x16x32_bf16 v[64:67], v[224:227], v[204:207], v[64:67]\n" \
    "ds_read_b128 v[144:147], v240 offset:0\n" \
    "buffer_load_dwordx4 v14, s[44:47], s72 offen lds\n" \
    "v_mfma_f32_16x16x32_bf16 v[68:71], v[228:231], v[204:207], v[68:71]\n" \
    "ds_read_b128 v[148:151], v240 offset:2048\n" \
    "v_mfma_f32_16x16x32_bf16 v[72:75], v[232:235], v[204:207], v[72:75]\n" \
    "ds_read_b128 v[152:155], v240 offset:4096\n" \
    "v_mfma_f32_16x16x32_bf16 v[76:79], v[236:239], v[204:207], v[76:79]\n" \
    "ds_read_b128 v[156:159], v240 offset:6144\n" \
    "v_mfma_f32_16x16x32_bf16 v[80:83], v[224:227], v[208:211], v[80:83]\n" \
    "s_add_u32 m0, m0, 1024\n" \
    "s_add_u32 s72, s72, s68\n" \
    "v_mfma_f32_16x16x32_bf16 v[84:87], v[228:231], v[208:211], v[84:87]\n" \
    "ds_read_b128 v[160:163], v240 offset:8192\n" \
    "buffer_load_dwordx4 v14, s[44:47], s72 offen lds\n" \
    "v_mfma_f32_16x16x32_bf16 v[88:91], v[232:235], v[208:211], v[88:91]\n" \
    "ds_read_b128 v[164:167], v240 offset:10240\n" \
    "v_mfma_f32_16x16x32_bf16 v[92:95], v[236:239], v[208:211], v[92:95]\n" \
    "ds_read_b128 v[168:171], v240 offset:12288\n" \
    "v_mfma_f32_16x16x32_bf16 v[96:99], v[224:227], v[212:215], v[96:99]\n" \
    "ds_read_b128 v[172:175], v240 offset:14336\n" \
    "v_mfma_f32_16x16x32_bf16 v[100:103], v[228:231], v[212:215], v[100:103]\n" \
    "s_add_u32 m0, s74, s78\n" \
    "s_mov_b32 s71, s73\n" \
    "v_mfma_f32_16x16x32_bf16 v[104:107], v[232:235], v[212:215], v[104:107]\n" \
    "buffer_load_dwordx4 v246, s[40:43], s71 offen sc1 lds\n" \
    "s_add_u32 s73, s73, 128\n" \
    "v_mfma_f32_16x16x32_bf16 v[108:111], v[236:239], v[212:215], v[108:111]\n" \
    "buffer_store_dwordx4 a[224:227], v15, s[48:51], s66 offen sc1\n" \
    "s_add_u32 s80, s66, s65\n" \
    "v_mfma_f32_16x16x32_bf16 v[112:115], v[224:227], v[216:219], v[112:115]\n" \
    "v_mfma_f32_16x16x32_bf16 v[116:119], v[228:231], v[216:219], v[116:119]\n" \
    "v_mfma_f32_16x16x32_bf16 v[120:123], v[232:235], v[216:219], v[120:123]\n" \
    "s_add_u32 m0, m0, 4096\n" \
    "s_add_u32 s71, s71, 65536\n" \
    "v_mfma_f32_16x16x32_bf16 v[124:127], v[236:239], v[216:219], v[124:127]\n" \
    "buffer_load_dwordx4 v246, s[40:43], s71 offen sc1 lds\n" \
    "s_and_b32 s73, s73, 2047\n" \
    "v_mfma_f32_16x16x32_bf16 v[128:131], v[224:227], v[220:223], v[128:131]\n" \
    "buffer_store_dwordx4 a[228:231], v15, s[48:51], s80 offen sc1\n" \
    "s_add_u32 s80, s66, s64\n" \
    "v_mfma_f32_16x16x32_bf16 v[132:135], v[228:231], v[220:223], v[132:135]\n" \
    "s_add_u32 s74, s74, s76\n" \
    "s_sub_u32 s75, s75, 1\n" \
    "v_mfma_f32_16x16x32_bf16 v[136:139], v[232:235], v[220:223], v[136:139]\n" \
    "v_mfma_f32_16x16x32_bf16 v[140:143], v[236:239], v[220:223], v[140:143]\n" \
    "s_cmp_lg_u32 s75, 0\n" \
    "s_waitcnt lgkmcnt(0)\n" \
    "v_mfma_f32_16x16x32_bf16 v[16:19], v[176:179], v[144:147], v[16:19]\n" \
    "ds_read_b64_tr_b16 v[224:225], v242 offset:4096\n" \
    "v_mfma_f32_16x16x32_bf16 v[20:23], v[180:183], v[144:147], v[20:23]\n" \
    "s_add_u32 m0, m0, 4096\n" \
    "s_add_u32 s71, s71, 65536\n" \
    "v_mfma_f32_16x16x32_bf16 v[24:27], v[184:187], v[144:147], v[24:27]\n" \
    "ds_read_b64_tr_b16 v[226:227], v242 offset:6144\n" \
    "buffer_load_dwordx4 v246, s[40:43], s71 offen sc1 lds\n" \
    "v_mfma_f32_16x16x32_bf16 v[28:31], v[188:191], v[144:147], v[28:31]\n" \
    "ds_read_b64_tr_b16 v[228:229], v243 offset:4096\n" \
    "v_mfma_f32_16x16x32_bf16 v[32:35], v[176:179], v[148:151], v[32:35]\n" \
    "ds_read_b64_tr_b16 v[230:231], v243 offset:6144\n" \
    "v_mfma_f32_16x16x32_bf16 v[36:39], v[180:183], v[148:151], v[36:39]\n" \
    "ds_read_b64_tr_b16 v[232:233], v244 offset:4096\n" \
    "v_mfma_f32_16x16x32_bf16 v[40:43], v[184:187], v[148:151], v[40:43]\n" \
    "s_add_u32 m0, m0, 4096\n" \
    "s_add_u32 s71, s71, 65536\n" \
    "v_mfma_f32_16x16x32_bf16 v[44:47], v[188:191], v[148:151], v[44:47]\n" \
    "ds_read_b64_tr_b16 v[234:235], v244 offset:6144\n" \
    "buffer_load_dwordx4 v246, s[40:43], s71 offen sc1 lds\n" \
    "v_mfma_f32_16x16x32_bf16 v[48:51], v[176:179], v[152:155], v[48:51]\n" \
    "ds_read_b64_tr_b16 v[236:237], v245 offset:4096\n" \
    "v_mfma_f32_16x16x32_bf16 v[52:55], v[180:183], v[152:155], v[52:55]\n" \
    "ds_read_b64_tr_b16 v[238:239], v245 offset:6144\n" \
    "v_mfma_f32_16x16x32_bf16 v[56:59], v[184:187], v[152:155], v[56:59]\n" \
    "ds_read_b128 v[192:195], v241 offset:0\n" \
    "v_mfma_f32_16x16x32_bf16 v[60:63], v[188:191], v[152:155], v[60:63]\n" \
    "s_add_u32 m0, m0, 4096\n" \
    "s_add_u32 s71, s71, 65536\n" \
    "v_mfma_f32_16x16x32_bf16 v[64:67], v[176:179], v[156:159], v[64:67]\n" \
    "ds_read_b128 v[196:199], v241 offset:2048\n" \
    "buffer_load_dwordx4 v246, s[40:43], s71 offen sc1 lds\n" \
    "v_mfma_f32_16x16x32_bf16 v[68:71], v[180:183], v[156:159], v[68:71]\n" \
    "ds_read_b128 v[200:203], v241 offset:4096\n" \
    "v_mfma_f32_16x16x32_bf16 v[72:75], v[184:187], v[156:159], v[72:75]\n" \
    "ds_read_b128 v[204:207], v241 offset:6144\n" \
    "v_mfma_f32_16x16x32_bf16 v[76:79], v[188:191], v[156:159], v[76:79]\n" \
    "ds_read_b128 v[208:211], v241 offset:8192\n" \
    "v_mfma_f32_16x16x32_bf16 v[80:83], v[176:179], v[160:163], v[80:83]\n" \
    "s_add_u32 m0, m0, 4096\n" \
    "s_add_u32 s71, s71, 65536\n" \
    "v_mfma_f32_16x16x32_bf16 v[84:87], v[180:183], v[160:163], v[84:87]\n" \
    "ds_read_b128 v[212:215], v241 offset:10240\n" \
    "buffer_load_dwordx4 v246, s[40:43], s71 offen sc1 lds\n" \
    "v_mfma_f32_16x16x32_bf16 v[88:91], v[184:187], v[160:163], v[88:91]\n" \
    "ds_read_b128 v[216:219], v241 offset:12288\n" \
    "v_mfma_f32_16x16x32_bf16 v[92:95], v[188:191], v[160:163], v[92:95]\n" \
    "ds_read_b128 v[220:223], v241 offset:14336\n" \
    "v_mfma_f32_16x16x32_bf16 v[96:99], v[176:179], v[164:167], v[96:99]\n" \
    "s_cmp_eq_u32 s74, 98304\n" \
    "s_cselect_b32 s76, s77, 49152\n" \
    "v_mfma_f32_16x16x32_bf16 v[100:103], v[180:183], v[164:167], v[100:103]\n" \
    "s_add_u32 m0, m0, 4096\n" \
    "s_add_u32 s71, s71, 65536\n" \
    "v_mfma_f32_16x16x32_bf16 v[104:107], v[184:187], v[164:167], v[104:107]\n" \
    "buffer_load_dwordx4 v246, s[40:43], s71 offen sc1 lds\n" \
    "v_mfma_f32_16x16x32_bf16 v[108:111], v[188:191], v[164:167], v[108:111]\n" \
    "v_add_u32 v242, s76, v242\n" \
    "v_mfma_f32_16x16x32_bf16 v[112:115], v[176:179], v[168:171], v[112:115]\n" \
    "v_add_u32 v243, s76, v243\n" \
    "v_mfma_f32_16x16x32_bf16 v[116:119], v[180:183], v[168:171], v[116:119]\n" \
    "v_add_u32 v244, s76, v244\n" \
    "v_mfma_f32_16x16x32_bf16 v[120:123], v[184:187], v[168:171], v[120:123]\n" \
    "s_add_u32 m0, m0, 4096\n" \
    "s_add_u32 s71, s71, 65536\n" \
    "v_mfma_f32_16x16x32_bf16 v[124:127], v[188:191], v[168:171], v[124:127]\n" \
    "buffer_load_dwordx4 v246, s[40:43], s71 offen sc1 lds\n" \
    "v_mfma_f32_16x16x32_bf16 v[128:131], v[176:179], v[172:175], v[128:131]\n" \
    "v_add_u32 v245, s76, v245\n" \
    "v_mfma_f32_16x16x32_bf16 v[132:135], v[180:183], v[172:175], v[132:135]\n" \
    "v_add_u32 v240, s76, v240\n" \
    "v_mfma_f32_16x16x32_bf16 v[136:139], v[184:187], v[172:175], v[136:139]\n" \
    "v_add_u32 v241, s76, v241\n" \
    "v_mfma_f32_16x16x32_bf16 v[140:143], v[188:191], v[172:175], v[140:143]\n" \
    "s_waitcnt vmcnt(14) lgkmcnt(0)\n" \
    "v_mfma_f32_16x16x32_bf16 v[16:19], v[224:227], v[192:195], v[16:19]\n" \
    "s_barrier\n" \
    "v_mfma_f32_16x16x32_bf16 v[20:23], v[228:231], v[192:195], v[20:23]\n" \
    "ds_read_b64_tr_b16 v[176:177], v242 offset:0\n" \
    "v_mfma_f32_16x16x32_bf16 v[24:27], v[232:235], v[192:195], v[24:27]\n" \
    "s_add_u32 m0, s74, s79\n" \
    "s_mul_i32 s72, s73, s70\n" \
    "v_mfma_f32_16x16x32_bf16 v[28:31], v[236:239], v[192:195], v[28:31]\n" \
    "ds_read_b64_tr_b16 v[178:179], v242 offset:2048\n" \
    "buffer_load_dwordx4 v14, s[44:47], s72 offen lds\n" \
    "v_mfma_f32_16x16x32_bf16 v[32:35], v[224:227], v[196:199], v[32:35]\n" \
    "ds_read_b64_tr_b16 v[180:181], v243 offset:0\n" \
    "v_mfma_f32_16x16x32_bf16 v[36:39], v[228:231], v[196:199], v[36:39]\n" \
    "ds_read_b64_tr_b16 v[182:183], v243 offset:2048\n" \
    "v_mfma_f32_16x16x32_bf16 v[40:43], v[232:235], v[196:199], v[40:43]\n" \
    "s_add_u32 m0, m0, 1024\n" \
    "s_add_u32 s72, s72, s68\n" \
    "v_mfma_f32_16x16x32_bf16 v[44:47], v[236:239], v[196:199], v[44:47]\n" \
    "ds_read_b64_tr_b16 v[184:185], v244 offset:0\n" \
    "buffer_load_dwordx4 v14, s[44:47], s72 offen lds\n" \
    "v_mfma_f32_16x16x32_bf16 v[48:51], v[224:227], v[200:203], v[48:51]\n" \
    "ds_read_b64_tr_b16 v[186:187], v244 offset:2048\n" \
    "v_mfma_f32_16x16x32_bf16 v[52:55], v[228:231], v[200:203], v[52:55]\n" \
    "ds_read_b64_tr_b16 v[188:189], v245 offset:0\n" \
    "v_mfma_f32_16x16x32_bf16 v[56:59], v[232:235], v[200:203], v[56:59]\n" \
    "ds_read_b64_tr_b16 v[190:191], v245 offset:2048\n" \
    "v_mfma_f32_16x16x32_bf16 v[60:63], v[236:239], v[200:203], v[60:63]\n" \
    "s_add_u32 m0, m0, 1024\n" \
    "s_sub_u32 s72, s72, s69\n" \
    "v_mfma_f32_16x16x32_bf16 v[64:67], v[224:227], v[204:207], v[64:67]\n" \
    "ds_read_b128 v[144:147], v240 offset:0\n" \
    "buffer_load_dwordx4 v14, s[44:47], s72 offen lds\n" \
    "v_mfma_f32_16x16x32_bf16 v[68:71], v[228:231], v[204:207], v[68:71]\n" \
    "ds_read_b128 v[148:151], v240 offset:2048\n" \
    "v_mfma_f32_16x16x32_bf16 v[72:75], v[232:235], v[204:207], v[72:75]\n" \
    "ds_read_b128 v[152:155], v240 offset:4096\n" \
    "v_mfma_f32_16x16x32_bf16 v[76:79], v[236:239], v[204:207], v[76:79]\n" \
    "ds_read_b128 v[156:159], v240 offset:6144\n" \
    "v_mfma_f32_16x16x32_bf16 v[80:83], v[224:227], v[208:211], v[80:83]\n" \
    "s_add_u32 m0, m0, 1024\n" \
    "s_add_u32 s72, s72, s68\n" \
    "v_mfma_f32_16x16x32_bf16 v[84:87], v[228:231], v[208:211], v[84:87]\n" \
    "ds_read_b128 v[160:163], v240 offset:8192\n" \
    "buffer_load_dwordx4 v14, s[44:47], s72 offen lds\n" \
    "v_mfma_f32_16x16x32_bf16 v[88:91], v[232:235], v[208:211], v[88:91]\n" \
    "ds_read_b128 v[164:167], v240 offset:10240\n" \
    "v_mfma_f32_16x16x32_bf16 v[92:95], v[236:239], v[208:211], v[92:95]\n" \
    "ds_read_b128 v[168:171], v240 offset:12288\n" \
    "v_mfma_f32_16x16x32_bf16 v[96:99], v[224:227], v[212:215], v[96:99]\n" \
    "ds_read_b128 v[172:175], v240 offset:14336\n" \
    "v_mfma_f32_16x16x32_bf16 v[100:103], v[228:231], v[212:215], v[100:103]\n" \
    "s_add_u32 m0, s74, s78\n" \
    "s_mov_b32 s71, s73\n" \
    "v_mfma_f32_16x16x32_bf16 v[104:107], v[232:235], v[212:215], v[104:107]\n" \
    "buffer_load_dwordx4 v246, s[40:43], s71 offen sc1 lds\n" \
    "s_add_u32 s73, s73, 128\n" \
    "v_mfma_f32_16x16x32_bf16 v[108:111], v[236:239], v[212:215], v[108:111]\n" \
    "buffer_store_dwordx4 a[232:235], v15, s[48:51], s80 offen sc1\n" \
    "s_add_u32 s80, s80, s65\n" \
    "v_mfma_f32_16x16x32_bf16 v[112:115], v[224:227], v[216:219], v[112:115]\n" \
    "v_mfma_f32_16x16x32_bf16 v[116:119], v[228:231], v[216:219], v[116:119]\n" \
    "v_mfma_f32_16x16x32_bf16 v[120:123], v[232:235], v[216:219], v[120:123]\n" \
    "s_add_u32 m0, m0, 4096\n" \
    "s_add_u32 s71, s71, 65536\n" \
    "v_mfma_f32_16x16x32_bf16 v[124:127], v[236:239], v[216:219], v[124:127]\n" \
    "buffer_load_dwordx4 v246, s[40:43], s71 offen sc1 lds\n" \
    "s_and_b32 s73, s73, 2047\n" \
    "v_mfma_f32_16x16x32_bf16 v[128:131], v[224:227], v[220:223], v[128:131]\n" \
    "buffer_store_dwordx4 a[236:239], v15, s[48:51], s80 offen sc1\n" \
    "s_add_u32 s66, s66, s63\n" \
    "v_mfma_f32_16x16x32_bf16 v[132:135], v[228:231], v[220:223], v[132:135]\n" \
    "s_add_u32 s74, s74, s76\n" \
    "s_sub_u32 s75, s75, 1\n" \
    "v_mfma_f32_16x16x32_bf16 v[136:139], v[232:235], v[220:223], v[136:139]\n" \
    "v_mfma_f32_16x16x32_bf16 v[140:143], v[236:239], v[220:223], v[140:143]\n" \
    "s_cmp_lg_u32 s75, 0\n" \
    "s_waitcnt lgkmcnt(0)\n" \
    "v_mfma_f32_16x16x32_bf16 v[16:19], v[176:179], v[144:147], v[16:19]\n" \
    "ds_read_b64_tr_b16 v[224:225], v242 offset:4096\n" \
    "v_mfma_f32_16x16x32_bf16 v[20:23], v[180:183], v[144:147], v[20:23]\n" \
    "s_add_u32 m0, m0, 4096\n" \
    "s_add_u32 s71, s71, 65536\n" \
    "v_mfma_f32_16x16x32_bf16 v[24:27], v[184:187], v[144:147], v[24:27]\n" \
    "ds_read_b64_tr_b16 v[226:227], v242 offset:6144\n" \
    "buffer_load_dwordx4 v246, s[40:43], s71 offen sc1 lds\n" \
    "v_mfma_f32_16x16x32_bf16 v[28:31], v[188:191], v[144:147], v[28:31]\n" \
    "ds_read_b64_tr_b16 v[228:229], v243 offset:4096\n" \
    "v_mfma_f32_16x16x32_bf16 v[32:35], v[176:179], v[148:151], v[32:35]\n" \
    "ds_read_b64_tr_b16 v[230:231], v243 offset:6144\n" \
    "v_mfma_f32_16x16x32_bf16 v[36:39], v[180:183], v[148:151], v[36:39]\n" \
    "ds_read_b64_tr_b16 v[232:233], v244 offset:4096\n" \
    "v_mfma_f32_16x16x32_bf16 v[40:43], v[184:187], v[148:151], v[40:43]\n" \
    "s_add_u32 m0, m0, 4096\n" \
    "s_add_u32 s71, s71, 65536\n" \
    "v_mfma_f32_16x16x32_bf16 v[44:47], v[188:191], v[148:151], v[44:47]\n" \
    "ds_read_b64_tr_b16 v[234:235], v244 offset:6144\n" \
    "buffer_load_dwordx4 v246, s[40:43], s71 offen sc1 lds\n" \
    "v_mfma_f32_16x16x32_bf16 v[48:51], v[176:179], v[152:155], v[48:51]\n" \
    "ds_read_b64_tr_b16 v[236:237], v245 offset:4096\n" \
    "v_mfma_f32_16x16x32_bf16 v[52:55], v[180:183], v[152:155], v[52:55]\n" \
    "ds_read_b64_tr_b16 v[238:239], v245 offset:6144\n" \
    "v_mfma_f32_16x16x32_bf16 v[56:59], v[184:187], v[152:155], v[56:59]\n" \
    "ds_read_b128 v[192:195], v241 offset:0\n" \
    "v_mfma_f32_16x16x32_bf16 v[60:63], v[188:191], v[152:155], v[60:63]\n" \
    "s_add_u32 m0, m0, 4096\n" \
    "s_add_u32 s71, s71, 65536\n" \
    "v_mfma_f32_16x16x32_bf16 v[64:67], v[176:179], v[156:159], v[64:67]\n" \
    "ds_read_b128 v[196:199], v241 offset:2048\n" \
    "buffer_load_dwordx4 v246, s[40:43], s71 offen sc1 lds\n" \
    "v_mfma_f32_16x16x32_bf16 v[68:71], v[180:183], v[156:159], v[68:71]\n" \
    "ds_read_b128 v[200:203], v241 offset:4096\n" \
    "v_mfma_f32_16x16x32_bf16 v[72:75], v[184:187], v[156:159], v[72:75]\n" \
    "ds_read_b128 v[204:207], v241 offset:6144\n" \
    "v_mfma_f32_16x16x32_bf16 v[76:79], v[188:191], v[156:159], v[76:79]\n" \
    "ds_read_b128 v[208:211], v241 offset:8192\n" \
    "v_mfma_f32_16x16x32_bf16 v[80:83], v[176:179], v[160:163], v[80:83]\n" \
    "s_add_u32 m0, m0, 4096\n" \
    "s_add_u32 s71, s71, 65536\n" \
    "v_mfma_f32_16x16x32_bf16 v[84:87], v[180:183], v[160:163], v[84:87]\n" \
    "ds_read_b128 v[212:215], v241 offset:10240\n" \
    "buffer_load_dwordx4 v246, s[40:43], s71 offen sc1 lds\n" \
    "v_mfma_f32_16x16x32_bf16 v[88:91], v[184:187], v[160:163], v[88:91]\n" \
    "ds_read_b128 v[216:219], v241 offset:12288\n" \
    "v_mfma_f32_16x16x32_bf16 v[92:95], v[188:191], v[160:163], v[92:95]\n" \
    "ds_read_b128 v[220:223], v241 offset:14336\n" \
    "v_mfma_f32_16x16x32_bf16 v[96:99], v[176:179], v[164:167], v[96:99]\n" \
    "s_cmp_eq_u32 s74, 98304\n" \
    "s_cselect_b32 s76, s77, 49152\n" \
    "v_mfma_f32_16x16x32_bf16 v[100:103], v[180:183], v[164:167], v[100:103]\n" \
    "s_add_u32 m0, m0, 4096\n" \
    "s_add_u32 s71, s71, 65536\n" \
    "v_mfma_f32_16x16x32_bf16 v[104:107], v[184:187], v[164:167], v[104:107]\n" \
    "buffer_load_dwordx4 v246, s[40:43], s71 offen sc1 lds\n" \
    "v_mfma_f32_16x16x32_bf16 v[108:111], v[188:191], v[164:167], v[108:111]\n" \
    "v_add_u32 v242, s76, v242\n" \
    "v_mfma_f32_16x16x32_bf16 v[112:115], v[176:179], v[168:171], v[112:115]\n" \
    "v_add_u32 v243, s76, v243\n" \
    "v_mfma_f32_16x16x32_bf16 v[116:119], v[180:183], v[168:171], v[116:119]\n" \
    "v_add_u32 v244, s76, v244\n" \
    "v_mfma_f32_16x16x32_bf16 v[120:123], v[184:187], v[168:171], v[120:123]\n" \
    "s_add_u32 m0, m0, 4096\n" \
    "s_add_u32 s71, s71, 65536\n" \
    "v_mfma_f32_16x16x32_bf16 v[124:127], v[188:191], v[168:171], v[124:127]\n" \
    "buffer_load_dwordx4 v246, s[40:43], s71 offen sc1 lds\n" \
    "v_mfma_f32_16x16x32_bf16 v[128:131], v[176:179], v[172:175], v[128:131]\n" \
    "v_add_u32 v245, s76, v245\n" \
    "v_mfma_f32_16x16x32_bf16 v[132:135], v[180:183], v[172:175], v[132:135]\n" \
    "v_add_u32 v240, s76, v240\n" \
    "v_mfma_f32_16x16x32_bf16 v[136:139], v[184:187], v[172:175], v[136:139]\n" \
    "v_add_u32 v241, s76, v241\n" \
    "v_mfma_f32_16x16x32_bf16 v[140:143], v[188:191], v[172:175], v[140:143]\n" \
    "s_waitcnt vmcnt(14) lgkmcnt(0)\n" \
    "v_mfma_f32_16x16x32_bf16 v[16:19], v[224:227], v[192:195], v[16:19]\n" \
    "s_barrier\n" \
    "v_mfma_f32_16x16x32_bf16 v[20:23], v[228:231], v[192:195], v[20:23]\n" \
    "ds_read_b64_tr_b16 v[176:177], v242 offset:0\n" \
    "v_mfma_f32_16x16x32_bf16 v[24:27], v[232:235], v[192:195], v[24:27]\n" \
    "s_add_u32 m0, s74, s79\n" \
    "s_mul_i32 s72, s73, s70\n" \
    "v_mfma_f32_16x16x32_bf16 v[28:31], v[236:239], v[192:195], v[28:31]\n" \
    "ds_read_b64_tr_b16 v[178:179], v242 offset:2048\n" \
    "buffer_load_dwordx4 v14, s[44:47], s72 offen lds\n" \
    "v_mfma_f32_16x16x32_bf16 v[32:35], v[224:227], v[196:199], v[32:35]\n" \
    "ds_read_b64_tr_b16 v[180:181], v243 offset:0\n" \
    "v_mfma_f32_16x16x32_bf16 v[36:39], v[228:231], v[196:199], v[36:39]\n" \
    "ds_read_b64_tr_b16 v[182:183], v243 offset:2048\n" \
    "v_mfma_f32_16x16x32_bf16 v[40:43], v[232:235], v[196:199], v[40:43]\n" \
    "s_add_u32 m0, m0, 1024\n" \
    "s_add_u32 s72, s72, s68\n" \
    "v_mfma_f32_16x16x32_bf16 v[44:47], v[236:239], v[196:199], v[44:47]\n" \
    "ds_read_b64_tr_b16 v[184:185], v244 offset:0\n" \
    "buffer_load_dwordx4 v14, s[44:47], s72 offen lds\n" \
    "v_mfma_f32_16x16x32_bf16 v[48:51], v[224:227], v[200:203], v[48:51]\n" \
    "ds_read_b64_tr_b16 v[186:187], v244 offset:2048\n" \
    "v_mfma_f32_16x16x32_bf16 v[52:55], v[228:231], v[200:203], v[52:55]\n" \
    "ds_read_b64_tr_b16 v[188:189], v245 offset:0\n" \
    "v_mfma_f32_16x16x32_bf16 v[56:59], v[232:235], v[200:203], v[56:59]\n" \
    "ds_read_b64_tr_b16 v[190:191], v245 offset:2048\n" \
    "v_mfma_f32_16x16x32_bf16 v[60:63], v[236:239], v[200:203], v[60:63]\n" \
    "s_add_u32 m0, m0, 1024\n" \
    "s_sub_u32 s72, s72, s69\n" \
    "v_mfma_f32_16x16x32_bf16 v[64:67], v[224:227], v[204:207], v[64:67]\n" \
    "ds_read_b128 v[144:147], v240 offset:0\n" \
    "buffer_load_dwordx4 v14, s[44:47], s72 offen lds\n" \
    "v_mfma_f32_16x16x32_bf16 v[68:71], v[228:231], v[204:207], v[68:71]\n" \
    "ds_read_b128 v[148:151], v240 offset:2048\n" \
    "v_mfma_f32_16x16x32_bf16 v[72:75], v[232:235], v[204:207], v[72:75]\n" \
    "ds_read_b128 v[152:155], v240 offset:4096\n" \
    "v_mfma_f32_16x16x32_bf16 v[76:79], v[236:239], v[204:207], v[76:79]\n" \
    "ds_read_b128 v[156:159], v240 offset:6144\n" \
    "v_mfma_f32_16x16x32_bf16 v[80:83], v[224:227], v[208:211], v[80:83]\n" \
    "s_add_u32 m0, m0, 1024\n" \
    "s_add_u32 s72, s72, s68\n" \
    "v_mfma_f32_16x16x32_bf16 v[84:87], v[228:231], v[208:211], v[84:87]\n" \
    "ds_read_b128 v[160:163], v240 offset:8192\n" \
    "buffer_load_dwordx4 v14, s[44:47], s72 offen lds\n" \
    "v_mfma_f32_16x16x32_bf16 v[88:91], v[232:235], v[208:211], v[88:91]\n" \
    "ds_read_b128 v[164:167], v240 offset:10240\n" \
    "v_mfma_f32_16x16x32_bf16 v[92:95], v[236:239], v[208:211], v[92:95]\n" \
    "ds_read_b128 v[168:171], v240 offset:12288\n" \
    "v_mfma_f32_16x16x32_bf16 v[96:99], v[224:227], v[212:215], v[96:99]\n" \
    "ds_read_b128 v[172:175], v240 offset:14336\n" \
    "v_mfma_f32_16x16x32_bf16 v[100:103], v[228:231], v[212:215], v[100:103]\n" \
    "s_add_u32 m0, s74, s78\n" \
    "s_mov_b32 s71, s73\n" \
    "v_mfma_f32_16x16x32_bf16 v[104:107], v[232:235], v[212:215], v[104:107]\n" \
    "buffer_load_dwordx4 v246, s[40:43], s71 offen sc1 lds\n" \
    "s_add_u32 s73, s73, 128\n" \
    "v_mfma_f32_16x16x32_bf16 v[108:111], v[236:239], v[212:215], v[108:111]\n" \
    "buffer_store_dwordx4 a[240:243], v15, s[48:51], s66 offen sc1\n" \
    "s_add_u32 s80, s66, s65\n" \
    "v_mfma_f32_16x16x32_bf16 v[112:115], v[224:227], v[216:219], v[112:115]\n" \
    "v_mfma_f32_16x16x32_bf16 v[116:119], v[228:231], v[216:219], v[116:119]\n" \
    "v_mfma_f32_16x16x32_bf16 v[120:123], v[232:235], v[216:219], v[120:123]\n" \
    "s_add_u32 m0, m0, 4096\n" \
    "s_add_u32 s71, s71, 65536\n" \
    "v_mfma_f32_16x16x32_bf16 v[124:127], v[236:239], v[216:219], v[124:127]\n" \
    "buffer_load_dwordx4 v246, s[40:43], s71 offen sc1 lds\n" \
    "s_and_b32 s73, s73, 2047\n" \
    "v_mfma_f32_16x16x32_bf16 v[128:131], v[224:227], v[220:223], v[128:131]\n" \
    "buffer_store_dwordx4 a[244:247], v15, s[48:51], s80 offen sc1\n" \
    "s_add_u32 s80, s66, s64\n" \
    "v_mfma_f32_16x16x32_bf16 v[132:135], v[228:231], v[220:223], v[132:135]\n" \
    "s_add_u32 s74, s74, s76\n" \
    "s_sub_u32 s75, s75, 1\n" \
    "v_mfma_f32_16x16x32_bf16 v[136:139], v[232:235], v[220:223], v[136:139]\n" \
    "v_mfma_f32_16x16x32_bf16 v[140:143], v[236:239], v[220:223], v[140:143]\n" \
    "s_cmp_lg_u32 s75, 0\n" \
    "s_waitcnt lgkmcnt(0)\n" \
    "v_mfma_f32_16x16x32_bf16 v[16:19], v[176:179], v[144:147], v[16:19]\n" \
    "ds_read_b64_tr_b16 v[224:225], v242 offset:4096\n" \
    "v_mfma_f32_16x16x32_bf16 v[20:23], v[180:183], v[144:147], v[20:23]\n" \
    "s_add_u32 m0, m0, 4096\n" \
    "s_add_u32 s71, s71, 65536\n" \
    "v_mfma_f32_16x16x32_bf16 v[24:27], v[184:187], v[144:147], v[24:27]\n" \
    "ds_read_b64_tr_b16 v[226:227], v242 offset:6144\n" \
    "buffer_load_dwordx4 v246, s[40:43], s71 offen sc1 lds\n" \
    "v_mfma_f32_16x16x32_bf16 v[28:31], v[188:191], v[144:147], v[28:31]\n" \
    "ds_read_b64_tr_b16 v[228:229], v243 offset:4096\n" \
    "v_mfma_f32_16x16x32_bf16 v[32:35], v[176:179], v[148:151], v[32:35]\n" \
    "ds_read_b64_tr_b16 v[230:231], v243 offset:6144\n" \
    "v_mfma_f32_16x16x32_bf16 v[36:39], v[180:183], v[148:151], v[36:39]\n" \
    "ds_read_b64_tr_b16 v[232:233], v244 offset:4096\n" \
    "v_mfma_f32_16x16x32_bf16 v[40:43], v[184:187], v[148:151], v[40:43]\n" \
    "s_add_u32 m0, m0, 4096\n" \
    "s_add_u32 s71, s71, 65536\n" \
    "v_mfma_f32_16x16x32_bf16 v[44:47], v[188:191], v[148:151], v[44:47]\n" \
    "ds_read_b64_tr_b16 v[234:235], v244 offset:6144\n" \
    "buffer_load_dwordx4 v246, s[40:43], s71 offen sc1 lds\n" \
    "v_mfma_f32_16x16x32_bf16 v[48:51], v[176:179], v[152:155], v[48:51]\n" \
    "ds_read_b64_tr_b16 v[236:237], v245 offset:4096\n" \
    "v_mfma_f32_16x16x32_bf16 v[52:55], v[180:183], v[152:155], v[52:55]\n" \
    "ds_read_b64_tr_b16 v[238:239], v245 offset:6144\n" \
    "v_mfma_f32_16x16x32_bf16 v[56:59], v[184:187], v[152:155], v[56:59]\n" \
    "ds_read_b128 v[192:195], v241 offset:0\n" \
    "v_mfma_f32_16x16x32_bf16 v[60:63], v[188:191], v[152:155], v[60:63]\n" \
    "s_add_u32 m0, m0, 4096\n" \
    "s_add_u32 s71, s71, 65536\n" \
    "v_mfma_f32_16x16x32_bf16 v[64:67], v[176:179], v[156:159], v[64:67]\n" \
    "ds_read_b128 v[196:199], v241 offset:2048\n" \
    "buffer_load_dwordx4 v246, s[40:43], s71 offen sc1 lds\n" \
    "v_mfma_f32_16x16x32_bf16 v[68:71], v[180:183], v[156:159], v[68:71]\n" \
    "ds_read_b128 v[200:203], v241 offset:4096\n" \
    "v_mfma_f32_16x16x32_bf16 v[72:75], v[184:187], v[156:159], v[72:75]\n" \
    "ds_read_b128 v[204:207], v241 offset:6144\n" \
    "v_mfma_f32_16x16x32_bf16 v[76:79], v[188:191], v[156:159], v[76:79]\n" \
    "ds_read_b128 v[208:211], v241 offset:8192\n" \
    "v_mfma_f32_16x16x32_bf16 v[80:83], v[176:179], v[160:163], v[80:83]\n" \
    "s_add_u32 m0, m0, 4096\n" \
    "s_add_u32 s71, s71, 65536\n" \
    "v_mfma_f32_16x16x32_bf16 v[84:87], v[180:183], v[160:163], v[84:87]\n" \
    "ds_read_b128 v[212:215], v241 offset:10240\n" \
    "buffer_load_dwordx4 v246, s[40:43], s71 offen sc1 lds\n" \
    "v_mfma_f32_16x16x32_bf16 v[88:91], v[184:187], v[160:163], v[88:91]\n" \
    "ds_read_b128 v[216:219], v241 offset:12288\n" \
    "v_mfma_f32_16x16x32_bf16 v[92:95], v[188:191], v[160:163], v[92:95]\n" \
    "ds_read_b128 v[220:223], v241 offset:14336\n" \
    "v_mfma_f32_16x16x32_bf16 v[96:99], v[176:179], v[164:167], v[96:99]\n" \
    "s_cmp_eq_u32 s74, 98304\n" \
    "s_cselect_b32 s76, s77, 49152\n" \
    "v_mfma_f32_16x16x32_bf16 v[100:103], v[180:183], v[164:167], v[100:103]\n" \
    "s_add_u32 m0, m0, 4096\n" \
    "s_add_u32 s71, s71, 65536\n" \
    "v_mfma_f32_16x16x32_bf16 v[104:107], v[184:187], v[164:167], v[104:107]\n" \
    "buffer_load_dwordx4 v246, s[40:43], s71 offen sc1 lds\n" \
    "v_mfma_f32_16x16x32_bf16 v[108:111], v[188:191], v[164:167], v[108:111]\n" \
    "v_add_u32 v242, s76, v242\n" \
    "v_mfma_f32_16x16x32_bf16 v[112:115], v[176:179], v[168:171], v[112:115]\n" \
    "v_add_u32 v243, s76, v243\n" \
    "v_mfma_f32_16x16x32_bf16 v[116:119], v[180:183], v[168:171], v[116:119]\n" \
    "v_add_u32 v244, s76, v244\n" \
    "v_mfma_f32_16x16x32_bf16 v[120:123], v[184:187], v[168:171], v[120:123]\n" \
    "s_add_u32 m0, m0, 4096\n" \
    "s_add_u32 s71, s71, 65536\n" \
    "v_mfma_f32_16x16x32_bf16 v[124:127], v[188:191], v[168:171], v[124:127]\n" \
    "buffer_load_dwordx4 v246, s[40:43], s71 offen sc1 lds\n" \
    "v_mfma_f32_16x16x32_bf16 v[128:131], v[176:179], v[172:175], v[128:131]\n" \
    "v_add_u32 v245, s76, v245\n" \
    "v_mfma_f32_16x16x32_bf16 v[132:135], v[180:183], v[172:175], v[132:135]\n" \
    "v_add_u32 v240, s76, v240\n" \
    "v_mfma_f32_16x16x32_bf16 v[136:139], v[184:187], v[172:175], v[136:139]\n" \
    "v_add_u32 v241, s76, v241\n" \
    "v_mfma_f32_16x16x32_bf16 v[140:143], v[188:191], v[172:175], v[140:143]\n" \
    "s_waitcnt vmcnt(14) lgkmcnt(0)\n" \
    "v_mfma_f32_16x16x32_bf16 v[16:19], v[224:227], v[192:195], v[16:19]\n" \
    "s_barrier\n" \
    "v_mfma_f32_16x16x32_bf16 v[20:23], v[228:231], v[192:195], v[20:23]\n" \
    "ds_read_b64_tr_b16 v[176:177], v242 offset:0\n" \
    "v_mfma_f32_16x16x32_bf16 v[24:27], v[232:235], v[192:195], v[24:27]\n" \
    "s_add_u32 m0, s74, s79\n" \
    "s_mul_i32 s72, s73, s70\n" \
    "v_mfma_f32_16x16x32_bf16 v[28:31], v[236:239], v[192:195], v[28:31]\n" \
    "ds_read_b64_tr_b16 v[178:179], v242 offset:2048\n" \
    "buffer_load_dwordx4 v14, s[44:47], s72 offen lds\n" \
    "v_mfma_f32_16x16x32_bf16 v[32:35], v[224:227], v[196:199], v[32:35]\n" \
    "ds_read_b64_tr_b16 v[180:181], v243 offset:0\n" \
    "v_mfma_f32_16x16x32_bf16 v[36:39], v[228:231], v[196:199], v[36:39]\n" \
    "ds_read_b64_tr_b16 v[182:183], v243 offset:2048\n" \
    "v_mfma_f32_16x16x32_bf16 v[40:43], v[232:235], v[196:199], v[40:43]\n" \
    "s_add_u32 m0, m0, 1024\n" \
    "s_add_u32 s72, s72, s68\n" \
    "v_mfma_f32_16x16x32_bf16 v[44:47], v[236:239], v[196:199], v[44:47]\n" \
    "ds_read_b64_tr_b16 v[184:185], v244 offset:0\n" \
    "buffer_load_dwordx4 v14, s[44:47], s72 offen lds\n" \
    "v_mfma_f32_16x16x32_bf16 v[48:51], v[224:227], v[200:203], v[48:51]\n" \
    "ds_read_b64_tr_b16 v[186:187], v244 offset:2048\n" \
    "v_mfma_f32_16x16x32_bf16 v[52:55], v[228:231], v[200:203], v[52:55]\n" \
    "ds_read_b64_tr_b16 v[188:189], v245 offset:0\n" \
    "v_mfma_f32_16x16x32_bf16 v[56:59], v[232:235], v[200:203], v[56:59]\n" \
    "ds_read_b64_tr_b16 v[190:191], v245 offset:2048\n" \
    "v_mfma_f32_16x16x32_bf16 v[60:63], v[236:239], v[200:203], v[60:63]\n" \
    "s_add_u32 m0, m0, 1024\n" \
    "s_sub_u32 s72, s72, s69\n" \
    "v_mfma_f32_16x16x32_bf16 v[64:67], v[224:227], v[204:207], v[64:67]\n" \
    "ds_read_b128 v[144:147], v240 offset:0\n" \
    "buffer_load_dwordx4 v14, s[44:47], s72 offen lds\n" \
    "v_mfma_f32_16x16x32_bf16 v[68:71], v[228:231], v[204:207], v[68:71]\n" \
    "ds_read_b128 v[148:151], v240 offset:2048\n" \
    "v_mfma_f32_16x16x32_bf16 v[72:75], v[232:235], v[204:207], v[72:75]\n" \
    "ds_read_b128 v[152:155], v240 offset:4096\n" \
    "v_mfma_f32_16x16x32_bf16 v[76:79], v[236:239], v[204:207], v[76:79]\n" \
    "ds_read_b128 v[156:159], v240 offset:6144\n" \
    "v_mfma_f32_16x16x32_bf16 v[80:83], v[224:227], v[208:211], v[80:83]\n" \
    "s_add_u32 m0, m0, 1024\n" \
    "s_add_u32 s72, s72, s68\n" \
    "v_mfma_f32_16x16x32_bf16 v[84:87], v[228:231], v[208:211], v[84:87]\n" \
    "ds_read_b128 v[160:163], v240 offset:8192\n" \
    "buffer_load_dwordx4 v14, s[44:47], s72 offen lds\n" \
    "v_mfma_f32_16x16x32_bf16 v[88:91], v[232:235], v[208:211], v[88:91]\n" \
    "ds_read_b128 v[164:167], v240 offset:10240\n" \
    "v_mfma_f32_16x16x32_bf16 v[92:95], v[236:239], v[208:211], v[92:95]\n" \
    "ds_read_b128 v[168:171], v240 offset:12288\n" \
    "v_mfma_f32_16x16x32_bf16 v[96:99], v[224:227], v[212:215], v[96:99]\n" \
    "ds_read_b128 v[172:175], v240 offset:14336\n" \
    "v_mfma_f32_16x16x32_bf16 v[100:103], v[228:231], v[212:215], v[100:103]\n" \
    "s_add_u32 m0, s74, s78\n" \
    "s_mov_b32 s71, s73\n" \
    "v_mfma_f32_16x16x32_bf16 v[104:107], v[232:235], v[212:215], v[104:107]\n" \
    "buffer_load_dwordx4 v246, s[40:43], s71 offen sc1 lds\n" \
    "s_add_u32 s73, s73, 128\n" \
    "v_mfma_f32_16x16x32_bf16 v[108:111], v[236:239], v[212:215], v[108:111]\n" \
    "buffer_store_dwordx4 a[248:251], v15, s[48:51], s80 offen sc1\n" \
    "s_add_u32 s80, s80, s65\n" \
    "v_mfma_f32_16x16x32_bf16 v[112:115], v[224:227], v[216:219], v[112:115]\n" \
    "v_mfma_f32_16x16x32_bf16 v[116:119], v[228:231], v[216:219], v[116:119]\n" \
    "v_mfma_f32_16x16x32_bf16 v[120:123], v[232:235], v[216:219], v[120:123]\n" \
    "s_add_u32 m0, m0, 4096\n" \
    "s_add_u32 s71, s71, 65536\n" \
    "v_mfma_f32_16x16x32_bf16 v[124:127], v[236:239], v[216:219], v[124:127]\n" \
    "buffer_load_dwordx4 v246, s[40:43], s71 offen sc1 lds\n" \
    "s_and_b32 s73, s73, 2047\n" \
    "v_mfma_f32_16x16x32_bf16 v[128:131], v[224:227], v[220:223], v[128:131]\n" \
    "buffer_store_dwordx4 a[252:255], v15, s[48:51], s80 offen sc1\n" \
    "s_add_u32 s66, s66, s63\n" \
    "v_mfma_f32_16x16x32_bf16 v[132:135], v[228:231], v[220:223], v[132:135]\n" \
    "s_add_u32 s74, s74, s76\n" \
    "s_sub_u32 s75, s75, 1\n" \
    "v_mfma_f32_16x16x32_bf16 v[136:139], v[232:235], v[220:223], v[136:139]\n" \
    "v_mfma_f32_16x16x32_bf16 v[140:143], v[236:239], v[220:223], v[140:143]\n" \
    "s_cmp_lg_u32 s75, 0\n" \
    "Lg80_p1%=:\n" \
    "s_waitcnt lgkmcnt(0)\n" \
    "v_mfma_f32_16x16x32_bf16 v[16:19], v[176:179], v[144:147], v[16:19]\n" \
    "ds_read_b64_tr_b16 v[224:225], v242 offset:4096\n" \
    "v_mfma_f32_16x16x32_bf16 v[20:23], v[180:183], v[144:147], v[20:23]\n" \
    "s_add_u32 m0, m0, 4096\n" \
    "s_add_u32 s71, s71, 65536\n" \
    "v_mfma_f32_16x16x32_bf16 v[24:27], v[184:187], v[144:147], v[24:27]\n" \
    "ds_read_b64_tr_b16 v[226:227], v242 offset:6144\n" \
    "buffer_load_dwordx4 v246, s[40:43], s71 offen sc1 lds\n" \
    "v_mfma_f32_16x16x32_bf16 v[28:31], v[188:191], v[144:147], v[28:31]\n" \
    "ds_read_b64_tr_b16 v[228:229], v243 offset:4096\n" \
    "v_mfma_f32_16x16x32_bf16 v[32:35], v[176:179], v[148:151], v[32:35]\n" \
    "ds_read_b64_tr_b16 v[230:231], v243 offset:6144\n" \
    "v_mfma_f32_16x16x32_bf16 v[36:39], v[180:183], v[148:151], v[36:39]\n" \
    "ds_read_b64_tr_b16 v[232:233], v244 offset:4096\n" \
    "v_mfma_f32_16x16x32_bf16 v[40:43], v[184:187], v[148:151], v[40:43]\n" \
    "s_add_u32 m0, m0, 4096\n" \
    "s_add_u32 s71, s71, 65536\n" \
    "v_mfma_f32_16x16x32_bf16 v[44:47], v[188:191], v[148:151], v[44:47]\n" \
    "ds_read_b64_tr_b16 v[234:235], v244 offset:6144\n" \
    "buffer_load_dwordx4 v246, s[40:43], s71 offen sc1 lds\n" \
    "v_mfma_f32_16x16x32_bf16 v[48:51], v[176:179], v[152:155], v[48:51]\n" \
    "ds_read_b64_tr_b16 v[236:237], v245 offset:4096\n" \
    "v_mfma_f32_16x16x32_bf16 v[52:55], v[180:183], v[152:155], v[52:55]\n" \
    "ds_read_b64_tr_b16 v[238:239], v245 offset:6144\n" \
    "v_mfma_f32_16x16x32_bf16 v[56:59], v[184:187], v[152:155], v[56:59]\n" \
    "ds_read_b128 v[192:195], v241 offset:0\n" \
    "v_mfma_f32_16x16x32_bf16 v[60:63], v[188:191], v[152:155], v[60:63]\n" \
    "s_add_u32 m0, m0, 4096\n" \
    "s_add_u32 s71, s71, 65536\n" \
    "v_mfma_f32_16x16x32_bf16 v[64:67], v[176:179], v[156:159], v[64:67]\n" \
    "ds_read_b128 v[196:199], v241 offset:2048\n" \
    "buffer_load_dwordx4 v246, s[40:43], s71 offen sc1 lds\n" \
    "v_mfma_f32_16x16x32_bf16 v[68:71], v[180:183], v[156:159], v[68:71]\n" \
    "ds_read_b128 v[200:203], v241 offset:4096\n" \
    "v_mfma_f32_16x16x32_bf16 v[72:75], v[184:187], v[156:159], v[72:75]\n" \
    "ds_read_b128 v[204:207], v241 offset:6144\n" \
    "v_mfma_f32_16x16x32_bf16 v[76:79], v[188:191], v[156:159], v[76:79]\n" \
    "ds_read_b128 v[208:211], v241 offset:8192\n" \
    "v_mfma_f32_16x16x32_bf16 v[80:83], v[176:179], v[160:163], v[80:83]\n" \
    "s_add_u32 m0, m0, 4096\n" \
    "s_add_u32 s71, s71, 65536\n" \
    "v_mfma_f32_16x16x32_bf16 v[84:87], v[180:183], v[160:163], v[84:87]\n" \
    "ds_read_b128 v[212:215], v241 offset:10240\n" \
    "buffer_load_dwordx4 v246, s[40:43], s71 offen sc1 lds\n" \
    "v_mfma_f32_16x16x32_bf16 v[88:91], v[184:187], v[160:163], v[88:91]\n" \
    "ds_read_b128 v[216:219], v241 offset:12288\n" \
    "v_mfma_f32_16x16x32_bf16 v[92:95], v[188:191], v[160:163], v[92:95]\n" \
    "ds_read_b128 v[220:223], v241 offset:14336\n" \
    "v_mfma_f32_16x16x32_bf16 v[96:99], v[176:179], v[164:167], v[96:99]\n" \
    "s_cmp_eq_u32 s74, 98304\n" \
    "s_cselect_b32 s76, s77, 49152\n" \
    "v_mfma_f32_16x16x32_bf16 v[100:103], v[180:183], v[164:167], v[100:103]\n" \
    "s_add_u32 m0, m0, 4096\n" \
    "s_add_u32 s71, s71, 65536\n" \
    "v_mfma_f32_16x16x32_bf16 v[104:107], v[184:187], v[164:167], v[104:107]\n" \
    "buffer_load_dwordx4 v246, s[40:43], s71 offen sc1 lds\n" \
    "v_mfma_f32_16x16x32_bf16 v[108:111], v[188:191], v[164:167], v[108:111]\n" \
    "v_add_u32 v242, s76, v242\n" \
    "v_mfma_f32_16x16x32_bf16 v[112:115], v[176:179], v[168:171], v[112:115]\n" \
    "v_add_u32 v243, s76, v243\n" \
    "v_mfma_f32_16x16x32_bf16 v[116:119], v[180:183], v[168:171], v[116:119]\n" \
    "v_add_u32 v244, s76, v244\n" \
    "v_mfma_f32_16x16x32_bf16 v[120:123], v[184:187], v[168:171], v[120:123]\n" \
    "s_add_u32 m0, m0, 4096\n" \
    "s_add_u32 s71, s71, 65536\n" \
    "v_mfma_f32_16x16x32_bf16 v[124:127], v[188:191], v[168:171], v[124:127]\n" \
    "buffer_load_dwordx4 v246, s[40:43], s71 offen sc1 lds\n" \
    "v_mfma_f32_16x16x32_bf16 v[128:131], v[176:179], v[172:175], v[128:131]\n" \
    "v_add_u32 v245, s76, v245\n" \
    "v_mfma_f32_16x16x32_bf16 v[132:135], v[180:183], v[172:175], v[132:135]\n" \
    "v_add_u32 v240, s76, v240\n" \
    "v_mfma_f32_16x16x32_bf16 v[136:139], v[184:187], v[172:175], v[136:139]\n" \
    "v_add_u32 v241, s76, v241\n" \
    "v_mfma_f32_16x16x32_bf16 v[140:143], v[188:191], v[172:175], v[140:143]\n" \
    "Lg80_p2%=:\n" \
    "s_waitcnt vmcnt(12) lgkmcnt(0)\n" \
    "v_mfma_f32_16x16x32_bf16 v[16:19], v[224:227], v[192:195], v[16:19]\n" \
    "s_barrier\n" \
    "s_bitcmp1_b32 s81, s75\n" \
    "s_cbranch_scc1 Lg80%=_sw\n" \
    "Lg80%=_swx:\n" \
    "v_mfma_f32_16x16x32_bf16 v[20:23], v[228:231], v[192:195], v[20:23]\n" \
    "ds_read_b64_tr_b16 v[176:177], v242 offset:0\n" \
    "v_mfma_f32_16x16x32_bf16 v[24:27], v[232:235], v[192:195], v[24:27]\n" \
    "s_add_u32 m0, s74, s79\n" \
    "s_mul_i32 s72, s73, s70\n" \
    "v_mfma_f32_16x16x32_bf16 v[28:31], v[236:239], v[192:195], v[28:31]\n" \
    "ds_read_b64_tr_b16 v[178:179], v242 offset:2048\n" \
    "buffer_load_dwordx4 v14, s[44:47], s72 offen lds\n" \
    "v_mfma_f32_16x16x32_bf16 v[32:35], v[224:227], v[196:199], v[32:35]\n" \
    "ds_read_b64_tr_b16 v[180:181], v243 offset:0\n" \
    "v_mfma_f32_16x16x32_bf16 v[36:39], v[228:231], v[196:199], v[36:39]\n" \
    "ds_read_b64_tr_b16 v[182:183], v243 offset:2048\n" \
    "v_mfma_f32_16x16x32_bf16 v[40:43], v[232:235], v[196:199], v[40:43]\n" \
    "s_add_u32 m0, m0, 1024\n" \
    "s_add_u32 s72, s72, s68\n" \
    "v_mfma_f32_16x16x32_bf16 v[44:47], v[236:239], v[196:199], v[44:47]\n" \
    "ds_read_b64_tr_b16 v[184:185], v244 offset:0\n" \
    "buffer_load_dwordx4 v14, s[44:47], s72 offen lds\n" \
    "v_mfma_f32_16x16x32_bf16 v[48:51], v[224:227], v[200:203], v[48:51]\n" \
    "ds_read_b64_tr_b16 v[186:187], v244 offset:2048\n" \
    "v_mfma_f32_16x16x32_bf16 v[52:55], v[228:231], v[200:203], v[52:55]\n" \
    "ds_read_b64_tr_b16 v[188:189], v245 offset:0\n" \
    "v_mfma_f32_16x16x32_bf16 v[56:59], v[232:235], v[200:203], v[56:59]\n" \
    "ds_read_b64_tr_b16 v[190:191], v245 offset:2048\n" \
    "v_mfma_f32_16x16x32_bf16 v[60:63], v[236:239], v[200:203], v[60:63]\n" \
    "s_add_u32 m0, m0, 1024\n" \
    "s_sub_u32 s72, s72, s69\n" \
    "v_mfma_f32_16x16x32_bf16 v[64:67], v[224:227], v[204:207], v[64:67]\n" \
    "ds_read_b128 v[144:147], v240 offset:0\n" \
    "buffer_load_dwordx4 v14, s[44:47], s72 offen lds\n" \
    "v_mfma_f32_16x16x32_bf16 v[68:71], v[228:231], v[204:207], v[68:71]\n" \
    "ds_read_b128 v[148:151], v240 offset:2048\n" \
    "v_mfma_f32_16x16x32_bf16 v[72:75], v[232:235], v[204:207], v[72:75]\n" \
    "ds_read_b128 v[152:155], v240 offset:4096\n" \
    "v_mfma_f32_16x16x32_bf16 v[76:79], v[236:239], v[204:207], v[76:79]\n" \
    "ds_read_b128 v[156:159], v240 offset:6144\n" \
    "v_mfma_f32_16x16x32_bf16 v[80:83], v[224:227], v[208:211], v[80:83]\n" \
    "s_add_u32 m0, m0, 1024\n" \
    "s_add_u32 s72, s72, s68\n" \
    "v_mfma_f32_16x16x32_bf16 v[84:87], v[228:231], v[208:211], v[84:87]\n" \
    "ds_read_b128 v[160:163], v240 offset:8192\n" \
    "buffer_load_dwordx4 v14, s[44:47], s72 offen lds\n" \
    "v_mfma_f32_16x16x32_bf16 v[88:91], v[232:235], v[208:211], v[88:91]\n" \
    "ds_read_b128 v[164:167], v240 offset:10240\n" \
    "v_mfma_f32_16x16x32_bf16 v[92:95], v[236:239], v[208:211], v[92:95]\n" \
    "ds_read_b128 v[168:171], v240 offset:12288\n" \
    "v_mfma_f32_16x16x32_bf16 v[96:99], v[224:227], v[212:215], v[96:99]\n" \
    "ds_read_b128 v[172:175], v240 offset:14336\n" \
    "v_mfma_f32_16x16x32_bf16 v[100:103], v[228:231], v[212:215], v[100:103]\n" \
    "s_add_u32 m0, s74, s78\n" \
    "s_mov_b32 s71, s73\n" \
    "v_mfma_f32_16x16x32_bf16 v[104:107], v[232:235], v[212:215], v[104:107]\n" \
    "buffer_load_dwordx4 v246, s[40:43], s71 offen sc1 lds\n" \
    "s_add_u32 s73, s73, 128\n" \
    "v_mfma_f32_16x16x32_bf16 v[108:111], v[236:239], v[212:215], v[108:111]\n" \
    "v_mfma_f32_16x16x32_bf16 v[112:115], v[224:227], v[216:219], v[112:115]\n" \
    "v_mfma_f32_16x16x32_bf16 v[116:119], v[228:231], v[216:219], v[116:119]\n" \
    "v_mfma_f32_16x16x32_bf16 v[120:123], v[232:235], v[216:219], v[120:123]\n" \
    "s_add_u32 m0, m0, 4096\n" \
    "s_add_u32 s71, s71, 65536\n" \
    "v_mfma_f32_16x16x32_bf16 v[124:127], v[236:239], v[216:219], v[124:127]\n" \
    "buffer_load_dwordx4 v246, s[40:43], s71 offen sc1 lds\n" \
    "s_and_b32 s73, s73, 2047\n" \
    "v_mfma_f32_16x16x32_bf16 v[128:131], v[224:227], v[220:223], v[128:131]\n" \
    "v_mfma_f32_16x16x32_bf16 v[132:135], v[228:231], v[220:223], v[132:135]\n" \
    "s_add_u32 s74, s74, s76\n" \
    "s_sub_u32 s75, s75, 1\n" \
    "v_mfma_f32_16x16x32_bf16 v[136:139], v[232:235], v[220:223], v[136:139]\n" \
    "v_mfma_f32_16x16x32_bf16 v[140:143], v[236:239], v[220:223], v[140:143]\n" \
    "s_cmp_lg_u32 s75, 0\n" \
    "s_cbranch_scc1 Lg80_p1%=\n" \
    "s_and_b32 s80, s62, 12\n" \
    "s_cbranch_scc1 Lg8_sp%=\n" \
    "Lg8_pk%=:\n" \
    "v_cvt_pk_bf16_f32 v192, v16, v17\n" \
    "v_cvt_pk_bf16_f32 v193, v18, v19\n" \
    "v_cvt_pk_bf16_f32 v194, v20, v21\n" \
    "v_cvt_pk_bf16_f32 v195, v22, v23\n" \
    "v_accvgpr_write_b32 a192, v192\n" \
    "v_accvgpr_write_b32 a193, v193\n" \
    "v_accvgpr_write_b32 a194, v194\n" \
    "v_accvgpr_write_b32 a195, v195\n" \
    "v_cvt_pk_bf16_f32 v196, v24, v25\n" \
    "v_cvt_pk_bf16_f32 v197, v26, v27\n" \
    "v_cvt_pk_bf16_f32 v198, v28, v29\n" \
    "v_cvt_pk_bf16_f32 v199, v30, v31\n" \
    "v_accvgpr_write_b32 a196, v196\n" \
    "v_accvgpr_write_b32 a197, v197\n" \
    "v_accvgpr_write_b32 a198, v198\n" \
    "v_accvgpr_write_b32 a199, v199\n" \
    "v_cvt_pk_bf16_f32 v200, v32, v33\n" \
    "v_cvt_pk_bf16_f32 v201, v34, v35\n" \
    "v_cvt_pk_bf16_f32 v202, v36, v37\n" \
    "v_cvt_pk_bf16_f32 v203, v38, v39\n" \
    "v_accvgpr_write_b32 a200, v200\n" \
    "v_accvgpr_write_b32 a201, v201\n" \
    "v_accvgpr_write_b32 a202, v202\n" \
    "v_accvgpr_write_b32 a203, v203\n" \
    "v_cvt_pk_bf16_f32 v204, v40, v41\n" \
    "v_cvt_pk_bf16_f32 v205, v42, v43\n" \
    "v_cvt_pk_bf16_f32 v206, v44, v45\n" \
    "v_cvt_pk_bf16_f32 v207, v46, v47\n" \
    "v_accvgpr_write_b32 a204, v204\n" \
    "v_accvgpr_write_b32 a205, v205\n" \
    "v_accvgpr_write_b32 a206, v206\n" \
    "v_accvgpr_write_b32 a207, v207\n" \
    "v_cvt_pk_bf16_f32 v208, v48, v49\n" \
    "v_cvt_pk_bf16_f32 v209, v50, v51\n" \
    "v_cvt_pk_bf16_f32 v210, v52, v53\n" \
    "v_cvt_pk_bf16_f32 v211, v54, v55\n" \
    "v_accvgpr_write_b32 a208, v208\n" \
    "v_accvgpr_write_b32 a209, v209\n" \
    "v_accvgpr_write_b32 a210, v210\n" \
    "v_accvgpr_write_b32 a211, v211\n" \
    "v_cvt_pk_bf16_f32 v212, v56, v57\n" \
    "v_cvt_pk_bf16_f32 v213, v58, v59\n" \
    "v_cvt_pk_bf16_f32 v214, v60, v61\n" \
    "v_cvt_pk_bf16_f32 v215, v62, v63\n" \
    "v_accvgpr_write_b32 a212, v212\n" \
    "v_accvgpr_write_b32 a213, v213\n" \
    "v_accvgpr_write_b32 a214, v214\n" \
    "v_accvgpr_write_b32 a215, v215\n" \
    "v_cvt_pk_bf16_f32 v216, v64, v65\n" \
    "v_cvt_pk_bf16_f32 v217, v66, v67\n" \
    "v_cvt_pk_bf16_f32 v218, v68, v69\n" \
    "v_cvt_pk_bf16_f32 v219, v70, v71\n" \
    "v_accvgpr_write_b32 a216, v216\n" \
    "v_accvgpr_write_b32 a217, v217\n" \
    "v_accvgpr_write_b32 a218, v218\n" \
    "v_accvgpr_write_b32 a219, v219\n" \
    "v_cvt_pk_bf16_f32 v220, v72, v73\n" \
    "v_cvt_pk_bf16_f32 v221, v74, v75\n" \
    "v_cvt_pk_bf16_f32 v222, v76, v77\n" \
    "v_cvt_pk_bf16_f32 v223, v78, v79\n" \
    "v_accvgpr_write_b32 a220, v220\n" \
    "v_accvgpr_write_b32 a221, v221\n" \
    "v_accvgpr_write_b32 a222, v222\n" \
    "v_accvgpr_write_b32 a223, v223\n" \
    "v_cvt_pk_bf16_f32 v192, v80, v81\n" \
    "v_cvt_pk_bf16_f32 v193, v82, v83\n" \
    "v_cvt_pk_bf16_f32 v194, v84, v85\n" \
    "v_cvt_pk_bf16_f32 v195, v86, v87\n" \
    "v_accvgpr_write_b32 a224, v192\n" \
    "v_accvgpr_write_b32 a225, v193\n" \
    "v_accvgpr_write_b32 a226, v194\n" \
    "v_accvgpr_write_b32 a227, v195\n" \
    "v_cvt_pk_bf16_f32 v196, v88, v89\n" \
    "v_cvt_pk_bf16_f32 v197, v90, v91\n" \
    "v_cvt_pk_bf16_f32 v198, v92, v93\n" \
    "v_cvt_pk_bf16_f32 v199, v94, v95\n" \
    "v_accvgpr_write_b32 a228, v196\n" \
    "v_accvgpr_write_b32 a229, v197\n" \
    "v_accvgpr_write_b32 a230, v198\n" \
    "v_accvgpr_write_b32 a231, v199\n" \
    "v_cvt_pk_bf16_f32 v200, v96, v97\n" \
    "v_cvt_pk_bf16_f32 v201, v98, v99\n" \
    "v_cvt_pk_bf16_f32 v202, v100, v101\n" \
    "v_cvt_pk_bf16_f32 v203, v102, v103\n" \
    "v_accvgpr_write_b32 a232, v200\n" \
    "v_accvgpr_write_b32 a233, v201\n" \
    "v_accvgpr_write_b32 a234, v202\n" \
    "v_accvgpr_write_b32 a235, v203\n" \
    "v_cvt_pk_bf16_f32 v204, v104, v105\n" \
    "v_cvt_pk_bf16_f32 v205, v106, v107\n" \
    "v_cvt_pk_bf16_f32 v206, v108, v109\n" \
    "v_cvt_pk_bf16_f32 v207, v110, v111\n" \
    "v_accvgpr_write_b32 a236, v204\n" \
    "v_accvgpr_write_b32 a237, v205\n" \
    "v_accvgpr_write_b32 a238, v206\n" \
    "v_accvgpr_write_b32 a239, v207\n" \
    "v_cvt_pk_bf16_f32 v208, v112, v113\n" \
    "v_cvt_pk_bf16_f32 v209, v114, v115\n" \
    "v_cvt_pk_bf16_f32 v210, v116, v117\n" \
    "v_cvt_pk_bf16_f32 v211, v118, v119\n" \
    "v_accvgpr_write_b32 a240, v208\n" \
    "v_accvgpr_write_b32 a241, v209\n" \
    "v_accvgpr_write_b32 a242, v210\n" \
    "v_accvgpr_write_b32 a243, v211\n" \
    "v_cvt_pk_bf16_f32 v212, v120, v121\n" \
    "v_cvt_pk_bf16_f32 v213, v122, v123\n" \
    "v_cvt_pk_bf16_f32 v214, v124, v125\n" \
    "v_cvt_pk_bf16_f32 v215, v126, v127\n" \
    "v_accvgpr_write_b32 a244, v212\n" \
    "v_accvgpr_write_b32 a245, v213\n" \
    "v_accvgpr_write_b32 a246, v214\n" \
    "v_accvgpr_write_b32 a247, v215\n" \
    "v_cvt_pk_bf16_f32 v216, v128, v129\n" \
    "v_cvt_pk_bf16_f32 v217, v130, v131\n" \
    "v_cvt_pk_bf16_f32 v218, v132, v133\n" \
    "v_cvt_pk_bf16_f32 v219, v134, v135\n" \
    "v_accvgpr_write_b32 a248, v216\n" \
    "v_accvgpr_write_b32 a249, v217\n" \
    "v_accvgpr_write_b32 a250, v218\n" \
    "v_accvgpr_write_b32 a251, v219\n" \
    "v_cvt_pk_bf16_f32 v220, v136, v137\n" \
    "v_cvt_pk_bf16_f32 v221, v138, v139\n" \
    "v_cvt_pk_bf16_f32 v222, v140, v141\n" \
    "v_cvt_pk_bf16_f32 v223, v142, v143\n" \
    "v_accvgpr_write_b32 a252, v220\n" \
    "v_accvgpr_write_b32 a253, v221\n" \
    "v_accvgpr_write_b32 a254, v222\n" \
    "v_accvgpr_write_b32 a255, v223\n" \
    "s_branch Lg8_end%=\n" \
    "Lg8_sp%=:\n" \
    "s_nop 7\n" \
    "s_bitcmp1_b32 s62, 2\n" \
    "s_cbranch_scc1 Lg8_rm%=\n" \
    "s_mov_b32 s82, 0x3e8293ee\n" \
    "s_mov_b32 s83, 0x3e8293ee\n" \
    "v_pk_mul_f32 v[16:17], v[16:17], s[82:83]\n" \
    "v_pk_mul_f32 v[18:19], v[18:19], s[82:83]\n" \
    "v_pk_mul_f32 v[20:21], v[20:21], s[82:83]\n" \
    "v_pk_mul_f32 v[22:23], v[22:23], s[82:83]\n" \
    "v_pk_mul_f32 v[24:25], v[24:25], s[82:83]\n" \
    "v_pk_mul_f32 v[26:27], v[26:27], s[82:83]\n" \
    "v_pk_mul_f32 v[28:29], v[28:29], s[82:83]\n" \
    "v_pk_mul_f32 v[30:31], v[30:31], s[82:83]\n" \
    "v_pk_mul_f32 v[32:33], v[32:33], s[82:83]\n" \
    "v_pk_mul_f32 v[34:35], v[34:35], s[82:83]\n" \
    "v_pk_mul_f32 v[36:37], v[36:37], s[82:83]\n" \
    "v_pk_mul_f32 v[38:39], v[38:39], s[82:83]\n" \
    "v_pk_mul_f32 v[40:41], v[40:41], s[82:83]\n" \
    "v_pk_mul_f32 v[42:43], v[42:43], s[82:83]\n" \
    "v_pk_mul_f32 v[44:45], v[44:45], s[82:83]\n" \
    "v_pk_mul_f32 v[46:47], v[46:47], s[82:83]\n" \
    "v_pk_mul_f32 v[48:49], v[48:49], s[82:83]\n" \
    "v_pk_mul_f32 v[50:51], v[50:51], s[82:83]\n" \
    "v_pk_mul_f32 v[52:53], v[52:53], s[82:83]\n" \
    "v_pk_mul_f32 v[54:55], v[54:55], s[82:83]\n" \
    "v_pk_mul_f32 v[56:57], v[56:57], s[82:83]\n" \
    "v_pk_mul_f32 v[58:59], v[58:59], s[82:83]\n" \
    "v_pk_mul_f32 v[60:61], v[60:61], s[82:83]\n" \
    "v_pk_mul_f32 v[62:63], v[62:63], s[82:83]\n" \
    "v_pk_mul_f32 v[64:65], v[64:65], s[82:83]\n" \
    "v_pk_mul_f32 v[66:67], v[66:67], s[82:83]\n" \
    "v_pk_mul_f32 v[68:69], v[68:69], s[82:83]\n" \
    "v_pk_mul_f32 v[70:71], v[70:71], s[82:83]\n" \
    "v_pk_mul_f32 v[72:73], v[72:73], s[82:83]\n" \
    "v_pk_mul_f32 v[74:75], v[74:75], s[82:83]\n" \
    "v_pk_mul_f32 v[76:77], v[76:77], s[82:83]\n" \
    "v_pk_mul_f32 v[78:79], v[78:79], s[82:83]\n" \
    "v_pk_mul_f32 v[80:81], v[80:81], s[82:83]\n" \
    "v_pk_mul_f32 v[82:83], v[82:83], s[82:83]\n" \
    "v_pk_mul_f32 v[84:85], v[84:85], s[82:83]\n" \
    "v_pk_mul_f32 v[86:87], v[86:87], s[82:83]\n" \
    "v_pk_mul_f32 v[88:89], v[88:89], s[82:83]\n" \
    "v_pk_mul_f32 v[90:91], v[90:91], s[82:83]\n" \
    "v_pk_mul_f32 v[92:93], v[92:93], s[82:83]\n" \
    "v_pk_mul_f32 v[94:95], v[94:95], s[82:83]\n" \
    "v_pk_mul_f32 v[96:97], v[96:97], s[82:83]\n" \
    "v_pk_mul_f32 v[98:99], v[98:99], s[82:83]\n" \
    "v_pk_mul_f32 v[100:101], v[100:101], s[82:83]\n" \
    "v_pk_mul_f32 v[102:103], v[102:103], s[82:83]\n" \
    "v_pk_mul_f32 v[104:105], v[104:105], s[82:83]\n" \
    "v_pk_mul_f32 v[106:107], v[106:107], s[82:83]\n" \
    "v_pk_mul_f32 v[108:109], v[108:109], s[82:83]\n" \
    "v_pk_mul_f32 v[110:111], v[110:111], s[82:83]\n" \
    "v_pk_mul_f32 v[112:113], v[112:113], s[82:83]\n" \
    "v_pk_mul_f32 v[114:115], v[114:115], s[82:83]\n" \
    "v_pk_mul_f32 v[116:117], v[116:117], s[82:83]\n" \
    "v_pk_mul_f32 v[118:119], v[118:119], s[82:83]\n" \
    "v_pk_mul_f32 v[120:121], v[120:121], s[82:83]\n" \
    "v_pk_mul_f32 v[122:123], v[122:123], s[82:83]\n" \
    "v_pk_mul_f32 v[124:125], v[124:125], s[82:83]\n" \
    "v_pk_mul_f32 v[126:127], v[126:127], s[82:83]\n" \
    "v_pk_mul_f32 v[128:129], v[128:129], s[82:83]\n" \
    "v_pk_mul_f32 v[130:131], v[130:131], s[82:83]\n" \
    "v_pk_mul_f32 v[132:133], v[132:133], s[82:83]\n" \
    "v_pk_mul_f32 v[134:135], v[134:135], s[82:83]\n" \
    "v_pk_mul_f32 v[136:137], v[136:137], s[82:83]\n" \
    "v_pk_mul_f32 v[138:139], v[138:139], s[82:83]\n" \
    "v_pk_mul_f32 v[140:141], v[140:141], s[82:83]\n" \
    "v_pk_mul_f32 v[142:143], v[142:143], s[82:83]\n" \
    "s_branch Lg8_pk%=\n" \
    "Lg8_rm%=:\n" \
    "v_cvt_pk_bf16_f32 v192, v16, v17\n" \
    "v_cvt_pk_bf16_f32 v193, v18, v19\n" \
    "v_cvt_pk_bf16_f32 v194, v20, v21\n" \
    "v_cvt_pk_bf16_f32 v195, v22, v23\n" \
    "s_nop 0\n" \
    "v_permlane16_swap_b32 v192, v194\n" \
    "v_permlane16_swap_b32 v193, v195\n" \
    "v_accvgpr_write_b32 a192, v192\n" \
    "v_accvgpr_write_b32 a193, v193\n" \
    "v_accvgpr_write_b32 a194, v194\n" \
    "v_accvgpr_write_b32 a195, v195\n" \
    "v_cvt_pk_bf16_f32 v196, v24, v25\n" \
    "v_cvt_pk_bf16_f32 v197, v26, v27\n" \
    "v_cvt_pk_bf16_f32 v198, v28, v29\n" \
    "v_cvt_pk_bf16_f32 v199, v30, v31\n" \
    "s_nop 0\n" \
    "v_permlane16_swap_b32 v196, v198\n" \
    "v_permlane16_swap_b32 v197, v199\n" \
    "v_accvgpr_write_b32 a196, v196\n" \
    "v_accvgpr_write_b32 a197, v197\n" \
    "v_accvgpr_write_b32 a198, v198\n" \
    "v_accvgpr_write_b32 a199, v199\n" \
    "v_cvt_pk_bf16_f32 v200, v32, v33\n" \
    "v_cvt_pk_bf16_f32 v201, v34, v35\n" \
    "v_cvt_pk_bf16_f32 v202, v36, v37\n" \
    "v_cvt_pk_bf16_f32 v203, v38, v39\n" \
    "s_nop 0\n" \
    "v_permlane16_swap_b32 v200, v202\n" \
    "v_permlane16_swap_b32 v201, v203\n" \
    "v_accvgpr_write_b32 a200, v200\n" \
    "v_accvgpr_write_b32 a201, v201\n" \
    "v_accvgpr_write_b32 a202, v202\n" \
    "v_accvgpr_write_b32 a203, v203\n" \
    "v_cvt_pk_bf16_f32 v204, v40, v41\n" \
    "v_cvt_pk_bf16_f32 v205, v42, v43\n" \
    "v_cvt_pk_bf16_f32 v206, v44, v45\n" \
    "v_cvt_pk_bf16_f32 v207, v46, v47\n" \
    "s_nop 0\n" \
    "v_permlane16_swap_b32 v204, v206\n" \
    "v_permlane16_swap_b32 v205, v207\n" \
    "v_accvgpr_write_b32 a204, v204\n" \
    "v_accvgpr_write_b32 a205, v205\n" \
    "v_accvgpr_write_b32 a206, v206\n" \
    "v_accvgpr_write_b32 a207, v207\n" \
    "v_cvt_pk_bf16_f32 v208, v48, v49\n" \
    "v_cvt_pk_bf16_f32 v209, v50, v51\n" \
    "v_cvt_pk_bf16_f32 v210, v52, v53\n" \
    "v_cvt_pk_bf16_f32 v211, v54, v55\n" \
    "s_nop 0\n" \
    "v_permlane16_swap_b32 v208, v210\n" \
    "v_permlane16_swap_b32 v209, v211\n" \
    "v_accvgpr_write_b32 a208, v208\n" \
    "v_accvgpr_write_b32 a209, v209\n" \
    "v_accvgpr_write_b32 a210, v210\n" \
    "v_accvgpr_write_b32 a211, v211\n" \
    "v_cvt_pk_bf16_f32 v212, v56, v57\n" \
    "v_cvt_pk_bf16_f32 v213, v58, v59\n" \
    "v_cvt_pk_bf16_f32 v214, v60, v61\n" \
    "v_cvt_pk_bf16_f32 v215, v62, v63\n" \
    "s_nop 0\n" \
    "v_permlane16_swap_b32 v212, v214\n" \
    "v_permlane16_swap_b32 v213, v215\n" \
    "v_accvgpr_write_b32 a212, v212\n" \
    "v_accvgpr_write_b32 a213, v213\n" \
    "v_accvgpr_write_b32 a214, v214\n" \
    "v_accvgpr_write_b32 a215, v215\n" \
    "v_cvt_pk_bf16_f32 v216, v64, v65\n" \
    "v_cvt_pk_bf16_f32 v217, v66, v67\n" \
    "v_cvt_pk_bf16_f32 v218, v68, v69\n" \
    "v_cvt_pk_bf16_f32 v219, v70, v71\n" \
    "s_nop 0\n" \
    "v_permlane16_swap_b32 v216, v218\n" \
    "v_permlane16_swap_b32 v217, v219\n" \
    "v_accvgpr_write_b32 a216, v216\n" \
    "v_accvgpr_write_b32 a217, v217\n" \
    "v_accvgpr_write_b32 a218, v218\n" \
    "v_accvgpr_write_b32 a219, v219\n" \
    "v_cvt_pk_bf16_f32 v220, v72, v73\n" \
    "v_cvt_pk_bf16_f32 v221, v74, v75\n" \
    "v_cvt_pk_bf16_f32 v222, v76, v77\n" \
    "v_cvt_pk_bf16_f32 v223, v78, v79\n" \
    "s_nop 0\n" \
    "v_permlane16_swap_b32 v220, v222\n" \
    "v_permlane16_swap_b32 v221, v223\n" \
    "v_accvgpr_write_b32 a220, v220\n" \
    "v_accvgpr_write_b32 a221, v221\n" \
    "v_accvgpr_write_b32 a222, v222\n" \
    "v_accvgpr_write_b32 a223, v223\n" \
    "v_cvt_pk_bf16_f32 v192, v80, v81\n" \
    "v_cvt_pk_bf16_f32 v193, v82, v83\n" \
    "v_cvt_pk_bf16_f32 v194, v84, v85\n" \
    "v_cvt_pk_bf16_f32 v195, v86, v87\n" \
    "s_nop 0\n" \
    "v_permlane16_swap_b32 v192, v194\n" \
    "v_permlane16_swap_b32 v193, v195\n" \
    "v_accvgpr_write_b32 a224, v192\n" \
    "v_accvgpr_write_b32 a225, v193\n" \
    "v_accvgpr_write_b32 a226, v194\n" \
    "v_accvgpr_write_b32 a227, v195\n" \
    "v_cvt_pk_bf16_f32 v196, v88, v89\n" \
    "v_cvt_pk_bf16_f32 v197, v90, v91\n" \
    "v_cvt_pk_bf16_f32 v198, v92, v93\n" \
    "v_cvt_pk_bf16_f32 v199, v94, v95\n" \
    "s_nop 0\n" \
    "v_permlane16_swap_b32 v196, v198\n" \
    "v_permlane16_swap_b32 v197, v199\n" \
    "v_accvgpr_write_b32 a228, v196\n" \
    "v_accvgpr_write_b32 a229, v197\n" \
    "v_accvgpr_write_b32 a230, v198\n" \
    "v_accvgpr_write_b32 a231, v199\n" \
    "v_cvt_pk_bf16_f32 v200, v96, v97\n" \
    "v_cvt_pk_bf16_f32 v201, v98, v99\n" \
    "v_cvt_pk_bf16_f32 v202, v100, v101\n" \
    "v_cvt_pk_bf16_f32 v203, v102, v103\n" \
    "s_nop 0\n" \
    "v_permlane16_swap_b32 v200, v202\n" \
    "v_permlane16_swap_b32 v201, v203\n" \
    "v_accvgpr_write_b32 a232, v200\n" \
    "v_accvgpr_write_b32 a233, v201\n" \
    "v_accvgpr_write_b32 a234, v202\n" \
    "v_accvgpr_write_b32 a235, v203\n" \
    "v_cvt_pk_bf16_f32 v204, v104, v105\n" \
    "v_cvt_pk_bf16_f32 v205, v106, v107\n" \
    "v_cvt_pk_bf16_f32 v206, v108, v109\n" \
    "v_cvt_pk_bf16_f32 v207, v110, v111\n" \
    "s_nop 0\n" \
    "v_permlane16_swap_b32 v204, v206\n" \
    "v_permlane16_swap_b32 v205, v207\n" \
    "v_accvgpr_write_b32 a236, v204\n" \
    "v_accvgpr_write_b32 a237, v205\n" \
    "v_accvgpr_write_b32 a238, v206\n" \
    "v_accvgpr_write_b32 a239, v207\n" \
    "v_cvt_pk_bf16_f32 v208, v112, v113\n" \
    "v_cvt_pk_bf16_f32 v209, v114, v115\n" \
    "v_cvt_pk_bf16_f32 v210, v116, v117\n" \
    "v_cvt_pk_bf16_f32 v211, v118, v119\n" \
    "s_nop 0\n" \
    "v_permlane16_swap_b32 v208, v210\n" \
    "v_permlane16_swap_b32 v209, v211\n" \
    "v_accvgpr_write_b32 a240, v208\n" \
    "v_accvgpr_write_b32 a241, v209\n" \
    "v_accvgpr_write_b32 a242, v210\n" \
    "v_accvgpr_write_b32 a243, v211\n" \
    "v_cvt_pk_bf16_f32 v212, v120, v121\n" \
    "v_cvt_pk_bf16_f32 v213, v122, v123\n" \
    "v_cvt_pk_bf16_f32 v214, v124, v125\n" \
    "v_cvt_pk_bf16_f32 v215, v126, v127\n" \
    "s_nop 0\n" \
    "v_permlane16_swap_b32 v212, v214\n" \
    "v_permlane16_swap_b32 v213, v215\n" \
    "v_accvgpr_write_b32 a244, v212\n" \
    "v_accvgpr_write_b32 a245, v213\n" \
    "v_accvgpr_write_b32 a246, v214\n" \
    "v_accvgpr_write_b32 a247, v215\n" \
    "v_cvt_pk_bf16_f32 v216, v128, v129\n" \
    "v_cvt_pk_bf16_f32 v217, v130, v131\n" \
    "v_cvt_pk_bf16_f32 v218, v132, v133\n" \
    "v_cvt_pk_bf16_f32 v219, v134, v135\n" \
    "s_nop 0\n" \
    "v_permlane16_swap_b32 v216, v218\n" \
    "v_permlane16_swap_b32 v217, v219\n" \
    "v_accvgpr_write_b32 a248, v216\n" \
    "v_accvgpr_write_b32 a249, v217\n" \
    "v_accvgpr_write_b32 a250, v218\n" \
    "v_accvgpr_write_b32 a251, v219\n" \
    "v_cvt_pk_bf16_f32 v220, v136, v137\n" \
    "v_cvt_pk_bf16_f32 v221, v138, v139\n" \
    "v_cvt_pk_bf16_f32 v222, v140, v141\n" \
    "v_cvt_pk_bf16_f32 v223, v142, v143\n" \
    "s_nop 0\n" \
    "v_permlane16_swap_b32 v220, v222\n" \
    "v_permlane16_swap_b32 v221, v223\n" \
    "v_accvgpr_write_b32 a252, v220\n" \
    "v_accvgpr_write_b32 a253, v221\n" \
    "v_accvgpr_write_b32 a254, v222\n" \
    "v_accvgpr_write_b32 a255, v223\n" \
    "s_branch Lg8_end%=\n" \
    "Lg80%=_sw:\n" \
    "s_cmp_eq_u32 s75, 3\n" \
    "s_cbranch_scc1 Lg80%=_sw2\n" \
    "buffer_atomic_add v252, v251, s[56:59], s67 offen\n" \
    "s_branch Lg80%=_swx\n" \
    "Lg80%=_sw2:\n" \
    "v_readlane_b32 s40, v255, 0\n" \
    "v_readlane_b32 s41, v255, 1\n" \
    "v_readlane_b32 s42, v255, 2\n" \
    "v_readlane_b32 s44, v255, 3\n" \
    "v_readlane_b32 s45, v255, 4\n" \
    "v_readlane_b32 s46, v255, 5\n" \
    "v_readlane_b32 s61, v255, 6\n" \
    "v_readlane_b32 s73, v255, 7\n" \
    "s_lshr_b32 s73, s73, 16\n" \
    "s_lshl_b32 s68, s61, 4\n" \
    "s_mul_i32 s69, s61, 12\n" \
    "s_lshr_b32 s70, s61, 1\n" \
    "v_mad_u32_u24 v14, v247, s61, v248\n" \
    "s_nop 0\n" \
    "s_bitcmp0_b32 s62, 7\n" \
    "s_cselect_b32 s42, s42, 0\n" \
    "s_cselect_b32 s46, s46, 0\n" \
    "s_branch Lg80%=_swx\n" \
    "Lg81_in%=:\n" \
    "s_waitcnt lgkmcnt(0)\n" \
    "v_mfma_f32_16x16x32_bf16 v[16:19], v[144:147], v[176:179], 0\n" \
    "ds_read_b64_tr_b16 v[224:225], v242 offset:4096\n" \
    "v_mfma_f32_16x16x32_bf16 v[20:23], v[144:147], v[180:183], 0\n" \
    "s_add_u32 m0, m0, 4096\n" \
    "s_add_u32 s71, s71, 65536\n" \
    "v_mfma_f32_16x16x32_bf16 v[24:27], v[144:147], v[184:187], 0\n" \
    "ds_read_b64_tr_b16 v[226:227], v242 offset:6144\n" \
    "buffer_load_dwordx4 v246, s[40:43], s71 offen sc1 lds\n" \
    "v_mfma_f32_16x16x32_bf16 v[28:31], v[144:147], v[188:191], 0\n" \
    "ds_read_b64_tr_b16 v[228:229], v243 offset:4096\n" \
    "v_mfma_f32_16x16x32_bf16 v[32:35], v[148:151], v[176:179], 0\n" \
    "ds_read_b64_tr_b16 v[230:231], v243 offset:6144\n" \
    "v_mfma_f32_16x16x32_bf16 v[36:39], v[148:151], v[180:183], 0\n" \
    "ds_read_b64_tr_b16 v[232:233], v244 offset:4096\n" \
    "v_mfma_f32_16x16x32_bf16 v[40:43], v[148:151], v[184:187], 0\n" \
    "s_add_u32 m0, m0, 4096\n" \
    "s_add_u32 s71, s71, 65536\n" \
    "v_mfma_f32_16x16x32_bf16 v[44:47], v[148:151], v[188:191], 0\n" \
    "ds_read_b64_tr_b16 v[234:235], v244 offset:6144\n" \
    "buffer_load_dwordx4 v246, s[40:43], s71 offen sc1 lds\n" \
    "v_mfma_f32_16x16x32_bf16 v[48:51], v[152:155], v[176:179], 0\n" \
    "ds_read_b64_tr_b16 v[236:237], v245 offset:4096\n" \
    "v_mfma_f32_16x16x32_bf16 v[52:55], v[152:155], v[180:183], 0\n" \
    "ds_read_b64_tr_b16 v[238:239], v245 offset:6144\n" \
    "v_mfma_f32_16x16x32_bf16 v[56:59], v[152:155], v[184:187], 0\n" \
    "ds_read_b128 v[192:195], v241 offset:0\n" \
    "v_mfma_f32_16x16x32_bf16 v[60:63], v[152:155], v[188:191], 0\n" \
    "s_add_u32 m0, m0, 4096\n" \
    "s_add_u32 s71, s71, 65536\n" \
    "v_mfma_f32_16x16x32_bf16 v[64:67], v[156:159], v[176:179], 0\n" \
    "ds_read_b128 v[196:199], v241 offset:2048\n" \
    "buffer_load_dwordx4 v246, s[40:43], s71 offen sc1 lds\n" \
    "v_mfma_f32_16x16x32_bf16 v[68:71], v[156:159], v[180:183], 0\n" \
    "ds_read_b128 v[200:203], v241 offset:4096\n" \
    "v_mfma_f32_16x16x32_bf16 v[72:75], v[156:159], v[184:187], 0\n" \
    "ds_read_b128 v[204:207], v241 offset:6144\n" \
    "v_mfma_f32_16x16x32_bf16 v[76:79], v[156:159], v[188:191], 0\n" \
    "ds_read_b128 v[208:211], v241 offset:8192\n" \
    "v_mfma_f32_16x16x32_bf16 v[80:83], v[160:163], v[176:179], 0\n" \
    "s_add_u32 m0, m0, 4096\n" \
    "s_add_u32 s71, s71, 65536\n" \
    "v_mfma_f32_16x16x32_bf16 v[84:87], v[160:163], v[180:183], 0\n" \
    "ds_read_b128 v[212:215], v241 offset:10240\n" \
    "buffer_load_dwordx4 v246, s[40:43], s71 offen sc1 lds\n" \
    "v_mfma_f32_16x16x32_bf16 v[88:91], v[160:163], v[184:187], 0\n" \
    "ds_read_b128 v[216:219], v241 offset:12288\n" \
    "v_mfma_f32_16x16x32_bf16 v[92:95], v[160:163], v[188:191], 0\n" \
    "ds_read_b128 v[220:223], v241 offset:14336\n" \
    "v_mfma_f32_16x16x32_bf16 v[96:99], v[164:167], v[176:179], 0\n" \
    "s_cmp_eq_u32 s74, 98304\n" \
    "s_cselect_b32 s76, s77, 49152\n" \
    "v_mfma_f32_16x16x32_bf16 v[100:103], v[164:167], v[180:183], 0\n" \
    "s_add_u32 m0, m0, 4096\n" \
    "s_add_u32 s71, s71, 65536\n" \
    "v_mfma_f32_16x16x32_bf16 v[104:107], v[164:167], v[184:187], 0\n" \
    "buffer_load_dwordx4 v246, s[40:43], s71 offen sc1 lds\n" \
    "v_mfma_f32_16x16x32_bf16 v[108:111], v[164:167], v[188:191], 0\n" \
    "v_add_u32 v242, s76, v242\n" \
    "v_mfma_f32_16x16x32_bf16 v[112:115], v[168:171], v[176:179], 0\n" \
    "v_add_u32 v243, s76, v243\n" \
    "v_mfma_f32_16x16x32_bf16 v[116:119], v[168:171], v[180:183], 0\n" \
    "v_add_u32 v244, s76, v244\n" \
    "v_mfma_f32_16x16x32_bf16 v[120:123], v[168:171], v[184:187], 0\n" \
    "s_add_u32 m0, m0, 4096\n" \
    "s_add_u32 s71, s71, 65536\n" \
    "v_mfma_f32_16x16x32_bf16 v[124:127], v[168:171], v[188:191], 0\n" \
    "buffer_load_dwordx4 v246, s[40:43], s71 offen sc1 lds\n" \
    "v_mfma_f32_16x16x32_bf16 v[128:131], v[172:175], v[176:179], 0\n" \
    "v_add_u32 v245, s76, v245\n" \
    "v_mfma_f32_16x16x32_bf16 v[132:135], v[172:175], v[180:183], 0\n" \
    "v_add_u32 v240, s76, v240\n" \
    "v_mfma_f32_16x16x32_bf16 v[136:139], v[172:175], v[184:187], 0\n" \
    "v_add_u32 v241, s76, v241\n" \
    "v_mfma_f32_16x16x32_bf16 v[140:143], v[172:175], v[188:191], 0\n" \
    "s_waitcnt vmcnt(12) lgkmcnt(0)\n" \
    "v_mfma_f32_16x16x32_bf16 v[16:19], v[192:195], v[224:227], v[16:19]\n" \
    "s_barrier\n" \
    "v_mfma_f32_16x16x32_bf16 v[20:23], v[192:195], v[228:231], v[20:23]\n" \
    "ds_read_b64_tr_b16 v[176:177], v242 offset:0\n" \
    "v_mfma_f32_16x16x32_bf16 v[24:27], v[192:195], v[232:235], v[24:27]\n" \
    "s_add_u32 m0, s74, s79\n" \
    "s_mul_i32 s72, s73, s70\n" \
    "v_mfma_f32_16x16x32_bf16 v[28:31], v[192:195], v[236:239], v[28:31]\n" \
    "ds_read_b64_tr_b16 v[178:179], v242 offset:2048\n" \
    "buffer_load_dwordx4 v14, s[44:47], s72 offen lds\n" \
    "v_mfma_f32_16x16x32_bf16 v[32:35], v[196:199], v[224:227], v[32:35]\n" \
    "ds_read_b64_tr_b16 v[180:181], v243 offset:0\n" \
    "v_mfma_f32_16x16x32_bf16 v[36:39], v[196:199], v[228:231], v[36:39]\n" \
    "ds_read_b64_tr_b16 v[182:183], v243 offset:2048\n" \
    "v_mfma_f32_16x16x32_bf16 v[40:43], v[196:199], v[232:235], v[40:43]\n" \
    "s_add_u32 m0, m0, 1024\n" \
    "s_add_u32 s72, s72, s68\n" \
    "v_mfma_f32_16x16x32_bf16 v[44:47], v[196:199], v[236:239], v[44:47]\n" \
    "ds_read_b64_tr_b16 v[184:185], v244 offset:0\n" \
    "buffer_load_dwordx4 v14, s[44:47], s72 offen lds\n" \
    "v_mfma_f32_16x16x32_bf16 v[48:51], v[200:203], v[224:227], v[48:51]\n" \
    "ds_read_b64_tr_b16 v[186:187], v244 offset:2048\n" \
    "v_mfma_f32_16x16x32_bf16 v[52:55], v[200:203], v[228:231], v[52:55]\n" \
    "ds_read_b64_tr_b16 v[188:189], v245 offset:0\n" \
    "v_mfma_f32_16x16x32_bf16 v[56:59], v[200:203], v[232:235], v[56:59]\n" \
    "ds_read_b64_tr_b16 v[190:191], v245 offset:2048\n" \
    "v_mfma_f32_16x16x32_bf16 v[60:63], v[200:203], v[236:239], v[60:63]\n" \
    "s_add_u32 m0, m0, 1024\n" \
    "s_sub_u32 s72, s72, s69\n" \
    "v_mfma_f32_16x16x32_bf16 v[64:67], v[204:207], v[224:227], v[64:67]\n" \
    "ds_read_b128 v[144:147], v240 offset:0\n" \
    "buffer_load_dwordx4 v14, s[44:47], s72 offen lds\n" \
    "v_mfma_f32_16x16x32_bf16 v[68:71], v[204:207], v[228:231], v[68:71]\n" \
    "ds_read_b128 v[148:151], v240 offset:2048\n" \
    "v_mfma_f32_16x16x32_bf16 v[72:75], v[204:207], v[232:235], v[72:75]\n" \
    "ds_read_b128 v[152:155], v240 offset:4096\n" \
    "v_mfma_f32_16x16x32_bf16 v[76:79], v[204:207], v[236:239], v[76:79]\n" \
    "ds_read_b128 v[156:159], v240 offset:6144\n" \
    "v_mfma_f32_16x16x32_bf16 v[80:83], v[208:211], v[224:227], v[80:83]\n" \
    "s_add_u32 m0, m0, 1024\n" \
    "s_add_u32 s72, s72, s68\n" \
    "v_mfma_f32_16x16x32_bf16 v[84:87], v[208:211], v[228:231], v[84:87]\n" \
    "ds_read_b128 v[160:163], v240 offset:8192\n" \
    "buffer_load_dwordx4 v14, s[44:47], s72 offen lds\n" \
    "v_mfma_f32_16x16x32_bf16 v[88:91], v[208:211], v[232:235], v[88:91]\n" \
    "ds_read_b128 v[164:167], v240 offset:10240\n" \
    "v_mfma_f32_16x16x32_bf16 v[92:95], v[208:211], v[236:239], v[92:95]\n" \
    "ds_read_b128 v[168:171], v240 offset:12288\n" \
    "v_mfma_f32_16x16x32_bf16 v[96:99], v[212:215], v[224:227], v[96:99]\n" \
    "ds_read_b128 v[172:175], v240 offset:14336\n" \
    "v_mfma_f32_16x16x32_bf16 v[100:103], v[212:215], v[228:231], v[100:103]\n" \
    "s_add_u32 m0, s74, s78\n" \
    "s_mov_b32 s71, s73\n" \
    "v_mfma_f32_16x16x32_bf16 v[104:107], v[212:215], v[232:235], v[104:107]\n" \
    "buffer_load_dwordx4 v246, s[40:43], s71 offen sc1 lds\n" \
    "s_add_u32 s73, s73, 128\n" \
    "v_mfma_f32_16x16x32_bf16 v[108:111], v[212:215], v[236:239], v[108:111]\n" \
    "buffer_store_dwordx4 a[192:195], v15, s[48:51], s66 offen sc1\n" \
    "s_add_u32 s80, s66, s65\n" \
    "v_mfma_f32_16x16x32_bf16 v[112:115], v[216:219], v[224:227], v[112:115]\n" \
    "v_mfma_f32_16x16x32_bf16 v[116:119], v[216:219], v[228:231], v[116:119]\n" \
    "v_mfma_f32_16x16x32_bf16 v[120:123], v[216:219], v[232:235], v[120:123]\n" \
    "s_add_u32 m0, m0, 4096\n" \
    "s_add_u32 s71, s71, 65536\n" \
    "v_mfma_f32_16x16x32_bf16 v[124:127], v[216:219], v[236:239], v[124:127]\n" \
    "buffer_load_dwordx4 v246, s[40:43], s71 offen sc1 lds\n" \
    "s_and_b32 s73, s73, 2047\n" \
    "v_mfma_f32_16x16x32_bf16 v[128:131], v[220:223], v[224:227], v[128:131]\n" \
    "buffer_store_dwordx4 a[196:199], v15, s[48:51], s80 offen sc1\n" \
    "s_add_u32 s80, s66, s64\n" \
    "v_mfma_f32_16x16x32_bf16 v[132:135], v[220:223], v[228:231], v[132:135]\n" \
    "s_add_u32 s74, s74, s76\n" \
    "s_sub_u32 s75, s75, 1\n" \
    "v_mfma_f32_16x16x32_bf16 v[136:139], v[220:223], v[232:235], v[136:139]\n" \
    "v_mfma_f32_16x16x32_bf16 v[140:143], v[220:223], v[236:239], v[140:143]\n" \
    "s_cmp_lg_u32 s75, 0\n" \
    "s_waitcnt lgkmcnt(0)\n" \
    "v_mfma_f32_16x16x32_bf16 v[16:19], v[144:147], v[176:179], v[16:19]\n" \
    "ds_read_b64_tr_b16 v[224:225], v242 offset:4096\n" \
    "v_mfma_f32_16x16x32_bf16 v[20:23], v[144:147], v[180:183], v[20:23]\n" \
    "s_add_u32 m0, m0, 4096\n" \
    "s_add_u32 s71, s71, 65536\n" \
    "v_mfma_f32_16x16x32_bf16 v[24:27], v[144:147], v[184:187], v[24:27]\n" \
    "ds_read_b64_tr_b16 v[226:227], v242 offset:6144\n" \
    "buffer_load_dwordx4 v246, s[40:43], s71 offen sc1 lds\n" \
    "v_mfma_f32_16x16x32_bf16 v[28:31], v[144:147], v[188:191], v[28:31]\n" \
    "ds_read_b64_tr_b16 v[228:229], v243 offset:4096\n" \
    "v_mfma_f32_16x16x32_bf16 v[32:35], v[148:151], v[176:179], v[32:35]\n" \
    "ds_read_b64_tr_b16 v[230:231], v243 offset:6144\n" \
    "v_mfma_f32_16x16x32_bf16 v[36:39], v[148:151], v[180:183], v[36:39]\n" \
    "ds_read_b64_tr_b16 v[232:233], v244 offset:4096\n" \
    "v_mfma_f32_16x16x32_bf16 v[40:43], v[148:151], v[184:187], v[40:43]\n" \
    "s_add_u32 m0, m0, 4096\n" \
    "s_add_u32 s71, s71, 65536\n" \
    "v_mfma_f32_16x16x32_bf16 v[44:47], v[148:151], v[188:191], v[44:47]\n" \
    "ds_read_b64_tr_b16 v[234:235], v244 offset:6144\n" \
    "buffer_load_dwordx4 v246, s[40:43], s71 offen sc1 lds\n" \
    "v_mfma_f32_16x16x32_bf16 v[48:51], v[152:155], v[176:179], v[48:51]\n" \
    "ds_read_b64_tr_b16 v[236:237], v245 offset:4096\n" \
    "v_mfma_f32_16x16x32_bf16 v[52:55], v[152:155], v[180:183], v[52:55]\n" \
    "ds_read_b64_tr_b16 v[238:239], v245 offset:6144\n" \
    "v_mfma_f32_16x16x32_bf16 v[56:59], v[152:155], v[184:187], v[56:59]\n" \
    "ds_read_b128 v[192:195], v241 offset:0\n" \
    "v_mfma_f32_16x16x32_bf16 v[60:63], v[152:155], v[188:191], v[60:63]\n" \
    "s_add_u32 m0, m0, 4096\n" \
    "s_add_u32 s71, s71, 65536\n" \
    "v_mfma_f32_16x16x32_bf16 v[64:67], v[156:159], v[176:179], v[64:67]\n" \
    "ds_read_b128 v[196:199], v241 offset:2048\n" \
    "buffer_load_dwordx4 v246, s[40:43], s71 offen sc1 lds\n" \
    "v_mfma_f32_16x16x32_bf16 v[68:71], v[156:159], v[180:183], v[68:71]\n" \
    "ds_read_b128 v[200:203], v241 offset:4096\n" \
    "v_mfma_f32_16x16x32_bf16 v[72:75], v[156:159], v[184:187], v[72:75]\n" \
    "ds_read_b128 v[204:207], v241 offset:6144\n" \
    "v_mfma_f32_16x16x32_bf16 v[76:79], v[156:159], v[188:191], v[76:79]\n" \
    "ds_read_b128 v[208:211], v241 offset:8192\n" \
    "v_mfma_f32_16x16x32_bf16 v[80:83], v[160:163], v[176:179], v[80:83]\n" \
    "s_add_u32 m0, m0, 4096\n" \
    "s_add_u32 s71, s71, 65536\n" \
    "v_mfma_f32_16x16x32_bf16 v[84:87], v[160:163], v[180:183], v[84:87]\n" \
    "ds_read_b128 v[212:215], v241 offset:10240\n" \
    "buffer_load_dwordx4 v246, s[40:43], s71 offen sc1 lds\n" \
    "v_mfma_f32_16x16x32_bf16 v[88:91], v[160:163], v[184:187], v[88:91]\n" \
    "ds_read_b128 v[216:219], v241 offset:12288\n" \
    "v_mfma_f32_16x16x32_bf16 v[92:95], v[160:163], v[188:191], v[92:95]\n" \
    "ds_read_b128 v[220:223], v241 offset:14336\n" \
    "v_mfma_f32_16x16x32_bf16 v[96:99], v[164:167], v[176:179], v[96:99]\n" \
    "s_cmp_eq_u32 s74, 98304\n" \
    "s_cselect_b32 s76, s77, 49152\n" \
    "v_mfma_f32_16x16x32_bf16 v[100:103], v[164:167], v[180:183], v[100:103]\n" \
    "s_add_u32 m0, m0, 4096\n" \
    "s_add_u32 s71, s71, 65536\n" \
    "v_mfma_f32_16x16x32_bf16 v[104:107], v[164:167], v[184:187], v[104:107]\n" \
    "buffer_load_dwordx4 v246, s[40:43], s71 offen sc1 lds\n" \
    "v_mfma_f32_16x16x32_bf16 v[108:111], v[164:167], v[188:191], v[108:111]\n" \
    "v_add_u32 v242, s76, v242\n" \
    "v_mfma_f32_16x16x32_bf16 v[112:115], v[168:171], v[176:179], v[112:115]\n" \
    "v_add_u32 v243, s76, v243\n" \
    "v_mfma_f32_16x16x32_bf16 v[116:119], v[168:171], v[180:183], v[116:119]\n" \
    "v_add_u32 v244, s76, v244\n" \
    "v_mfma_f32_16x16x32_bf16 v[120:123], v[168:171], v[184:187], v[120:123]\n" \
    "s_add_u32 m0, m0, 4096\n" \
    "s_add_u32 s71, s71, 65536\n" \
    "v_mfma_f32_16x16x32_bf16 v[124:127], v[168:171], v[188:191], v[124:127]\n" \
    "buffer_load_dwordx4 v246, s[40:43], s71 offen sc1 lds\n" \
    "v_mfma_f32_16x16x32_bf16 v[128:131], v[172:175], v[176:179], v[128:131]\n" \
    "v_add_u32 v245, s76, v245\n" \
    "v_mfma_f32_16x16x32_bf16 v[132:135], v[172:175], v[180:183], v[132:135]\n" \
    "v_add_u32 v240, s76, v240\n" \
    "v_mfma_f32_16x16x32_bf16 v[136:139], v[172:175], v[184:187], v[136:139]\n" \
    "v_add_u32 v241, s76, v241\n" \
    "v_mfma_f32_16x16x32_bf16 v[140:143], v[172:175], v[188:191], v[140:143]\n" \
    "s_waitcnt vmcnt(14) lgkmcnt(0)\n" \
    "v_mfma_f32_16x16x32_bf16 v[16:19], v[192:195], v[224:227], v[16:19]\n" \
    "s_barrier\n" \
    "v_mfma_f32_16x16x32_bf16 v[20:23], v[192:195], v[228:231], v[20:23]\n" \
    "ds_read_b64_tr_b16 v[176:177], v242 offset:0\n" \
    "v_mfma_f32_16x16x32_bf16 v[24:27], v[192:195], v[232:235], v[24:27]\n" \
    "s_add_u32 m0, s74, s79\n" \
    "s_mul_i32 s72, s73, s70\n" \
    "v_mfma_f32_16x16x32_bf16 v[28:31], v[192:195], v[236:239], v[28:31]\n" \
    "ds_read_b64_tr_b16 v[178:179], v242 offset:2048\n" \
    "buffer_load_dwordx4 v14, s[44:47], s72 offen lds\n" \
    "v_mfma_f32_16x16x32_bf16 v[32:35], v[196:199], v[224:227], v[32:35]\n" \
    "ds_read_b64_tr_b16 v[180:181], v243 offset:0\n" \
    "v_mfma_f32_16x16x32_bf16 v[36:39], v[196:199], v[228:231], v[36:39]\n" \
    "ds_read_b64_tr_b16 v[182:183], v243 offset:2048\n" \
    "v_mfma_f32_16x16x32_bf16 v[40:43], v[196:199], v[232:235], v[40:43]\n" \
    "s_add_u32 m0, m0, 1024\n" \
    "s_add_u32 s72, s72, s68\n" \
    "v_mfma_f32_16x16x32_bf16 v[44:47], v[196:199], v[236:239], v[44:47]\n" \
    "ds_read_b64_tr_b16 v[184:185], v244 offset:0\n" \
    "buffer_load_dwordx4 v14, s[44:47], s72 offen lds\n" \
    "v_mfma_f32_16x16x32_bf16 v[48:51], v[200:203], v[224:227], v[48:51]\n" \
    "ds_read_b64_tr_b16 v[186:187], v244 offset:2048\n" \
    "v_mfma_f32_16x16x32_bf16 v[52:55], v[200:203], v[228:231], v[52:55]\n" \
    "ds_read_b64_tr_b16 v[188:189], v245 offset:0\n" \
    "v_mfma_f32_16x16x32_bf16 v[56:59], v[200:203], v[232:235], v[56:59]\n" \
    "ds_read_b64_tr_b16 v[190:191], v245 offset:2048\n" \
    "v_mfma_f32_16x16x32_bf16 v[60:63], v[200:203], v[236:239], v[60:63]\n" \
    "s_add_u32 m0, m0, 1024\n" \
    "s_sub_u32 s72, s72, s69\n" \
    "v_mfma_f32_16x16x32_bf16 v[64:67], v[204:207], v[224:227], v[64:67]\n" \
    "ds_read_b128 v[144:147], v240 offset:0\n" \
    "buffer_load_dwordx4 v14, s[44:47], s72 offen lds\n" \
    "v_mfma_f32_16x16x32_bf16 v[68:71], v[204:207], v[228:231], v[68:71]\n" \
    "ds_read_b128 v[148:151], v240 offset:2048\n" \
    "v_mfma_f32_16x16x32_bf16 v[72:75], v[204:207], v[232:235], v[72:75]\n" \
    "ds_read_b128 v[152:155], v240 offset:4096\n" \
    "v_mfma_f32_16x16x32_bf16 v[76:79], v[204:207], v[236:239], v[76:79]\n" \
    "ds_read_b128 v[156:159], v240 offset:6144\n" \
    "v_mfma_f32_16x16x32_bf16 v[80:83], v[208:211], v[224:227], v[80:83]\n" \
    "s_add_u32 m0, m0, 1024\n" \
    "s_add_u32 s72, s72, s68\n" \
    "v_mfma_f32_16x16x32_bf16 v[84:87], v[208:211], v[228:231], v[84:87]\n" \
    "ds_read_b128 v[160:163], v240 offset:8192\n" \
    "buffer_load_dwordx4 v14, s[44:47], s72 offen lds\n" \
    "v_mfma_f32_16x16x32_bf16 v[88:91], v[208:211], v[232:235], v[88:91]\n" \
    "ds_read_b128 v[164:167], v240 offset:10240\n" \
    "v_mfma_f32_16x16x32_bf16 v[92:95], v[208:211], v[236:239], v[92:95]\n" \
    "ds_read_b128 v[168:171], v240 offset:12288\n" \
    "v_mfma_f32_16x16x32_bf16 v[96:99], v[212:215], v[224:227], v[96:99]\n" \
    "ds_read_b128 v[172:175], v240 offset:14336\n" \
    "v_mfma_f32_16x16x32_bf16 v[100:103], v[212:215], v[228:231], v[100:103]\n" \
    "s_add_u32 m0, s74, s78\n" \
    "s_mov_b32 s71, s73\n" \
    "v_mfma_f32_16x16x32_bf16 v[104:107], v[212:215], v[232:235], v[104:107]\n" \
    "buffer_load_dwordx4 v246, s[40:43], s71 offen sc1 lds\n" \
    "s_add_u32 s73, s73, 128\n" \
    "v_mfma_f32_16x16x32_bf16 v[108:111], v[212:215], v[236:239], v[108:111]\n" \
    "buffer_store_dwordx4 a[200:203], v15, s[48:51], s80 offen sc1\n" \
    "s_add_u32 s80, s80, s65\n" \
    "v_mfma_f32_16x16x32_bf16 v[112:115], v[216:219], v[224:227], v[112:115]\n" \
    "v_mfma_f32_16x16x32_bf16 v[116:119], v[216:219], v[228:231], v[116:119]\n" \
    "v_mfma_f32_16x16x32_bf16 v[120:123], v[216:219], v[232:235], v[120:123]\n" \
    "s_add_u32 m0, m0, 4096\n" \
    "s_add_u32 s71, s71, 65536\n" \
    "v_mfma_f32_16x16x32_bf16 v[124:127], v[216:219], v[236:239], v[124:127]\n" \
    "buffer_load_dwordx4 v246, s[40:43], s71 offen sc1 lds\n" \
    "s_and_b32 s73, s73, 2047\n" \
    "v_mfma_f32_16x16x32_bf16 v[128:131], v[220:223], v[224:227], v[128:131]\n" \
    "buffer_store_dwordx4 a[204:207], v15, s[48:51], s80 offen sc1\n" \
    "s_add_u32 s66, s66, s63\n" \
    "v_mfma_f32_16x16x32_bf16 v[132:135], v[220:223], v[228:231], v[132:135]\n" \
    "s_add_u32 s74, s74, s76\n" \
    "s_sub_u32 s75, s75, 1\n" \
    "v_mfma_f32_16x16x32_bf16 v[136:139], v[220:223], v[232:235], v[136:139]\n" \
    "v_mfma_f32_16x16x32_bf16 v[140:143], v[220:223], v[236:239], v[140:143]\n" \
    "s_cmp_lg_u32 s75, 0\n" \
    "s_waitcnt lgkmcnt(0)\n" \
    "v_mfma_f32_16x16x32_bf16 v[16:19], v[144:147], v[176:179], v[16:19]\n" \
    "ds_read_b64_tr_b16 v[224:225], v242 offset:4096\n" \
    "v_mfma_f32_16x16x32_bf16 v[20:23], v[144:147], v[180:183], v[20:23]\n" \
    "s_add_u32 m0, m0, 4096\n" \
    "s_add_u32 s71, s71, 65536\n" \
    "v_mfma_f32_16x16x32_bf16 v[24:27], v[144:147], v[184:187], v[24:27]\n" \
    "ds_read_b64_tr_b16 v[226:227], v242 offset:6144\n" \
    "buffer_load_dwordx4 v246, s[40:43], s71 offen sc1 lds\n" \
    "v_mfma_f32_16x16x32_bf16 v[28:31], v[144:147], v[188:191], v[28:31]\n" \
    "ds_read_b64_tr_b16 v[228:229], v243 offset:4096\n" \
    "v_mfma_f32_16x16x32_bf16 v[32:35], v[148:151], v[176:179], v[32:35]\n" \
    "ds_read_b64_tr_b16 v[230:231], v243 offset:6144\n" \
    "v_mfma_f32_16x16x32_bf16 v[36:39], v[148:151], v[180:183], v[36:39]\n" \
    "ds_read_b64_tr_b16 v[232:233], v244 offset:4096\n" \
    "v_mfma_f32_16x16x32_bf16 v[40:43], v[148:151], v[184:187], v[40:43]\n" \
    "s_add_u32 m0, m0, 4096\n" \
    "s_add_u32 s71, s71, 65536\n" \
    "v_mfma_f32_16x16x32_bf16 v[44:47], v[148:151], v[188:191], v[44:47]\n" \
    "ds_read_b64_tr_b16 v[234:235], v244 offset:6144\n" \
    "buffer_load_dwordx4 v246, s[40:43], s71 offen sc1 lds\n" \
    "v_mfma_f32_16x16x32_bf16 v[48:51], v[152:155], v[176:179], v[48:51]\n" \
    "ds_read_b64_tr_b16 v[236:237], v245 offset:4096\n" \
    "v_mfma_f32_16x16x32_bf16 v[52:55], v[152:155], v[180:183], v[52:55]\n" \
    "ds_read_b64_tr_b16 v[238:239], v245 offset:6144\n" \
    "v_mfma_f32_16x16x32_bf16 v[56:59], v[152:155], v[184:187], v[56:59]\n" \
    "ds_read_b128 v[192:195], v241 offset:0\n" \
    "v_mfma_f32_16x16x32_bf16 v[60:63], v[152:155], v[188:191], v[60:63]\n" \
    "s_add_u32 m0, m0, 4096\n" \
    "s_add_u32 s71, s71, 65536\n" \
    "v_mfma_f32_16x16x32_bf16 v[64:67], v[156:159], v[176:179], v[64:67]\n" \
    "ds_read_b128 v[196:199], v241 offset:2048\n" \
    "buffer_load_dwordx4 v246, s[40:43], s71 offen sc1 lds\n" \
    "v_mfma_f32_16x16x32_bf16 v[68:71], v[156:159], v[180:183], v[68:71]\n" \
    "ds_read_b128 v[200:203], v241 offset:4096\n" \
    "v_mfma_f32_16x16x32_bf16 v[72:75], v[156:159], v[184:187], v[72:75]\n" \
    "ds_read_b128 v[204:207], v241 offset:6144\n" \
    "v_mfma_f32_16x16x32_bf16 v[76:79], v[156:159], v[188:191], v[76:79]\n" \
    "ds_read_b128 v[208:211], v241 offset:8192\n" \
    "v_mfma_f32_16x16x32_bf16 v[80:83], v[160:163], v[176:179], v[80:83]\n" \
    "s_add_u32 m0, m0, 4096\n" \
    "s_add_u32 s71, s71, 65536\n" \
    "v_mfma_f32_16x16x32_bf16 v[84:87], v[160:163], v[180:183], v[84:87]\n" \
    "ds_read_b128 v[212:215], v241 offset:10240\n" \
    "buffer_load_dwordx4 v246, s[40:43], s71 offen sc1 lds\n" \
    "v_mfma_f32_16x16x32_bf16 v[88:91], v[160:163], v[184:187], v[88:91]\n" \
    "ds_read_b128 v[216:219], v241 offset:12288\n" \
    "v_mfma_f32_16x16x32_bf16 v[92:95], v[160:163], v[188:191], v[92:95]\n" \
    "ds_read_b128 v[220:223], v241 offset:14336\n" \
    "v_mfma_f32_16x16x32_bf16 v[96:99], v[164:167], v[176:179], v[96:99]\n" \
    "s_cmp_eq_u32 s74, 98304\n" \
    "s_cselect_b32 s76, s77, 49152\n" \
    "v_mfma_f32_16x16x32_bf16 v[100:103], v[164:167], v[180:183], v[100:103]\n" \
    "s_add_u32 m0, m0, 4096\n" \
    "s_add_u32 s71, s71, 65536\n" \
    "v_mfma_f32_16x16x32_bf16 v[104:107], v[164:167], v[184:187], v[104:107]\n" \
    "buffer_load_dwordx4 v246, s[40:43], s71 offen sc1 lds\n" \
    "v_mfma_f32_16x16x32_bf16 v[108:111], v[164:167], v[188:191], v[108:111]\n" \
    "v_add_u32 v242, s76, v242\n" \
    "v_mfma_f32_16x16x32_bf16 v[112:115], v[168:171], v[176:179], v[112:115]\n" \
    "v_add_u32 v243, s76, v243\n" \
    "v_mfma_f32_16x16x32_bf16 v[116:119], v[168:171], v[180:183], v[116:119]\n" \
    "v_add_u32 v244, s76, v244\n" \
    "v_mfma_f32_16x16x32_bf16 v[120:123], v[168:171], v[184:187], v[120:123]\n" \
    "s_add_u32 m0, m0, 4096\n" \
    "s_add_u32 s71, s71, 65536\n" \
    "v_mfma_f32_16x16x32_bf16 v[124:127], v[168:171], v[188:191], v[124:127]\n" \
    "buffer_load_dwordx4 v246, s[40:43], s71 offen sc1 lds\n" \
    "v_mfma_f32_16x16x32_bf16 v[128:131], v[172:175], v[176:179], v[128:131]\n" \
    "v_add_u32 v245, s76, v245\n" \
    "v_mfma_f32_16x16x32_bf16 v[132:135], v[172:175], v[180:183], v[132:135]\n" \
    "v_add_u32 v240, s76, v240\n" \
    "v_mfma_f32_16x16x32_bf16 v[136:139], v[172:175], v[184:187], v[136:139]\n" \
    "v_add_u32 v241, s76, v241\n" \
    "v_mfma_f32_16x16x32_bf16 v[140:143], v[172:175], v[188:191], v[140:143]\n" \
    "s_waitcnt vmcnt(14) lgkmcnt(0)\n" \
    "v_mfma_f32_16x16x32_bf16 v[16:19], v[192:195], v[224:227], v[16:19]\n" \
    "s_barrier\n" \
    "v_mfma_f32_16x16x32_bf16 v[20:23], v[192:195], v[228:231], v[20:23]\n" \
    "ds_read_b64_tr_b16 v[176:177], v242 offset:0\n" \
    "v_mfma_f32_16x16x32_bf16 v[24:27], v[192:195], v[232:235], v[24:27]\n" \
    "s_add_u32 m0, s74, s79\n" \
    "s_mul_i32 s72, s73, s70\n" \
    "v_mfma_f32_16x16x32_bf16 v[28:31], v[192:195], v[236:239], v[28:31]\n" \
    "ds_read_b64_tr_b16 v[178:179], v242 offset:2048\n" \
    "buffer_load_dwordx4 v14, s[44:47], s72 offen lds\n" \
    "v_mfma_f32_16x16x32_bf16 v[32:35], v[196:199], v[224:227], v[32:35]\n" \
    "ds_read_b64_tr_b16 v[180:181], v243 offset:0\n" \
    "v_mfma_f32_16x16x32_bf16 v[36:39], v[196:199], v[228:231], v[36:39]\n" \
    "ds_read_b64_tr_b16 v[182:183], v243 offset:2048\n" \
    "v_mfma_f32_16x16x32_bf16 v[40:43], v[196:199], v[232:235], v[40:43]\n" \
    "s_add_u32 m0, m0, 1024\n" \
    "s_add_u32 s72, s72, s68\n" \
    "v_mfma_f32_16x16x32_bf16 v[44:47], v[196:199], v[236:239], v[44:47]\n" \
    "ds_read_b64_tr_b16 v[184:185], v244 offset:0\n" \
    "buffer_load_dwordx4 v14, s[44:47], s72 offen lds\n" \
    "v_mfma_f32_16x16x32_bf16 v[48:51], v[200:203], v[224:227], v[48:51]\n" \
    "ds_read_b64_tr_b16 v[186:187], v244 offset:2048\n" \
    "v_mfma_f32_16x16x32_bf16 v[52:55], v[200:203], v[228:231], v[52:55]\n" \
    "ds_read_b64_tr_b16 v[188:189], v245 offset:0\n" \
    "v_mfma_f32_16x16x32_bf16 v[56:59], v[200:203], v[232:235], v[56:59]\n" \
    "ds_read_b64_tr_b16 v[190:191], v245 offset:2048\n" \
    "v_mfma_f32_16x16x32_bf16 v[60:63], v[200:203], v[236:239], v[60:63]\n" \
    "s_add_u32 m0, m0, 1024\n" \
    "s_sub_u32 s72, s72, s69\n" \
    "v_mfma_f32_16x16x32_bf16 v[64:67], v[204:207], v[224:227], v[64:67]\n" \
    "ds_read_b128 v[144:147], v240 offset:0\n" \
    "buffer_load_dwordx4 v14, s[44:47], s72 offen lds\n" \
    "v_mfma_f32_16x16x32_bf16 v[68:71], v[204:207], v[228:231], v[68:71]\n" \
    "ds_read_b128 v[148:151], v240 offset:2048\n" \
    "v_mfma_f32_16x16x32_bf16 v[72:75], v[204:207], v[232:235], v[72:75]\n" \
    "ds_read_b128 v[152:155], v240 offset:4096\n" \
    "v_mfma_f32_16x16x32_bf16 v[76:79], v[204:207], v[236:239], v[76:79]\n" \
    "ds_read_b128 v[156:159], v240 offset:6144\n" \
    "v_mfma_f32_16x16x32_bf16 v[80:83], v[208:211], v[224:227], v[80:83]\n" \
    "s_add_u32 m0, m0, 1024\n" \
    "s_add_u32 s72, s72, s68\n" \
    "v_mfma_f32_16x16x32_bf16 v[84:87], v[208:211], v[228:231], v[84:87]\n" \
    "ds_read_b128 v[160:163], v240 offset:8192\n" \
    "buffer_load_dwordx4 v14, s[44:47], s72 offen lds\n" \
    "v_mfma_f32_16x16x32_bf16 v[88:91], v[208:211], v[232:235], v[88:91]\n" \
    "ds_read_b128 v[164:167], v240 offset:10240\n" \
    "v_mfma_f32_16x16x32_bf16 v[92:95], v[208:211], v[236:239], v[92:95]\n" \
    "ds_read_b128 v[168:171], v240 offset:12288\n" \
    "v_mfma_f32_16x16x32_bf16 v[96:99], v[212:215], v[224:227], v[96:99]\n" \
    "ds_read_b128 v[172:175], v240 offset:14336\n" \
    "v_mfma_f32_16x16x32_bf16 v[100:103], v[212:215], v[228:231], v[100:103]\n" \
    "s_add_u32 m0, s74, s78\n" \
    "s_mov_b32 s71, s73\n" \
    "v_mfma_f32_16x16x32_bf16 v[104:107], v[212:215], v[232:235], v[104:107]\n" \
    "buffer_load_dwordx4 v246, s[40:43], s71 offen sc1 lds\n" \
    "s_add_u32 s73, s73, 128\n" \
    "v_mfma_f32_16x16x32_bf16 v[108:111], v[212:215], v[236:239], v[108:111]\n" \
    "buffer_store_dwordx4 a[208:211], v15, s[48:51], s66 offen sc1\n" \
    "s_add_u32 s80, s66, s65\n" \
    "v_mfma_f32_16x16x32_bf16 v[112:115], v[216:219], v[224:227], v[112:115]\n" \
    "v_mfma_f32_16x16x32_bf16 v[116:119], v[216:219], v[228:231], v[116:119]\n" \
    "v_mfma_f32_16x16x32_bf16 v[120:123], v[216:219], v[232:235], v[120:123]\n" \
    "s_add_u32 m0, m0, 4096\n" \
    "s_add_u32 s71, s71, 65536\n" \
    "v_mfma_f32_16x16x32_bf16 v[124:127], v[216:219], v[236:239], v[124:127]\n" \
    "buffer_load_dwordx4 v246, s[40:43], s71 offen sc1 lds\n" \
    "s_and_b32 s73, s73, 2047\n" \
    "v_mfma_f32_16x16x32_bf16 v[128:131], v[220:223], v[224:227], v[128:131]\n" \
    "buffer_store_dwordx4 a[212:215], v15, s[48:51], s80 offen sc1\n" \
    "s_add_u32 s80, s66, s64\n" \
    "v_mfma_f32_16x16x32_bf16 v[132:135], v[220:223], v[228:231], v[132:135]\n" \
    "s_add_u32 s74, s74, s76\n" \
    "s_sub_u32 s75, s75, 1\n" \
    "v_mfma_f32_16x16x32_bf16 v[136:139], v[220:223], v[232:235], v[136:139]\n" \
    "v_mfma_f32_16x16x32_bf16 v[140:143], v[220:223], v[236:239], v[140:143]\n" \
    "s_cmp_lg_u32 s75, 0\n" \
    "s_waitcnt lgkmcnt(0)\n" \
    "v_mfma_f32_16x16x32_bf16 v[16:19], v[144:147], v[176:179], v[16:19]\n" \
    "ds_read_b64_tr_b16 v[224:225], v242 offset:4096\n" \
    "v_mfma_f32_16x16x32_bf16 v[20:23], v[144:147], v[180:183], v[20:23]\n" \
    "s_add_u32 m0, m0, 4096\n" \
    "s_add_u32 s71, s71, 65536\n" \
    "v_mfma_f32_16x16x32_bf16 v[24:27], v[144:147], v[184:187], v[24:27]\n" \
    "ds_read_b64_tr_b16 v[226:227], v242 offset:6144\n" \
    "buffer_load_dwordx4 v246, s[40:43], s71 offen sc1 lds\n" \
    "v_mfma_f32_16x16x32_bf16 v[28:31], v[144:147], v[188:191], v[28:31]\n" \
    "ds_read_b64_tr_b16 v[228:229], v243 offset:4096\n" \
    "v_mfma_f32_16x16x32_bf16 v[32:35], v[148:151], v[176:179], v[32:35]\n" \
    "ds_read_b64_tr_b16 v[230:231], v243 offset:6144\n" \
    "v_mfma_f32_16x16x32_bf16 v[36:39], v[148:151], v[180:183], v[36:39]\n" \
    "ds_read_b64_tr_b16 v[232:233], v244 offset:4096\n" \
    "v_mfma_f32_16x16x32_bf16 v[40:43], v[148:151], v[184:187], v[40:43]\n" \
    "s_add_u32 m0, m0, 4096\n" \
    "s_add_u32 s71, s71, 65536\n" \
    "v_mfma_f32_16x16x32_bf16 v[44:47], v[148:151], v[188:191], v[44:47]\n" \
    "ds_read_b64_tr_b16 v[234:235], v244 offset:6144\n" \
    "buffer_load_dwordx4 v246, s[40:43], s71 offen sc1 lds\n" \
    "v_mfma_f32_16x16x32_bf16 v[48:51], v[152:155], v[176:179], v[48:51]\n" \
    "ds_read_b64_tr_b16 v[236:237], v245 offset:4096\n" \
    "v_mfma_f32_16x16x32_bf16 v[52:55], v[152:155], v[180:183], v[52:55]\n" \
    "ds_read_b64_tr_b16 v[238:239], v245 offset:6144\n" \
    "v_mfma_f32_16x16x32_bf16 v[56:59], v[152:155], v[184:187], v[56:59]\n" \
    "ds_read_b128 v[192:195], v241 offset:0\n" \
    "v_mfma_f32_16x16x32_bf16 v[60:63], v[152:155], v[188:191], v[60:63]\n" \
    "s_add_u32 m0, m0, 4096\n" \
    "s_add_u32 s71, s71, 65536\n" \
    "v_mfma_f32_16x16x32_bf16 v[64:67], v[156:159], v[176:179], v[64:67]\n" \
    "ds_read_b128 v[196:199], v241 offset:2048\n" \
    "buffer_load_dwordx4 v246, s[40:43], s71 offen sc1 lds\n" \
    "v_mfma_f32_16x16x32_bf16 v[68:71], v[156:159], v[180:183], v[68:71]\n" \
    "ds_read_b128 v[200:203], v241 offset:4096\n" \
    "v_mfma_f32_16x16x32_bf16 v[72:75], v[156:159], v[184:187], v[72:75]\n" \
    "ds_read_b128 v[204:207], v241 offset:6144\n" \
    "v_mfma_f32_16x16x32_bf16 v[76:79], v[156:159], v[188:191], v[76:79]\n" \
    "ds_read_b128 v[208:211], v241 offset:8192\n" \
    "v_mfma_f32_16x16x32_bf16 v[80:83], v[160:163], v[176:179], v[80:83]\n" \
    "s_add_u32 m0, m0, 4096\n" \
    "s_add_u32 s71, s71, 65536\n" \
    "v_mfma_f32_16x16x32_bf16 v[84:87], v[160:163], v[180:183], v[84:87]\n" \
    "ds_read_b128 v[212:215], v241 offset:10240\n" \
    "buffer_load_dwordx4 v246, s[40:43], s71 offen sc1 lds\n" \
    "v_mfma_f32_16x16x32_bf16 v[88:91], v[160:163], v[184:187], v[88:91]\n" \
    "ds_read_b128 v[216:219], v241 offset:12288\n" \
    "v_mfma_f32_16x16x32_bf16 v[92:95], v[160:163], v[188:191], v[92:95]\n" \
    "ds_read_b128 v[220:223], v241 offset:14336\n" \
    "v_mfma_f32_16x16x32_bf16 v[96:99], v[164:167], v[176:179], v[96:99]\n" \
    "s_cmp_eq_u32 s74, 98304\n" \
    "s_cselect_b32 s76, s77, 49152\n" \
    "v_mfma_f32_16x16x32_bf16 v[100:103], v[164:167], v[180:183], v[100:103]\n" \
    "s_add_u32 m0, m0, 4096\n" \
    "s_add_u32 s71, s71, 65536\n" \
    "v_mfma_f32_16x16x32_bf16 v[104:107], v[164:167], v[184:187], v[104:107]\n" \
    "buffer_load_dwordx4 v246, s[40:43], s71 offen sc1 lds\n" \
    "v_mfma_f32_16x16x32_bf16 v[108:111], v[164:167], v[188:191], v[108:111]\n" \
    "v_add_u32 v242, s76, v242\n" \
    "v_mfma_f32_16x16x32_bf16 v[112:115], v[168:171], v[176:179], v[112:115]\n" \
    "v_add_u32 v243, s76, v243\n" \
    "v_mfma_f32_16x16x32_bf16 v[116:119], v[168:171], v[180:183], v[116:119]\n" \
    "v_add_u32 v244, s76, v244\n" \
    "v_mfma_f32_16x16x32_bf16 v[120:123], v[168:171], v[184:187], v[120:123]\n" \
    "s_add_u32 m0, m0, 4096\n" \
    "s_add_u32 s71, s71, 65536\n" \
    "v_mfma_f32_16x16x32_bf16 v[124:127], v[168:171], v[188:191], v[124:127]\n" \
    "buffer_load_dwordx4 v246, s[40:43], s71 offen sc1 lds\n" \
    "v_mfma_f32_16x16x32_bf16 v[128:131], v[172:175], v[176:179], v[128:131]\n" \
    "v_add_u32 v245, s76, v245\n" \
    "v_mfma_f32_16x16x32_bf16 v[132:135], v[172:175], v[180:183], v[132:135]\n" \
    "v_add_u32 v240, s76, v240\n" \
    "v_mfma_f32_16x16x32_bf16 v[136:139], v[172:175], v[184:187], v[136:139]\n" \
    "v_add_u32 v241, s76, v241\n" \
    "v_mfma_f32_16x16x32_bf16 v[140:143], v[172:175], v[188:191], v[140:143]\n" \
    "s_waitcnt vmcnt(14) lgkmcnt(0)\n" \
    "v_mfma_f32_16x16x32_bf16 v[16:19], v[192:195], v[224:227], v[16:19]\n" \
    "s_barrier\n" \
    "v_mfma_f32_16x16x32_bf16 v[20:23], v[192:195], v[228:231], v[20:23]\n" \
    "ds_read_b64_tr_b16 v[176:177], v242 offset:0\n" \
    "v_mfma_f32_16x16x32_bf16 v[24:27], v[192:195], v[232:235], v[24:27]\n" \
    "s_add_u32 m0, s74, s79\n" \
    "s_mul_i32 s72, s73, s70\n" \
    "v_mfma_f32_16x16x32_bf16 v[28:31], v[192:195], v[236:239], v[28:31]\n" \
    "ds_read_b64_tr_b16 v[178:179], v242 offset:2048\n" \
    "buffer_load_dwordx4 v14, s[44:47], s72 offen lds\n" \
    "v_mfma_f32_16x16x32_bf16 v[32:35], v[196:199], v[224:227], v[32:35]\n" \
    "ds_read_b64_tr_b16 v[180:181], v243 offset:0\n" \
    "v_mfma_f32_16x16x32_bf16 v[36:39], v[196:199], v[228:231], v[36:39]\n" \
    "ds_read_b64_tr_b16 v[182:183], v243 offset:2048\n" \
    "v_mfma_f32_16x16x32_bf16 v[40:43], v[196:199], v[232:235], v[40:43]\n" \
    "s_add_u32 m0, m0, 1024\n" \
    "s_add_u32 s72, s72, s68\n" \
    "v_mfma_f32_16x16x32_bf16 v[44:47], v[196:199], v[236:239], v[44:47]\n" \
    "ds_read_b64_tr_b16 v[184:185], v244 offset:0\n" \
    "buffer_load_dwordx4 v14, s[44:47], s72 offen lds\n" \
    "v_mfma_f32_16x16x32_bf16 v[48:51], v[200:203], v[224:227], v[48:51]\n" \
    "ds_read_b64_tr_b16 v[186:187], v244 offset:2048\n" \
    "v_mfma_f32_16x16x32_bf16 v[52:55], v[200:203], v[228:231], v[52:55]\n" \
    "ds_read_b64_tr_b16 v[188:189], v245 offset:0\n" \
    "v_mfma_f32_16x16x32_bf16 v[56:59], v[200:203], v[232:235], v[56:59]\n" \
    "ds_read_b64_tr_b16 v[190:191], v245 offset:2048\n" \
    "v_mfma_f32_16x16x32_bf16 v[60:63], v[200:203], v[236:239], v[60:63]\n" \
    "s_add_u32 m0, m0, 1024\n" \
    "s_sub_u32 s72, s72, s69\n" \
    "v_mfma_f32_16x16x32_bf16 v[64:67], v[204:207], v[224:227], v[64:67]\n" \
    "ds_read_b128 v[144:147], v240 offset:0\n" \
    "buffer_load_dwordx4 v14, s[44:47], s72 offen lds\n" \
    "v_mfma_f32_16x16x32_bf16 v[68:71], v[204:207], v[228:231], v[68:71]\n" \
    "ds_read_b128 v[148:151], v240 offset:2048\n" \
    "v_mfma_f32_16x16x32_bf16 v[72:75], v[204:207], v[232:235], v[72:75]\n" \
    "ds_read_b128 v[152:155], v240 offset:4096\n" \
    "v_mfma_f32_16x16x32_bf16 v[76:79], v[204:207], v[236:239], v[76:79]\n" \
    "ds_read_b128 v[156:159], v240 offset:6144\n" \
    "v_mfma_f32_16x16x32_bf16 v[80:83], v[208:211], v[224:227], v[80:83]\n" \
    "s_add_u32 m0, m0, 1024\n" \
    "s_add_u32 s72, s72, s68\n" \
    "v_mfma_f32_16x16x32_bf16 v[84:87], v[208:211], v[228:231], v[84:87]\n" \
    "ds_read_b128 v[160:163], v240 offset:8192\n" \
    "buffer_load_dwordx4 v14, s[44:47], s72 offen lds\n" \
    "v_mfma_f32_16x16x32_bf16 v[88:91], v[208:211], v[232:235], v[88:91]\n" \
    "ds_read_b128 v[164:167], v240 offset:10240\n" \
    "v_mfma_f32_16x16x32_bf16 v[92:95], v[208:211], v[236:239], v[92:95]\n" \
    "ds_read_b128 v[168:171], v240 offset:12288\n" \
    "v_mfma_f32_16x16x32_bf16 v[96:99], v[212:215], v[224:227], v[96:99]\n" \
    "ds_read_b128 v[172:175], v240 offset:14336\n" \
    "v_mfma_f32_16x16x32_bf16 v[100:103], v[212:215], v[228:231], v[100:103]\n" \
    "s_add_u32 m0, s74, s78\n" \
    "s_mov_b32 s71, s73\n" \
    "v_mfma_f32_16x16x32_bf16 v[104:107], v[212:215], v[232:235], v[104:107]\n" \
    "buffer_load_dwordx4 v246, s[40:43], s71 offen sc1 lds\n" \
    "s_add_u32 s73, s73, 128\n" \
    "v_mfma_f32_16x16x32_bf16 v[108:111], v[212:215], v[236:239], v[108:111]\n" \
    "buffer_store_dwordx4 a[216:219], v15, s[48:51], s80 offen sc1\n" \
    "s_add_u32 s80, s80, s65\n" \
    "v_mfma_f32_16x16x32_bf16 v[112:115], v[216:219], v[224:227], v[112:115]\n" \
    "v_mfma_f32_16x16x32_bf16 v[116:119], v[216:219], v[228:231], v[116:119]\n" \
    "v_mfma_f32_16x16x32_bf16 v[120:123], v[216:219], v[232:235], v[120:123]\n" \
    "s_add_u32 m0, m0, 4096\n" \
    "s_add_u32 s71, s71, 65536\n" \
    "v_mfma_f32_16x16x32_bf16 v[124:127], v[216:219], v[236:239], v[124:127]\n" \
    "buffer_load_dwordx4 v246, s[40:43], s71 offen sc1 lds\n" \
    "s_and_b32 s73, s73, 2047\n" \
    "v_mfma_f32_16x16x32_bf16 v[128:131], v[220:223], v[224:227], v[128:131]\n" \
    "buffer_store_dwordx4 a[220:223], v15, s[48:51], s80 offen sc1\n" \
    "s_add_u32 s66, s66, s63\n" \
    "v_mfma_f32_16x16x32_bf16 v[132:135], v[220:223], v[228:231], v[132:135]\n" \
    "s_add_u32 s74, s74, s76\n" \
    "s_sub_u32 s75, s75, 1\n" \
    "v_mfma_f32_16x16x32_bf16 v[136:139], v[220:223], v[232:235], v[136:139]\n" \
    "v_mfma_f32_16x16x32_bf16 v[140:143], v[220:223], v[236:239], v[140:143]\n" \
    "s_cmp_lg_u32 s75, 0\n" \
    "s_waitcnt lgkmcnt(0)\n" \
    "v_mfma_f32_16x16x32_bf16 v[16:19], v[144:147], v[176:179], v[16:19]\n" \
    "ds_read_b64_tr_b16 v[224:225], v242 offset:4096\n" \
    "v_mfma_f32_16x16x32_bf16 v[20:23], v[144:147], v[180:183], v[20:23]\n" \
    "s_add_u32 m0, m0, 4096\n" \
    "s_add_u32 s71, s71, 65536\n" \
    "v_mfma_f32_16x16x32_bf16 v[24:27], v[144:147], v[184:187], v[24:27]\n" \
    "ds_read_b64_tr_b16 v[226:227], v242 offset:6144\n" \
    "buffer_load_dwordx4 v246, s[40:43], s71 offen sc1 lds\n" \
    "v_mfma_f32_16x16x32_bf16 v[28:31], v[144:147], v[188:191], v[28:31]\n" \
    "ds_read_b64_tr_b16 v[228:229], v243 offset:4096\n" \
    "v_mfma_f32_16x16x32_bf16 v[32:35], v[148:151], v[176:179], v[32:35]\n" \
    "ds_read_b64_tr_b16 v[230:231], v243 offset:6144\n" \
    "v_mfma_f32_16x16x32_bf16 v[36:39], v[148:151], v[180:183], v[36:39]\n" \
    "ds_read_b64_tr_b16 v[232:233], v244 offset:4096\n" \
    "v_mfma_f32_16x16x32_bf16 v[40:43], v[148:151], v[184:187], v[40:43]\n" \
    "s_add_u32 m0, m0, 4096\n" \
    "s_add_u32 s71, s71, 65536\n" \
    "v_mfma_f32_16x16x32_bf16 v[44:47], v[148:151], v[188:191], v[44:47]\n" \
    "ds_read_b64_tr_b16 v[234:235], v244 offset:6144\n" \
    "buffer_load_dwordx4 v246, s[40:43], s71 offen sc1 lds\n" \
    "v_mfma_f32_16x16x32_bf16 v[48:51], v[152:155], v[176:179], v[48:51]\n" \
    "ds_read_b64_tr_b16 v[236:237], v245 offset:4096\n" \
    "v_mfma_f32_16x16x32_bf16 v[52:55], v[152:155], v[180:183], v[52:55]\n" \
    "ds_read_b64_tr_b16 v[238:239], v245 offset:6144\n" \
    "v_mfma_f32_16x16x32_bf16 v[56:59], v[152:155], v[184:187], v[56:59]\n" \
    "ds_read_b128 v[192:195], v241 offset:0\n" \
    "v_mfma_f32_16x16x32_bf16 v[60:63], v[152:155], v[188:191], v[60:63]\n" \
    "s_add_u32 m0, m0, 4096\n" \
    "s_add_u32 s71, s71, 65536\n" \
    "v_mfma_f32_16x16x32_bf16 v[64:67], v[156:159], v[176:179], v[64:67]\n" \
    "ds_read_b128 v[196:199], v241 offset:2048\n" \
    "buffer_load_dwordx4 v246, s[40:43], s71 offen sc1 lds\n" \
    "v_mfma_f32_16x16x32_bf16 v[68:71], v[156:159], v[180:183], v[68:71]\n" \
    "ds_read_b128 v[200:203], v241 offset:4096\n" \
    "v_mfma_f32_16x16x32_bf16 v[72:75], v[156:159], v[184:187], v[72:75]\n" \
    "ds_read_b128 v[204:207], v241 offset:6144\n" \
    "v_mfma_f32_16x16x32_bf16 v[76:79], v[156:159], v[188:191], v[76:79]\n" \
    "ds_read_b128 v[208:211], v241 offset:8192\n" \
    "v_mfma_f32_16x16x32_bf16 v[80:83], v[160:163], v[176:179], v[80:83]\n" \
    "s_add_u32 m0, m0, 4096\n" \
    "s_add_u32 s71, s71, 65536\n" \
    "v_mfma_f32_16x16x32_bf16 v[84:87], v[160:163], v[180:183], v[84:87]\n" \
    "ds_read_b128 v[212:215], v241 offset:10240\n" \
    "buffer_load_dwordx4 v246, s[40:43], s71 offen sc1 lds\n" \
    "v_mfma_f32_16x16x32_bf16 v[88:91], v[160:163], v[184:187], v[88:91]\n" \
    "ds_read_b128 v[216:219], v241 offset:12288\n" \
    "v_mfma_f32_16x16x32_bf16 v[92:95], v[160:163], v[188:191], v[92:95]\n" \
    "ds_read_b128 v[220:223], v241 offset:14336\n" \
    "v_mfma_f32_16x16x32_bf16 v[96:99], v[164:167], v[176:179], v[96:99]\n" \
    "s_cmp_eq_u32 s74, 98304\n" \
    "s_cselect_b32 s76, s77, 49152\n" \
    "v_mfma_f32_16x16x32_bf16 v[100:103], v[164:167], v[180:183], v[100:103]\n" \
    "s_add_u32 m0, m0, 4096\n" \
    "s_add_u32 s71, s71, 65536\n" \
    "v_mfma_f32_16x16x32_bf16 v[104:107], v[164:167], v[184:187], v[104:107]\n" \
    "buffer_load_dwordx4 v246, s[40:43], s71 offen sc1 lds\n" \
    "v_mfma_f32_16x16x32_bf16 v[108:111], v[164:167], v[188:191], v[108:111]\n" \
    "v_add_u32 v242, s76, v242\n" \
    "v_mfma_f32_16x16x32_bf16 v[112:115], v[168:171], v[176:179], v[112:115]\n" \
    "v_add_u32 v243, s76, v243\n" \
    "v_mfma_f32_16x16x32_bf16 v[116:119], v[168:171], v[180:183], v[116:119]\n" \
    "v_add_u32 v244, s76, v244\n" \
    "v_mfma_f32_16x16x32_bf16 v[120:123], v[168:171], v[184:187], v[120:123]\n" \
    "s_add_u32 m0, m0, 4096\n" \
    "s_add_u32 s71, s71, 65536\n" \
    "v_mfma_f32_16x16x32_bf16 v[124:127], v[168:171], v[188:191], v[124:127]\n" \
    "buffer_load_dwordx4 v246, s[40:43], s71 offen sc1 lds\n" \
    "v_mfma_f32_16x16x32_bf16 v[128:131], v[172:175], v[176:179], v[128:131]\n" \
    "v_add_u32 v245, s76, v245\n" \
    "v_mfma_f32_16x16x32_bf16 v[132:135], v[172:175], v[180:183], v[132:135]\n" \
    "v_add_u32 v240, s76, v240\n" \
    "v_mfma_f32_16x16x32_bf16 v[136:139], v[172:175], v[184:187], v[136:139]\n" \
    "v_add_u32 v241, s76, v241\n" \
    "v_mfma_f32_16x16x32_bf16 v[140:143], v[172:175], v[188:191], v[140:143]\n" \
    "s_waitcnt vmcnt(14) lgkmcnt(0)\n" \
    "v_mfma_f32_16x16x32_bf16 v[16:19], v[192:195], v[224:227], v[16:19]\n" \
    "s_barrier\n" \
    "v_mfma_f32_16x16x32_bf16 v[20:23], v[192:195], v[228:231], v[20:23]\n" \
    "ds_read_b64_tr_b16 v[176:177], v242 offset:0\n" \
    "v_mfma_f32_16x16x32_bf16 v[24:27], v[192:195], v[232:235], v[24:27]\n" \
    "s_add_u32 m0, s74, s79\n" \
    "s_mul_i32 s72, s73, s70\n" \
    "v_mfma_f32_16x16x32_bf16 v[28:31], v[192:195], v[236:239], v[28:31]\n" \
    "ds_read_b64_tr_b16 v[178:179], v242 offset:2048\n" \
    "buffer_load_dwordx4 v14, s[44:47], s72 offen lds\n" \
    "v_mfma_f32_16x16x32_bf16 v[32:35], v[196:199], v[224:227], v[32:35]\n" \
    "ds_read_b64_tr_b16 v[180:181], v243 offset:0\n" \
    "v_mfma_f32_16x16x32_bf16 v[36:39], v[196:199], v[228:231], v[36:39]\n" \
    "ds_read_b64_tr_b16 v[182:183], v243 offset:2048\n" \
    "v_mfma_f32_16x16x32_bf16 v[40:43], v[196:199], v[232:235], v[40:43]\n" \
    "s_add_u32 m0, m0, 1024\n" \
    "s_add_u32 s72, s72, s68\n" \
    "v_mfma_f32_16x16x32_bf16 v[44:47], v[196:199], v[236:239], v[44:47]\n" \
    "ds_read_b64_tr_b16 v[184:185], v244 offset:0\n" \
    "buffer_load_dwordx4 v14, s[44:47], s72 offen lds\n" \
    "v_mfma_f32_16x16x32_bf16 v[48:51], v[200:203], v[224:227], v[48:51]\n" \
    "ds_read_b64_tr_b16 v[186:187], v244 offset:2048\n" \
    "v_mfma_f32_16x16x32_bf16 v[52:55], v[200:203], v[228:231], v[52:55]\n" \
    "ds_read_b64_tr_b16 v[188:189], v245 offset:0\n" \
    "v_mfma_f32_16x16x32_bf16 v[56:59], v[200:203], v[232:235], v[56:59]\n" \
    "ds_read_b64_tr_b16 v[190:191], v245 offset:2048\n" \
    "v_mfma_f32_16x16x32_bf16 v[60:63], v[200:203], v[236:239], v[60:63]\n" \
    "s_add_u32 m0, m0, 1024\n" \
    "s_sub_u32 s72, s72, s69\n" \
    "v_mfma_f32_16x16x32_bf16 v[64:67], v[204:207], v[224:227], v[64:67]\n" \
    "ds_read_b128 v[144:147], v240 offset:0\n" \
    "buffer_load_dwordx4 v14, s[44:47], s72 offen lds\n" \
    "v_mfma_f32_16x16x32_bf16 v[68:71], v[204:207], v[228:231], v[68:71]\n" \
    "ds_read_b128 v[148:151], v240 offset:2048\n" \
    "v_mfma_f32_16x16x32_bf16 v[72:75], v[204:207], v[232:235], v[72:75]\n" \
    "ds_read_b128 v[152:155], v240 offset:4096\n" \
    "v_mfma_f32_16x16x32_bf16 v[76:79], v[204:207], v[236:239], v[76:79]\n" \
    "ds_read_b128 v[156:159], v240 offset:6144\n" \
    "v_mfma_f32_16x16x32_bf16 v[80:83], v[208:211], v[224:227], v[80:83]\n" \
    "s_add_u32 m0, m0, 1024\n" \
    "s_add_u32 s72, s72, s68\n" \
    "v_mfma_f32_16x16x32_bf16 v[84:87], v[208:211], v[228:231], v[84:87]\n" \
    "ds_read_b128 v[160:163], v240 offset:8192\n" \
    "buffer_load_dwordx4 v14, s[44:47], s72 offen lds\n" \
    "v_mfma_f32_16x16x32_bf16 v[88:91], v[208:211], v[232:235], v[88:91]\n" \
    "ds_read_b128 v[164:167], v240 offset:10240\n" \
    "v_mfma_f32_16x16x32_bf16 v[92:95], v[208:211], v[236:239], v[92:95]\n" \
    "ds_read_b128 v[168:171], v240 offset:12288\n" \
    "v_mfma_f32_16x16x32_bf16 v[96:99], v[212:215], v[224:227], v[96:99]\n" \
    "ds_read_b128 v[172:175], v240 offset:14336\n" \
    "v_mfma_f32_16x16x32_bf16 v[100:103], v[212:215], v[228:231], v[100:103]\n" \
    "s_add_u32 m0, s74, s78\n" \
    "s_mov_b32 s71, s73\n" \
    "v_mfma_f32_16x16x32_bf16 v[104:107], v[212:215], v[232:235], v[104:107]\n" \
    "buffer_load_dwordx4 v246, s[40:43], s71 offen sc1 lds\n" \
    "s_add_u32 s73, s73, 128\n" \
    "v_mfma_f32_16x16x32_bf16 v[108:111], v[212:215], v[236:239], v[108:111]\n" \
    "buffer_store_dwordx4 a[224:227], v15, s[48:51], s66 offen sc1\n" \
    "s_add_u32 s80, s66, s65\n" \
    "v_mfma_f32_16x16x32_bf16 v[112:115], v[216:219], v[224:227], v[112:115]\n" \
    "v_mfma_f32_16x16x32_bf16 v[116:119], v[216:219], v[228:231], v[116:119]\n" \
    "v_mfma_f32_16x16x32_bf16 v[120:123], v[216:219], v[232:235], v[120:123]\n" \
    "s_add_u32 m0, m0, 4096\n" \
    "s_add_u32 s71, s71, 65536\n" \
    "v_mfma_f32_16x16x32_bf16 v[124:127], v[216:219], v[236:239], v[124:127]\n" \
    "buffer_load_dwordx4 v246, s[40:43], s71 offen sc1 lds\n" \
    "s_and_b32 s73, s73, 2047\n" \
    "v_mfma_f32_16x16x32_bf16 v[128:131], v[220:223], v[224:227], v[128:131]\n" \
    "buffer_store_dwordx4 a[228:231], v15, s[48:51], s80 offen sc1\n" \
    "s_add_u32 s80, s66, s64\n" \
    "v_mfma_f32_16x16x32_bf16 v[132:135], v[220:223], v[228:231], v[132:135]\n" \
    "s_add_u32 s74, s74, s76\n" \
    "s_sub_u32 s75, s75, 1\n" \
    "v_mfma_f32_16x16x32_bf16 v[136:139], v[220:223], v[232:235], v[136:139]\n" \
    "v_mfma_f32_16x16x32_bf16 v[140:143], v[220:223], v[236:239], v[140:143]\n" \
    "s_cmp_lg_u32 s75, 0\n" \
    "s_waitcnt lgkmcnt(0)\n" \
    "v_mfma_f32_16x16x32_bf16 v[16:19], v[144:147], v[176:179], v[16:19]\n" \
    "ds_read_b64_tr_b16 v[224:225], v242 offset:4096\n" \
    "v_mfma_f32_16x16x32_bf16 v[20:23], v[144:147], v[180:183], v[20:23]\n" \
    "s_add_u32 m0, m0, 4096\n" \
    "s_add_u32 s71, s71, 65536\n" \
    "v_mfma_f32_16x16x32_bf16 v[24:27], v[144:147], v[184:187], v[24:27]\n" \
    "ds_read_b64_tr_b16 v[226:227], v242 offset:6144\n" \
    "buffer_load_dwordx4 v246, s[40:43], s71 offen sc1 lds\n" \
    "v_mfma_f32_16x16x32_bf16 v[28:31], v[144:147], v[188:191], v[28:31]\n" \
    "ds_read_b64_tr_b16 v[228:229], v243 offset:4096\n" \
    "v_mfma_f32_16x16x32_bf16 v[32:35], v[148:151], v[176:179], v[32:35]\n" \
    "ds_read_b64_tr_b16 v[230:231], v243 offset:6144\n" \
    "v_mfma_f32_16x16x32_bf16 v[36:39], v[148:151], v[180:183], v[36:39]\n" \
    "ds_read_b64_tr_b16 v[232:233], v244 offset:4096\n" \
    "v_mfma_f32_16x16x32_bf16 v[40:43], v[148:151], v[184:187], v[40:43]\n" \
    "s_add_u32 m0, m0, 4096\n" \
    "s_add_u32 s71, s71, 65536\n" \
    "v_mfma_f32_16x16x32_bf16 v[44:47], v[148:151], v[188:191], v[44:47]\n" \
    "ds_read_b64_tr_b16 v[234:235], v244 offset:6144\n" \
    "buffer_load_dwordx4 v246, s[40:43], s71 offen sc1 lds\n" \
    "v_mfma_f32_16x16x32_bf16 v[48:51], v[152:155], v[176:179], v[48:51]\n" \
    "ds_read_b64_tr_b16 v[236:237], v245 offset:4096\n" \
    "v_mfma_f32_16x16x32_bf16 v[52:55], v[152:155], v[180:183], v[52:55]\n" \
    "ds_read_b64_tr_b16 v[238:239], v245 offset:6144\n" \
    "v_mfma_f32_16x16x32_bf16 v[56:59], v[152:155], v[184:187], v[56:59]\n" \
    "ds_read_b128 v[192:195], v241 offset:0\n" \
    "v_mfma_f32_16x16x32_bf16 v[60:63], v[152:155], v[188:191], v[60:63]\n" \
    "s_add_u32 m0, m0, 4096\n" \
    "s_add_u32 s71, s71, 65536\n" \
    "v_mfma_f32_16x16x32_bf16 v[64:67], v[156:159], v[176:179], v[64:67]\n" \
    "ds_read_b128 v[196:199], v241 offset:2048\n" \
    "buffer_load_dwordx4 v246, s[40:43], s71 offen sc1 lds\n" \
    "v_mfma_f32_16x16x32_bf16 v[68:71], v[156:159], v[180:183], v[68:71]\n" \
    "ds_read_b128 v[200:203], v241 offset:4096\n" \
    "v_mfma_f32_16x16x32_bf16 v[72:75], v[156:159], v[184:187], v[72:75]\n" \
    "ds_read_b128 v[204:207], v241 offset:6144\n" \
    "v_mfma_f32_16x16x32_bf16 v[76:79], v[156:159], v[188:191], v[76:79]\n" \
    "ds_read_b128 v[208:211], v241 offset:8192\n" \
    "v_mfma_f32_16x16x32_bf16 v[80:83], v[160:163], v[176:179], v[80:83]\n" \
    "s_add_u32 m0, m0, 4096\n" \
    "s_add_u32 s71, s71, 65536\n" \
    "v_mfma_f32_16x16x32_bf16 v[84:87], v[160:163], v[180:183], v[84:87]\n" \
    "ds_read_b128 v[212:215], v241 offset:10240\n" \
    "buffer_load_dwordx4 v246, s[40:43], s71 offen sc1 lds\n" \
    "v_mfma_f32_16x16x32_bf16 v[88:91], v[160:163], v[184:187], v[88:91]\n" \
    "ds_read_b128 v[216:219], v241 offset:12288\n" \
    "v_mfma_f32_16x16x32_bf16 v[92:95], v[160:163], v[188:191], v[92:95]\n" \
    "ds_read_b128 v[220:223], v241 offset:14336\n" \
    "v_mfma_f32_16x16x32_bf16 v[96:99], v[164:167], v[176:179], v[96:99]\n" \
    "s_cmp_eq_u32 s74, 98304\n" \
    "s_cselect_b32 s76, s77, 49152\n" \
    "v_mfma_f32_16x16x32_bf16 v[100:103], v[164:167], v[180:183], v[100:103]\n" \
    "s_add_u32 m0, m0, 4096\n" \
    "s_add_u32 s71, s71, 65536\n" \
    "v_mfma_f32_16x16x32_bf16 v[104:107], v[164:167], v[184:187], v[104:107]\n" \
    "buffer_load_dwordx4 v246, s[40:43], s71 offen sc1 lds\n" \
    "v_mfma_f32_16x16x32_bf16 v[108:111], v[164:167], v[188:191], v[108:111]\n" \
    "v_add_u32 v242, s76, v242\n" \
    "v_mfma_f32_16x16x32_bf16 v[112:115], v[168:171], v[176:179], v[112:115]\n" \
    "v_add_u32 v243, s76, v243\n" \
    "v_mfma_f32_16x16x32_bf16 v[116:119], v[168:171], v[180:183], v[116:119]\n" \
    "v_add_u32 v244, s76, v244\n" \
    "v_mfma_f32_16x16x32_bf16 v[120:123], v[168:171], v[184:187], v[120:123]\n" \
    "s_add_u32 m0, m0, 4096\n" \
    "s_add_u32 s71, s71, 65536\n" \
    "v_mfma_f32_16x16x32_bf16 v[124:127], v[168:171], v[188:191], v[124:127]\n" \
    "buffer_load_dwordx4 v246, s[40:43], s71 offen sc1 lds\n" \
    "v_mfma_f32_16x16x32_bf16 v[128:131], v[172:175], v[176:179], v[128:131]\n" \
    "v_add_u32 v245, s76, v245\n" \
    "v_mfma_f32_16x16x32_bf16 v[132:135], v[172:175], v[180:183], v[132:135]\n" \
    "v_add_u32 v240, s76, v240\n" \
    "v_mfma_f32_16x16x32_bf16 v[136:139], v[172:175], v[184:187], v[136:139]\n" \
    "v_add_u32 v241, s76, v241\n" \
    "v_mfma_f32_16x16x32_bf16 v[140:143], v[172:175], v[188:191], v[140:143]\n" \
    "s_waitcnt vmcnt(14) lgkmcnt(0)\n" \
    "v_mfma_f32_16x16x32_bf16 v[16:19], v[192:195], v[224:227], v[16:19]\n" \
    "s_barrier\n" \
    "v_mfma_f32_16x16x32_bf16 v[20:23], v[192:195], v[228:231], v[20:23]\n" \
    "ds_read_b64_tr_b16 v[176:177], v242 offset:0\n" \
    "v_mfma_f32_16x16x32_bf16 v[24:27], v[192:195], v[232:235], v[24:27]\n" \
    "s_add_u32 m0, s74, s79\n" \
    "s_mul_i32 s72, s73, s70\n" \
    "v_mfma_f32_16x16x32_bf16 v[28:31], v[192:195], v[236:239], v[28:31]\n" \
    "ds_read_b64_tr_b16 v[178:179], v242 offset:2048\n" \
    "buffer_load_dwordx4 v14, s[44:47], s72 offen lds\n" \
    "v_mfma_f32_16x16x32_bf16 v[32:35], v[196:199], v[224:227], v[32:35]\n" \
    "ds_read_b64_tr_b16 v[180:181], v243 offset:0\n" \
    "v_mfma_f32_16x16x32_bf16 v[36:39], v[196:199], v[228:231], v[36:39]\n" \
    "ds_read_b64_tr_b16 v[182:183], v243 offset:2048\n" \
    "v_mfma_f32_16x16x32_bf16 v[40:43], v[196:199], v[232:235], v[40:43]\n" \
    "s_add_u32 m0, m0, 1024\n" \
    "s_add_u32 s72, s72, s68\n" \
    "v_mfma_f32_16x16x32_bf16 v[44:47], v[196:199], v[236:239], v[44:47]\n" \
    "ds_read_b64_tr_b16 v[184:185], v244 offset:0\n" \
    "buffer_load_dwordx4 v14, s[44:47], s72 offen lds\n" \
    "v_mfma_f32_16x16x32_bf16 v[48:51], v[200:203], v[224:227], v[48:51]\n" \
    "ds_read_b64_tr_b16 v[186:187], v244 offset:2048\n" \
    "v_mfma_f32_16x16x32_bf16 v[52:55], v[200:203], v[228:231], v[52:55]\n" \
    "ds_read_b64_tr_b16 v[188:189], v245 offset:0\n" \
    "v_mfma_f32_16x16x32_bf16 v[56:59], v[200:203], v[232:235], v[56:59]\n" \
    "ds_read_b64_tr_b16 v[190:191], v245 offset:2048\n" \
    "v_mfma_f32_16x16x32_bf16 v[60:63], v[200:203], v[236:239], v[60:63]\n" \
    "s_add_u32 m0, m0, 1024\n" \
    "s_sub_u32 s72, s72, s69\n" \
    "v_mfma_f32_16x16x32_bf16 v[64:67], v[204:207], v[224:227], v[64:67]\n" \
    "ds_read_b128 v[144:147], v240 offset:0\n" \
    "buffer_load_dwordx4 v14, s[44:47], s72 offen lds\n" \
    "v_mfma_f32_16x16x32_bf16 v[68:71], v[204:207], v[228:231], v[68:71]\n" \
    "ds_read_b128 v[148:151], v240 offset:2048\n" \
    "v_mfma_f32_16x16x32_bf16 v[72:75], v[204:207], v[232:235], v[72:75]\n" \
    "ds_read_b128 v[152:155], v240 offset:4096\n" \
    "v_mfma_f32_16x16x32_bf16 v[76:79], v[204:207], v[236:239], v[76:79]\n" \
    "ds_read_b128 v[156:159], v240 offset:6144\n" \
    "v_mfma_f32_16x16x32_bf16 v[80:83], v[208:211], v[224:227], v[80:83]\n" \
    "s_add_u32 m0, m0, 1024\n" \
    "s_add_u32 s72, s72, s68\n" \
    "v_mfma_f32_16x16x32_bf16 v[84:87], v[208:211], v[228:231], v[84:87]\n" \
    "ds_read_b128 v[160:163], v240 offset:8192\n" \
    "buffer_load_dwordx4 v14, s[44:47], s72 offen lds\n" \
    "v_mfma_f32_16x16x32_bf16 v[88:91], v[208:211], v[232:235], v[88:91]\n" \
    "ds_read_b128 v[164:167], v240 offset:10240\n" \
    "v_mfma_f32_16x16x32_bf16 v[92:95], v[208:211], v[236:239], v[92:95]\n" \
    "ds_read_b128 v[168:171], v240 offset:12288\n" \
    "v_mfma_f32_16x16x32_bf16 v[96:99], v[212:215], v[224:227], v[96:99]\n" \
    "ds_read_b128 v[172:175], v240 offset:14336\n" \
    "v_mfma_f32_16x16x32_bf16 v[100:103], v[212:215], v[228:231], v[100:103]\n" \
    "s_add_u32 m0, s74, s78\n" \
    "s_mov_b32 s71, s73\n" \
    "v_mfma_f32_16x16x32_bf16 v[104:107], v[212:215], v[232:235], v[104:107]\n" \
    "buffer_load_dwordx4 v246, s[40:43], s71 offen sc1 lds\n" \
    "s_add_u32 s73, s73, 128\n" \
    "v_mfma_f32_16x16x32_bf16 v[108:111], v[212:215], v[236:239], v[108:111]\n" \
    "buffer_store_dwordx4 a[232:235], v15, s[48:51], s80 offen sc1\n" \
    "s_add_u32 s80, s80, s65\n" \
    "v_mfma_f32_16x16x32_bf16 v[112:115], v[216:219], v[224:227], v[112:115]\n" \
    "v_mfma_f32_16x16x32_bf16 v[116:119], v[216:219], v[228:231], v[116:119]\n" \
    "v_mfma_f32_16x16x32_bf16 v[120:123], v[216:219], v[232:235], v[120:123]\n" \
    "s_add_u32 m0, m0, 4096\n" \
    "s_add_u32 s71, s71, 65536\n" \
    "v_mfma_f32_16x16x32_bf16 v[124:127], v[216:219], v[236:239], v[124:127]\n" \
    "buffer_load_dwordx4 v246, s[40:43], s71 offen sc1 lds\n" \
    "s_and_b32 s73, s73, 2047\n" \
    "v_mfma_f32_16x16x32_bf16 v[128:131], v[220:223], v[224:227], v[128:131]\n" \
    "buffer_store_dwordx4 a[236:239], v15, s[48:51], s80 offen sc1\n" \
    "s_add_u32 s66, s66, s63\n" \
    "v_mfma_f32_16x16x32_bf16 v[132:135], v[220:223], v[228:231], v[132:135]\n" \
    "s_add_u32 s74, s74, s76\n" \
    "s_sub_u32 s75, s75, 1\n" \
    "v_mfma_f32_16x16x32_bf16 v[136:139], v[220:223], v[232:235], v[136:139]\n" \
    "v_mfma_f32_16x16x32_bf16 v[140:143], v[220:223], v[236:239], v[140:143]\n" \
    "s_cmp_lg_u32 s75, 0\n" \
    "s_waitcnt lgkmcnt(0)\n" \
    "v_mfma_f32_16x16x32_bf16 v[16:19], v[144:147], v[176:179], v[16:19]\n" \
    "ds_read_b64_tr_b16 v[224:225], v242 offset:4096\n" \
    "v_mfma_f32_16x16x32_bf16 v[20:23], v[144:147], v[180:183], v[20:23]\n" \
    "s_add_u32 m0, m0, 4096\n" \
    "s_add_u32 s71, s71, 65536\n" \
    "v_mfma_f32_16x16x32_bf16 v[24:27], v[144:147], v[184:187], v[24:27]\n" \
    "ds_read_b64_tr_b16 v[226:227], v242 offset:6144\n" \
    "buffer_load_dwordx4 v246, s[40:43], s71 offen sc1 lds\n" \
    "v_mfma_f32_16x16x32_bf16 v[28:31], v[144:147], v[188:191], v[28:31]\n" \
    "ds_read_b64_tr_b16 v[228:229], v243 offset:4096\n" \
    "v_mfma_f32_16x16x32_bf16 v[32:35], v[148:151], v[176:179], v[32:35]\n" \
    "ds_read_b64_tr_b16 v[230:231], v243 offset:6144\n" \
    "v_mfma_f32_16x16x32_bf16 v[36:39], v[148:151], v[180:183], v[36:39]\n" \
    "ds_read_b64_tr_b16 v[232:233], v244 offset:4096\n" \
    "v_mfma_f32_16x16x32_bf16 v[40:43], v[148:151], v[184:187], v[40:43]\n" \
    "s_add_u32 m0, m0, 4096\n" \
    "s_add_u32 s71, s71, 65536\n" \
    "v_mfma_f32_16x16x32_bf16 v[44:47], v[148:151], v[188:191], v[44:47]\n" \
    "ds_read_b64_tr_b16 v[234:235], v244 offset:6144\n" \
    "buffer_load_dwordx4 v246, s[40:43], s71 offen sc1 lds\n" \
    "v_mfma_f32_16x16x32_bf16 v[48:51], v[152:155], v[176:179], v[48:51]\n" \
    "ds_read_b64_tr_b16 v[236:237], v245 offset:4096\n" \
    "v_mfma_f32_16x16x32_bf16 v[52:55], v[152:155], v[180:183], v[52:55]\n" \
    "ds_read_b64_tr_b16 v[238:239], v245 offset:6144\n" \
    "v_mfma_f32_16x16x32_bf16 v[56:59], v[152:155], v[184:187], v[56:59]\n" \
    "ds_read_b128 v[192:195], v241 offset:0\n" \
    "v_mfma_f32_16x16x32_bf16 v[60:63], v[152:155], v[188:191], v[60:63]\n" \
    "s_add_u32 m0, m0, 4096\n" \
    "s_add_u32 s71, s71, 65536\n" \
    "v_mfma_f32_16x16x32_bf16 v[64:67], v[156:159], v[176:179], v[64:67]\n" \
    "ds_read_b128 v[196:199], v241 offset:2048\n" \
    "buffer_load_dwordx4 v246, s[40:43], s71 offen sc1 lds\n" \
    "v_mfma_f32_16x16x32_bf16 v[68:71], v[156:159], v[180:183], v[68:71]\n" \
    "ds_read_b128 v[200:203], v241 offset:4096\n" \
    "v_mfma_f32_16x16x32_bf16 v[72:75], v[156:159], v[184:187], v[72:75]\n" \
    "ds_read_b128 v[204:207], v241 offset:6144\n" \
    "v_mfma_f32_16x16x32_bf16 v[76:79], v[156:159], v[188:191], v[76:79]\n" \
    "ds_read_b128 v[208:211], v241 offset:8192\n" \
    "v_mfma_f32_16x16x32_bf16 v[80:83], v[160:163], v[176:179], v[80:83]\n" \
    "s_add_u32 m0, m0, 4096\n" \
    "s_add_u32 s71, s71, 65536\n" \
    "v_mfma_f32_16x16x32_bf16 v[84:87], v[160:163], v[180:183], v[84:87]\n" \
    "ds_read_b128 v[212:215], v241 offset:10240\n" \
    "buffer_load_dwordx4 v246, s[40:43], s71 offen sc1 lds\n" \
    "v_mfma_f32_16x16x32_bf16 v[88:91], v[160:163], v[184:187], v[88:91]\n" \
    "ds_read_b128 v[216:219], v241 offset:12288\n" \
    "v_mfma_f32_16x16x32_bf16 v[92:95], v[160:163], v[188:191], v[92:95]\n" \
    "ds_read_b128 v[220:223], v241 offset:14336\n" \
    "v_mfma_f32_16x16x32_bf16 v[96:99], v[164:167], v[176:179], v[96:99]\n" \
    "s_cmp_eq_u32 s74, 98304\n" \
    "s_cselect_b32 s76, s77, 49152\n" \
    "v_mfma_f32_16x16x32_bf16 v[100:103], v[164:167], v[180:183], v[100:103]\n" \
    "s_add_u32 m0, m0, 4096\n" \
    "s_add_u32 s71, s71, 65536\n" \
    "v_mfma_f32_16x16x32_bf16 v[104:107], v[164:167], v[184:187], v[104:107]\n" \
    "buffer_load_dwordx4 v246, s[40:43], s71 offen sc1 lds\n" \
    "v_mfma_f32_16x16x32_bf16 v[108:111], v[164:167], v[188:191], v[108:111]\n" \
    "v_add_u32 v242, s76, v242\n" \
    "v_mfma_f32_16x16x32_bf16 v[112:115], v[168:171], v[176:179], v[112:115]\n" \
    "v_add_u32 v243, s76, v243\n" \
    "v_mfma_f32_16x16x32_bf16 v[116:119], v[168:171], v[180:183], v[116:119]\n" \
    "v_add_u32 v244, s76, v244\n" \
    "v_mfma_f32_16x16x32_bf16 v[120:123], v[168:171], v[184:187], v[120:123]\n" \
    "s_add_u32 m0, m0, 4096\n" \
    "s_add_u32 s71, s71, 65536\n" \
    "v_mfma_f32_16x16x32_bf16 v[124:127], v[168:171], v[188:191], v[124:127]\n" \
    "buffer_load_dwordx4 v246, s[40:43], s71 offen sc1 lds\n" \
    "v_mfma_f32_16x16x32_bf16 v[128:131], v[172:175], v[176:179], v[128:131]\n" \
    "v_add_u32 v245, s76, v245\n" \
    "v_mfma_f32_16x16x32_bf16 v[132:135], v[172:175], v[180:183], v[132:135]\n" \
    "v_add_u32 v240, s76, v240\n" \
    "v_mfma_f32_16x16x32_bf16 v[136:139], v[172:175], v[184:187], v[136:139]\n" \
    "v_add_u32 v241, s76, v241\n" \
    "v_mfma_f32_16x16x32_bf16 v[140:143], v[172:175], v[188:191], v[140:143]\n" \
    "s_waitcnt vmcnt(14) lgkmcnt(0)\n" \
    "v_mfma_f32_16x16x32_bf16 v[16:19], v[192:195], v[224:227], v[16:19]\n" \
    "s_barrier\n" \
    "v_mfma_f32_16x16x32_bf16 v[20:23], v[192:195], v[228:231], v[20:23]\n" \
    "ds_read_b64_tr_b16 v[176:177], v242 offset:0\n" \
    "v_mfma_f32_16x16x32_bf16 v[24:27], v[192:195], v[232:235], v[24:27]\n" \
    "s_add_u32 m0, s74, s79\n" \
    "s_mul_i32 s72, s73, s70\n" \
    "v_mfma_f32_16x16x32_bf16 v[28:31], v[192:195], v[236:239], v[28:31]\n" \
    "ds_read_b64_tr_b16 v[178:179], v242 offset:2048\n" \
    "buffer_load_dwordx4 v14, s[44:47], s72 offen lds\n" \
    "v_mfma_f32_16x16x32_bf16 v[32:35], v[196:199], v[224:227], v[32:35]\n" \
    "ds_read_b64_tr_b16 v[180:181], v243 offset:0\n" \
    "v_mfma_f32_16x16x32_bf16 v[36:39], v[196:199], v[228:231], v[36:39]\n" \
    "ds_read_b64_tr_b16 v[182:183], v243 offset:2048\n" \
    "v_mfma_f32_16x16x32_bf16 v[40:43], v[196:199], v[232:235], v[40:43]\n" \
    "s_add_u32 m0, m0, 1024\n" \
    "s_add_u32 s72, s72, s68\n" \
    "v_mfma_f32_16x16x32_bf16 v[44:47], v[196:199], v[236:239], v[44:47]\n" \
    "ds_read_b64_tr_b16 v[184:185], v244 offset:0\n" \
    "buffer_load_dwordx4 v14, s[44:47], s72 offen lds\n" \
    "v_mfma_f32_16x16x32_bf16 v[48:51], v[200:203], v[224:227], v[48:51]\n" \
    "ds_read_b64_tr_b16 v[186:187], v244 offset:2048\n" \
    "v_mfma_f32_16x16x32_bf16 v[52:55], v[200:203], v[228:231], v[52:55]\n" \
    "ds_read_b64_tr_b16 v[188:189], v245 offset:0\n" \
    "v_mfma_f32_16x16x32_bf16 v[56:59], v[200:203], v[232:235], v[56:59]\n" \
    "ds_read_b64_tr_b16 v[190:191], v245 offset:2048\n" \
    "v_mfma_f32_16x16x32_bf16 v[60:63], v[200:203], v[236:239], v[60:63]\n" \
    "s_add_u32 m0, m0, 1024\n" \
    "s_sub_u32 s72, s72, s69\n" \
    "v_mfma_f32_16x16x32_bf16 v[64:67], v[204:207], v[224:227], v[64:67]\n" \
    "ds_read_b128 v[144:147], v240 offset:0\n" \
    "buffer_load_dwordx4 v14, s[44:47], s72 offen lds\n" \
    "v_mfma_f32_16x16x32_bf16 v[68:71], v[204:207], v[228:231], v[68:71]\n" \
    "ds_read_b128 v[148:151], v240 offset:2048\n" \
    "v_mfma_f32_16x16x32_bf16 v[72:75], v[204:207], v[232:235], v[72:75]\n" \
    "ds_read_b128 v[152:155], v240 offset:4096\n" \
    "v_mfma_f32_16x16x32_bf16 v[76:79], v[204:207], v[236:239], v[76:79]\n" \
    "ds_read_b128 v[156:159], v240 offset:6144\n" \
    "v_mfma_f32_16x16x32_bf16 v[80:83], v[208:211], v[224:227], v[80:83]\n" \
    "s_add_u32 m0, m0, 1024\n" \
    "s_add_u32 s72, s72, s68\n" \
    "v_mfma_f32_16x16x32_bf16 v[84:87], v[208:211], v[228:231], v[84:87]\n" \
    "ds_read_b128 v[160:163], v240 offset:8192\n" \
    "buffer_load_dwordx4 v14, s[44:47], s72 offen lds\n" \
    "v_mfma_f32_16x16x32_bf16 v[88:91], v[208:211], v[232:235], v[88:91]\n" \
    "ds_read_b128 v[164:167], v240 offset:10240\n" \
    "v_mfma_f32_16x16x32_bf16 v[92:95], v[208:211], v[236:239], v[92:95]\n" \
    "ds_read_b128 v[168:171], v240 offset:12288\n" \
    "v_mfma_f32_16x16x32_bf16 v[96:99], v[212:215], v[224:227], v[96:99]\n" \
    "ds_read_b128 v[172:175], v240 offset:14336\n" \
    "v_mfma_f32_16x16x32_bf16 v[100:103], v[212:215], v[228:231], v[100:103]\n" \
    "s_add_u32 m0, s74, s78\n" \
    "s_mov_b32 s71, s73\n" \
    "v_mfma_f32_16x16x32_bf16 v[104:107], v[212:215], v[232:235], v[104:107]\n" \
    "buffer_load_dwordx4 v246, s[40:43], s71 offen sc1 lds\n" \
    "s_add_u32 s73, s73, 128\n" \
    "v_mfma_f32_16x16x32_bf16 v[108:111], v[212:215], v[236:239], v[108:111]\n" \
    "buffer_store_dwordx4 a[240:243], v15, s[48:51], s66 offen sc1\n" \
    "s_add_u32 s80, s66, s65\n" \
    "v_mfma_f32_16x16x32_bf16 v[112:115], v[216:219], v[224:227], v[112:115]\n" \
    "v_mfma_f32_16x16x32_bf16 v[116:119], v[216:219], v[228:231], v[116:119]\n" \
    "v_mfma_f32_16x16x32_bf16 v[120:123], v[216:219], v[232:235], v[120:123]\n" \
    "s_add_u32 m0, m0, 4096\n" \
    "s_add_u32 s71, s71, 65536\n" \
    "v_mfma_f32_16x16x32_bf16 v[124:127], v[216:219], v[236:239], v[124:127]\n" \
    "buffer_load_dwordx4 v246, s[40:43], s71 offen sc1 lds\n" \
    "s_and_b32 s73, s73, 2047\n" \
    "v_mfma_f32_16x16x32_bf16 v[128:131], v[220:223], v[224:227], v[128:131]\n" \
    "buffer_store_dwordx4 a[244:247], v15, s[48:51], s80 offen sc1\n" \
    "s_add_u32 s80, s66, s64\n" \
    "v_mfma_f32_16x16x32_bf16 v[132:135], v[220:223], v[228:231], v[132:135]\n" \
    "s_add_u32 s74, s74, s76\n" \
    "s_sub_u32 s75, s75, 1\n" \
    "v_mfma_f32_16x16x32_bf16 v[136:139], v[220:223], v[232:235], v[136:139]\n" \
    "v_mfma_f32_16x16x32_bf16 v[140:143], v[220:223], v[236:239], v[140:143]\n" \
    "s_cmp_lg_u32 s75, 0\n" \
    "s_waitcnt lgkmcnt(0)\n" \
    "v_mfma_f32_16x16x32_bf16 v[16:19], v[144:147], v[176:179], v[16:19]\n" \
    "ds_read_b64_tr_b16 v[224:225], v242 offset:4096\n" \
    "v_mfma_f32_16x16x32_bf16 v[20:23], v[144:147], v[180:183], v[20:23]\n" \
    "s_add_u32 m0, m0, 4096\n" \
    "s_add_u32 s71, s71, 65536\n" \
    "v_mfma_f32_16x16x32_bf16 v[24:27], v[144:147], v[184:187], v[24:27]\n" \
    "ds_read_b64_tr_b16 v[226:227], v242 offset:6144\n" \
    "buffer_load_dwordx4 v246, s[40:43], s71 offen sc1 lds\n" \
    "v_mfma_f32_16x16x32_bf16 v[28:31], v[144:147], v[188:191], v[28:31]\n" \
    "ds_read_b64_tr_b16 v[228:229], v243 offset:4096\n" \
    "v_mfma_f32_16x16x32_bf16 v[32:35], v[148:151], v[176:179], v[32:35]\n" \
    "ds_read_b64_tr_b16 v[230:231], v243 offset:6144\n" \
    "v_mfma_f32_16x16x32_bf16 v[36:39], v[148:151], v[180:183], v[36:39]\n" \
    "ds_read_b64_tr_b16 v[232:233], v244 offset:4096\n" \
    "v_mfma_f32_16x16x32_bf16 v[40:43], v[148:151], v[184:187], v[40:43]\n" \
    "s_add_u32 m0, m0, 4096\n" \
    "s_add_u32 s71, s71, 65536\n" \
    "v_mfma_f32_16x16x32_bf16 v[44:47], v[148:151], v[188:191], v[44:47]\n" \
    "ds_read_b64_tr_b16 v[234:235], v244 offset:6144\n" \
    "buffer_load_dwordx4 v246, s[40:43], s71 offen sc1 lds\n" \
    "v_mfma_f32_16x16x32_bf16 v[48:51], v[152:155], v[176:179], v[48:51]\n" \
    "ds_read_b64_tr_b16 v[236:237], v245 offset:4096\n" \
    "v_mfma_f32_16x16x32_bf16 v[52:55], v[152:155], v[180:183], v[52:55]\n" \
    "ds_read_b64_tr_b16 v[238:239], v245 offset:6144\n" \
    "v_mfma_f32_16x16x32_bf16 v[56:59], v[152:155], v[184:187], v[56:59]\n" \
    "ds_read_b128 v[192:195], v241 offset:0\n" \
    "v_mfma_f32_16x16x32_bf16 v[60:63], v[152:155], v[188:191], v[60:63]\n" \
    "s_add_u32 m0, m0, 4096\n" \
    "s_add_u32 s71, s71, 65536\n" \
    "v_mfma_f32_16x16x32_bf16 v[64:67], v[156:159], v[176:179], v[64:67]\n" \
    "ds_read_b128 v[196:199], v241 offset:2048\n" \
    "buffer_load_dwordx4 v246, s[40:43], s71 offen sc1 lds\n" \
    "v_mfma_f32_16x16x32_bf16 v[68:71], v[156:159], v[180:183], v[68:71]\n" \
    "ds_read_b128 v[200:203], v241 offset:4096\n" \
    "v_mfma_f32_16x16x32_bf16 v[72:75], v[156:159], v[184:187], v[72:75]\n" \
    "ds_read_b128 v[204:207], v241 offset:6144\n" \
    "v_mfma_f32_16x16x32_bf16 v[76:79], v[156:159], v[188:191], v[76:79]\n" \
    "ds_read_b128 v[208:211], v241 offset:8192\n" \
    "v_mfma_f32_16x16x32_bf16 v[80:83], v[160:163], v[176:179], v[80:83]\n" \
    "s_add_u32 m0, m0, 4096\n" \
    "s_add_u32 s71, s71, 65536\n" \
    "v_mfma_f32_16x16x32_bf16 v[84:87], v[160:163], v[180:183], v[84:87]\n" \
    "ds_read_b128 v[212:215], v241 offset:10240\n" \
    "buffer_load_dwordx4 v246, s[40:43], s71 offen sc1 lds\n" \
    "v_mfma_f32_16x16x32_bf16 v[88:91], v[160:163], v[184:187], v[88:91]\n" \
    "ds_read_b128 v[216:219], v241 offset:12288\n" \
    "v_mfma_f32_16x16x32_bf16 v[92:95], v[160:163], v[188:191], v[92:95]\n" \
    "ds_read_b128 v[220:223], v241 offset:14336\n" \
    "v_mfma_f32_16x16x32_bf16 v[96:99], v[164:167], v[176:179], v[96:99]\n" \
    "s_cmp_eq_u32 s74, 98304\n" \
    "s_cselect_b32 s76, s77, 49152\n" \
    "v_mfma_f32_16x16x32_bf16 v[100:103], v[164:167], v[180:183], v[100:103]\n" \
    "s_add_u32 m0, m0, 4096\n" \
    "s_add_u32 s71, s71, 65536\n" \
    "v_mfma_f32_16x16x32_bf16 v[104:107], v[164:167], v[184:187], v[104:107]\n" \
    "buffer_load_dwordx4 v246, s[40:43], s71 offen sc1 lds\n" \
    "v_mfma_f32_16x16x32_bf16 v[108:111], v[164:167], v[188:191], v[108:111]\n" \
    "v_add_u32 v242, s76, v242\n" \
    "v_mfma_f32_16x16x32_bf16 v[112:115], v[168:171], v[176:179], v[112:115]\n" \
    "v_add_u32 v243, s76, v243\n" \
    "v_mfma_f32_16x16x32_bf16 v[116:119], v[168:171], v[180:183], v[116:119]\n" \
    "v_add_u32 v244, s76, v244\n" \
    "v_mfma_f32_16x16x32_bf16 v[120:123], v[168:171], v[184:187], v[120:123]\n" \
    "s_add_u32 m0, m0, 4096\n" \
    "s_add_u32 s71, s71, 65536\n" \
    "v_mfma_f32_16x16x32_bf16 v[124:127], v[168:171], v[188:191], v[124:127]\n" \
    "buffer_load_dwordx4 v246, s[40:43], s71 offen sc1 lds\n" \
    "v_mfma_f32_16x16x32_bf16 v[128:131], v[172:175], v[176:179], v[128:131]\n" \
    "v_add_u32 v245, s76, v245\n" \
    "v_mfma_f32_16x16x32_bf16 v[132:135], v[172:175], v[180:183], v[132:135]\n" \
    "v_add_u32 v240, s76, v240\n" \
    "v_mfma_f32_16x16x32_bf16 v[136:139], v[172:175], v[184:187], v[136:139]\n" \
    "v_add_u32 v241, s76, v241\n" \
    "v_mfma_f32_16x16x32_bf16 v[140:143], v[172:175], v[188:191], v[140:143]\n" \
    "s_waitcnt vmcnt(14) lgkmcnt(0)\n" \
    "v_mfma_f32_16x16x32_bf16 v[16:19], v[192:195], v[224:227], v[16:19]\n" \
    "s_barrier\n" \
    "v_mfma_f32_16x16x32_bf16 v[20:23], v[192:195], v[228:231], v[20:23]\n" \
    "ds_read_b64_tr_b16 v[176:177], v242 offset:0\n" \
    "v_mfma_f32_16x16x32_bf16 v[24:27], v[192:195], v[232:235], v[24:27]\n" \
    "s_add_u32 m0, s74, s79\n" \
    "s_mul_i32 s72, s73, s70\n" \
    "v_mfma_f32_16x16x32_bf16 v[28:31], v[192:195], v[236:239], v[28:31]\n" \
    "ds_read_b64_tr_b16 v[178:179], v242 offset:2048\n" \
    "buffer_load_dwordx4 v14, s[44:47], s72 offen lds\n" \
    "v_mfma_f32_16x16x32_bf16 v[32:35], v[196:199], v[224:227], v[32:35]\n" \
    "ds_read_b64_tr_b16 v[180:181], v243 offset:0\n" \
    "v_mfma_f32_16x16x32_bf16 v[36:39], v[196:199], v[228:231], v[36:39]\n" \
    "ds_read_b64_tr_b16 v[182:183], v243 offset:2048\n" \
    "v_mfma_f32_16x16x32_bf16 v[40:43], v[196:199], v[232:235], v[40:43]\n" \
    "s_add_u32 m0, m0, 1024\n" \
    "s_add_u32 s72, s72, s68\n" \
    "v_mfma_f32_16x16x32_bf16 v[44:47], v[196:199], v[236:239], v[44:47]\n" \
    "ds_read_b64_tr_b16 v[184:185], v244 offset:0\n" \
    "buffer_load_dwordx4 v14, s[44:47], s72 offen lds\n" \
    "v_mfma_f32_16x16x32_bf16 v[48:51], v[200:203], v[224:227], v[48:51]\n" \
    "ds_read_b64_tr_b16 v[186:187], v244 offset:2048\n" \
    "v_mfma_f32_16x16x32_bf16 v[52:55], v[200:203], v[228:231], v[52:55]\n" \
    "ds_read_b64_tr_b16 v[188:189], v245 offset:0\n" \
    "v_mfma_f32_16x16x32_bf16 v[56:59], v[200:203], v[232:235], v[56:59]\n" \
    "ds_read_b64_tr_b16 v[190:191], v245 offset:2048\n" \
    "v_mfma_f32_16x16x32_bf16 v[60:63], v[200:203], v[236:239], v[60:63]\n" \
    "s_add_u32 m0, m0, 1024\n" \
    "s_sub_u32 s72, s72, s69\n" \
    "v_mfma_f32_16x16x32_bf16 v[64:67], v[204:207], v[224:227], v[64:67]\n" \
    "ds_read_b128 v[144:147], v240 offset:0\n" \
    "buffer_load_dwordx4 v14, s[44:47], s72 offen lds\n" \
    "v_mfma_f32_16x16x32_bf16 v[68:71], v[204:207], v[228:231], v[68:71]\n" \
    "ds_read_b128 v[148:151], v240 offset:2048\n" \
    "v_mfma_f32_16x16x32_bf16 v[72:75], v[204:207], v[232:235], v[72:75]\n" \
    "ds_read_b128 v[152:155], v240 offset:4096\n" \
    "v_mfma_f32_16x16x32_bf16 v[76:79], v[204:207], v[236:239], v[76:79]\n" \
    "ds_read_b128 v[156:159], v240 offset:6144\n" \
    "v_mfma_f32_16x16x32_bf16 v[80:83], v[208:211], v[224:227], v[80:83]\n" \
    "s_add_u32 m0, m0, 1024\n" \
    "s_add_u32 s72, s72, s68\n" \
    "v_mfma_f32_16x16x32_bf16 v[84:87], v[208:211], v[228:231], v[84:87]\n" \
    "ds_read_b128 v[160:163], v240 offset:8192\n" \
    "buffer_load_dwordx4 v14, s[44:47], s72 offen lds\n" \
    "v_mfma_f32_16x16x32_bf16 v[88:91], v[208:211], v[232:235], v[88:91]\n" \
    "ds_read_b128 v[164:167], v240 offset:10240\n" \
    "v_mfma_f32_16x16x32_bf16 v[92:95], v[208:211], v[236:239], v[92:95]\n" \
    "ds_read_b128 v[168:171], v240 offset:12288\n" \
    "v_mfma_f32_16x16x32_bf16 v[96:99], v[212:215], v[224:227], v[96:99]\n" \
    "ds_read_b128 v[172:175], v240 offset:14336\n" \
    "v_mfma_f32_16x16x32_bf16 v[100:103], v[212:215], v[228:231], v[100:103]\n" \
    "s_add_u32 m0, s74, s78\n" \
    "s_mov_b32 s71, s73\n" \
    "v_mfma_f32_16x16x32_bf16 v[104:107], v[212:215], v[232:235], v[104:107]\n" \
    "buffer_load_dwordx4 v246, s[40:43], s71 offen sc1 lds\n" \
    "s_add_u32 s73, s73, 128\n" \
    "v_mfma_f32_16x16x32_bf16 v[108:111], v[212:215], v[236:239], v[108:111]\n" \
    "buffer_store_dwordx4 a[248:251], v15, s[48:51], s80 offen sc1\n" \
    "s_add_u32 s80, s80, s65\n" \
    "v_mfma_f32_16x16x32_bf16 v[112:115], v[216:219], v[224:227], v[112:115]\n" \
    "v_mfma_f32_16x16x32_bf16 v[116:119], v[216:219], v[228:231], v[116:119]\n" \
    "v_mfma_f32_16x16x32_bf16 v[120:123], v[216:219], v[232:235], v[120:123]\n" \
    "s_add_u32 m0, m0, 4096\n" \
    "s_add_u32 s71, s71, 65536\n" \
    "v_mfma_f32_16x16x32_bf16 v[124:127], v[216:219], v[236:239], v[124:127]\n" \
    "buffer_load_dwordx4 v246, s[40:43], s71 offen sc1 lds\n" \
    "s_and_b32 s73, s73, 2047\n" \
    "v_mfma_f32_16x16x32_bf16 v[128:131], v[220:223], v[224:227], v[128:131]\n" \
    "buffer_store_dwordx4 a[252:255], v15, s[48:51], s80 offen sc1\n" \
    "s_add_u32 s66, s66, s63\n" \
    "v_mfma_f32_16x16x32_bf16 v[132:135], v[220:223], v[228:231], v[132:135]\n" \
    "s_add_u32 s74, s74, s76\n" \
    "s_sub_u32 s75, s75, 1\n" \
    "v_mfma_f32_16x16x32_bf16 v[136:139], v[220:223], v[232:235], v[136:139]\n" \
    "v_mfma_f32_16x16x32_bf16 v[140:143], v[220:223], v[236:239], v[140:143]\n" \
    "s_cmp_lg_u32 s75, 0\n" \
    "Lg81_p1%=:\n" \
    "s_waitcnt lgkmcnt(0)\n" \
    "v_mfma_f32_16x16x32_bf16 v[16:19], v[144:147], v[176:179], v[16:19]\n" \
    "ds_read_b64_tr_b16 v[224:225], v242 offset:4096\n" \
    "v_mfma_f32_16x16x32_bf16 v[20:23], v[144:147], v[180:183], v[20:23]\n" \
    "s_add_u32 m0, m0, 4096\n" \
    "s_add_u32 s71, s71, 65536\n" \
    "v_mfma_f32_16x16x32_bf16 v[24:27], v[144:147], v[184:187], v[24:27]\n" \
    "ds_read_b64_tr_b16 v[226:227], v242 offset:6144\n" \
    "buffer_load_dwordx4 v246, s[40:43], s71 offen sc1 lds\n" \
    "v_mfma_f32_16x16x32_bf16 v[28:31], v[144:147], v[188:191], v[28:31]\n" \
    "ds_read_b64_tr_b16 v[228:229], v243 offset:4096\n" \
    "v_mfma_f32_16x16x32_bf16 v[32:35], v[148:151], v[176:179], v[32:35]\n" \
    "ds_read_b64_tr_b16 v[230:231], v243 offset:6144\n" \
    "v_mfma_f32_16x16x32_bf16 v[36:39], v[148:151], v[180:183], v[36:39]\n" \
    "ds_read_b64_tr_b16 v[232:233], v244 offset:4096\n" \
    "v_mfma_f32_16x16x32_bf16 v[40:43], v[148:151], v[184:187], v[40:43]\n" \
    "s_add_u32 m0, m0, 4096\n" \
    "s_add_u32 s71, s71, 65536\n" \
    "v_mfma_f32_16x16x32_bf16 v[44:47], v[148:151], v[188:191], v[44:47]\n" \
    "ds_read_b64_tr_b16 v[234:235], v244 offset:6144\n" \
    "buffer_load_dwordx4 v246, s[40:43], s71 offen sc1 lds\n" \
    "v_mfma_f32_16x16x32_bf16 v[48:51], v[152:155], v[176:179], v[48:51]\n" \
    "ds_read_b64_tr_b16 v[236:237], v245 offset:4096\n" \
    "v_mfma_f32_16x16x32_bf16 v[52:55], v[152:155], v[180:183], v[52:55]\n" \
    "ds_read_b64_tr_b16 v[238:239], v245 offset:6144\n" \
    "v_mfma_f32_16x16x32_bf16 v[56:59], v[152:155], v[184:187], v[56:59]\n" \
    "ds_read_b128 v[192:195], v241 offset:0\n" \
    "v_mfma_f32_16x16x32_bf16 v[60:63], v[152:155], v[188:191], v[60:63]\n" \
    "s_add_u32 m0, m0, 4096\n" \
    "s_add_u32 s71, s71, 65536\n" \
    "v_mfma_f32_16x16x32_bf16 v[64:67], v[156:159], v[176:179], v[64:67]\n" \
    "ds_read_b128 v[196:199], v241 offset:2048\n" \
    "buffer_load_dwordx4 v246, s[40:43], s71 offen sc1 lds\n" \
    "v_mfma_f32_16x16x32_bf16 v[68:71], v[156:159], v[180:183], v[68:71]\n" \
    "ds_read_b128 v[200:203], v241 offset:4096\n" \
    "v_mfma_f32_16x16x32_bf16 v[72:75], v[156:159], v[184:187], v[72:75]\n" \
    "ds_read_b128 v[204:207], v241 offset:6144\n" \
    "v_mfma_f32_16x16x32_bf16 v[76:79], v[156:159], v[188:191], v[76:79]\n" \
    "ds_read_b128 v[208:211], v241 offset:8192\n" \
    "v_mfma_f32_16x16x32_bf16 v[80:83], v[160:163], v[176:179], v[80:83]\n" \
    "s_add_u32 m0, m0, 4096\n" \
    "s_add_u32 s71, s71, 65536\n" \
    "v_mfma_f32_16x16x32_bf16 v[84:87], v[160:163], v[180:183], v[84:87]\n" \
    "ds_read_b128 v[212:215], v241 offset:10240\n" \
    "buffer_load_dwordx4 v246, s[40:43], s71 offen sc1 lds\n" \
    "v_mfma_f32_16x16x32_bf16 v[88:91], v[160:163], v[184:187], v[88:91]\n" \
    "ds_read_b128 v[216:219], v241 offset:12288\n" \
    "v_mfma_f32_16x16x32_bf16 v[92:95], v[160:163], v[188:191], v[92:95]\n" \
    "ds_read_b128 v[220:223], v241 offset:14336\n" \
    "v_mfma_f32_16x16x32_bf16 v[96:99], v[164:167], v[176:179], v[96:99]\n" \
    "s_cmp_eq_u32 s74, 98304\n" \
    "s_cselect_b32 s76, s77, 49152\n" \
    "v_mfma_f32_16x16x32_bf16 v[100:103], v[164:167], v[180:183], v[100:103]\n" \
    "s_add_u32 m0, m0, 4096\n" \
    "s_add_u32 s71, s71, 65536\n" \
    "v_mfma_f32_16x16x32_bf16 v[104:107], v[164:167], v[184:187], v[104:107]\n" \
    "buffer_load_dwordx4 v246, s[40:43], s71 offen sc1 lds\n" \
    "v_mfma_f32_16x16x32_bf16 v[108:111], v[164:167], v[188:191], v[108:111]\n" \
    "v_add_u32 v242, s76, v242\n" \
    "v_mfma_f32_16x16x32_bf16 v[112:115], v[168:171], v[176:179], v[112:115]\n" \
    "v_add_u32 v243, s76, v243\n" \
    "v_mfma_f32_16x16x32_bf16 v[116:119], v[168:171], v[180:183], v[116:119]\n" \
    "v_add_u32 v244, s76, v244\n" \
    "v_mfma_f32_16x16x32_bf16 v[120:123], v[168:171], v[184:187], v[120:123]\n" \
    "s_add_u32 m0, m0, 4096\n" \
    "s_add_u32 s71, s71, 65536\n" \
    "v_mfma_f32_16x16x32_bf16 v[124:127], v[168:171], v[188:191], v[124:127]\n" \
    "buffer_load_dwordx4 v246, s[40:43], s71 offen sc1 lds\n" \
    "v_mfma_f32_16x16x32_bf16 v[128:131], v[172:175], v[176:179], v[128:131]\n" \
    "v_add_u32 v245, s76, v245\n" \
    "v_mfma_f32_16x16x32_bf16 v[132:135], v[172:175], v[180:183], v[132:135]\n" \
    "v_add_u32 v240, s76, v240\n" \
    "v_mfma_f32_16x16x32_bf16 v[136:139], v[172:175], v[184:187], v[136:139]\n" \
    "v_add_u32 v241, s76, v241\n" \
    "v_mfma_f32_16x16x32_bf16 v[140:143], v[172:175], v[188:191], v[140:143]\n" \
    "Lg81_p2%=:\n" \
    "s_waitcnt vmcnt(12) lgkmcnt(0)\n" \
    "v_mfma_f32_16x16x32_bf16 v[16:19], v[192:195], v[224:227], v[16:19]\n" \
    "s_barrier\n" \
    "s_bitcmp1_b32 s81, s75\n" \
    "s_cbranch_scc1 Lg81%=_sw\n" \
    "Lg81%=_swx:\n" \
    "v_mfma_f32_16x16x32_bf16 v[20:23], v[192:195], v[228:231], v[20:23]\n" \
    "ds_read_b64_tr_b16 v[176:177], v242 offset:0\n" \
    "v_mfma_f32_16x16x32_bf16 v[24:27], v[192:195], v[232:235], v[24:27]\n" \
    "s_add_u32 m0, s74, s79\n" \
    "s_mul_i32 s72, s73, s70\n" \
    "v_mfma_f32_16x16x32_bf16 v[28:31], v[192:195], v[236:239], v[28:31]\n" \
    "ds_read_b64_tr_b16 v[178:179], v242 offset:2048\n" \
    "buffer_load_dwordx4 v14, s[44:47], s72 offen lds\n" \
    "v_mfma_f32_16x16x32_bf16 v[32:35], v[196:199], v[224:227], v[32:35]\n" \
    "ds_read_b64_tr_b16 v[180:181], v243 offset:0\n" \
    "v_mfma_f32_16x16x32_bf16 v[36:39], v[196:199], v[228:231], v[36:39]\n" \
    "ds_read_b64_tr_b16 v[182:183], v243 offset:2048\n" \
    "v_mfma_f32_16x16x32_bf16 v[40:43], v[196:199], v[232:235], v[40:43]\n" \
    "s_add_u32 m0, m0, 1024\n" \
    "s_add_u32 s72, s72, s68\n" \
    "v_mfma_f32_16x16x32_bf16 v[44:47], v[196:199], v[236:239], v[44:47]\n" \
    "ds_read_b64_tr_b16 v[184:185], v244 offset:0\n" \
    "buffer_load_dwordx4 v14, s[44:47], s72 offen lds\n" \
    "v_mfma_f32_16x16x32_bf16 v[48:51], v[200:203], v[224:227], v[48:51]\n" \
    "ds_read_b64_tr_b16 v[186:187], v244 offset:2048\n" \
    "v_mfma_f32_16x16x32_bf16 v[52:55], v[200:203], v[228:231], v[52:55]\n" \
    "ds_read_b64_tr_b16 v[188:189], v245 offset:0\n" \
    "v_mfma_f32_16x16x32_bf16 v[56:59], v[200:203], v[232:235], v[56:59]\n" \
    "ds_read_b64_tr_b16 v[190:191], v245 offset:2048\n" \
    "v_mfma_f32_16x16x32_bf16 v[60:63], v[200:203], v[236:239], v[60:63]\n" \
    "s_add_u32 m0, m0, 1024\n" \
    "s_sub_u32 s72, s72, s69\n" \
    "v_mfma_f32_16x16x32_bf16 v[64:67], v[204:207], v[224:227], v[64:67]\n" \
    "ds_read_b128 v[144:147], v240 offset:0\n" \
    "buffer_load_dwordx4 v14, s[44:47], s72 offen lds\n" \
    "v_mfma_f32_16x16x32_bf16 v[68:71], v[204:207], v[228:231], v[68:71]\n" \
    "ds_read_b128 v[148:151], v240 offset:2048\n" \
    "v_mfma_f32_16x16x32_bf16 v[72:75], v[204:207], v[232:235], v[72:75]\n" \
    "ds_read_b128 v[152:155], v240 offset:4096\n" \
    "v_mfma_f32_16x16x32_bf16 v[76:79], v[204:207], v[236:239], v[76:79]\n" \
    "ds_read_b128 v[156:159], v240 offset:6144\n" \
    "v_mfma_f32_16x16x32_bf16 v[80:83], v[208:211], v[224:227], v[80:83]\n" \
    "s_add_u32 m0, m0, 1024\n" \
    "s_add_u32 s72, s72, s68\n" \
    "v_mfma_f32_16x16x32_bf16 v[84:87], v[208:211], v[228:231], v[84:87]\n" \
    "ds_read_b128 v[160:163], v240 offset:8192\n" \
    "buffer_load_dwordx4 v14, s[44:47], s72 offen lds\n" \
    "v_mfma_f32_16x16x32_bf16 v[88:91], v[208:211], v[232:235], v[88:91]\n" \
    "ds_read_b128 v[164:167], v240 offset:10240\n" \
    "v_mfma_f32_16x16x32_bf16 v[92:95], v[208:211], v[236:239], v[92:95]\n" \
    "ds_read_b128 v[168:171], v240 offset:12288\n" \
    "v_mfma_f32_16x16x32_bf16 v[96:99], v[212:215], v[224:227], v[96:99]\n" \
    "ds_read_b128 v[172:175], v240 offset:14336\n" \
    "v_mfma_f32_16x16x32_bf16 v[100:103], v[212:215], v[228:231], v[100:103]\n" \
    "s_add_u32 m0, s74, s78\n" \
    "s_mov_b32 s71, s73\n" \
    "v_mfma_f32_16x16x32_bf16 v[104:107], v[212:215], v[232:235], v[104:107]\n" \
    "buffer_load_dwordx4 v246, s[40:43], s71 offen sc1 lds\n" \
    "s_add_u32 s73, s73, 128\n" \
    "v_mfma_f32_16x16x32_bf16 v[108:111], v[212:215], v[236:239], v[108:111]\n" \
    "v_mfma_f32_16x16x32_bf16 v[112:115], v[216:219], v[224:227], v[112:115]\n" \
    "v_mfma_f32_16x16x32_bf16 v[116:119], v[216:219], v[228:231], v[116:119]\n" \
    "v_mfma_f32_16x16x32_bf16 v[120:123], v[216:219], v[232:235], v[120:123]\n" \
    "s_add_u32 m0, m0, 4096\n" \
    "s_add_u32 s71, s71, 65536\n" \
    "v_mfma_f32_16x16x32_bf16 v[124:127], v[216:219], v[236:239], v[124:127]\n" \
    "buffer_load_dwordx4 v246, s[40:43], s71 offen sc1 lds\n" \
    "s_and_b32 s73, s73, 2047\n" \
    "v_mfma_f32_16x16x32_bf16 v[128:131], v[220:223], v[224:227], v[128:131]\n" \
    "v_mfma_f32_16x16x32_bf16 v[132:135], v[220:223], v[228:231], v[132:135]\n" \
    "s_add_u32 s74, s74, s76\n" \
    "s_sub_u32 s75, s75, 1\n" \
    "v_mfma_f32_16x16x32_bf16 v[136:139], v[220:223], v[232:235], v[136:139]\n" \
    "v_mfma_f32_16x16x32_bf16 v[140:143], v[220:223], v[236:239], v[140:143]\n" \
    "s_cmp_lg_u32 s75, 0\n" \
    "s_cbranch_scc1 Lg81_p1%=\n" \
    "v_cvt_pk_bf16_f32 v192, v16, v17\n" \
    "v_cvt_pk_bf16_f32 v193, v18, v19\n" \
    "v_cvt_pk_bf16_f32 v194, v32, v33\n" \
    "v_cvt_pk_bf16_f32 v195, v34, v35\n" \
    "v_accvgpr_write_b32 a192, v192\n" \
    "v_accvgpr_write_b32 a193, v193\n" \
    "v_accvgpr_write_b32 a194, v194\n" \
    "v_accvgpr_write_b32 a195, v195\n" \
    "v_cvt_pk_bf16_f32 v196, v20, v21\n" \
    "v_cvt_pk_bf16_f32 v197, v22, v23\n" \
    "v_cvt_pk_bf16_f32 v198, v36, v37\n" \
    "v_cvt_pk_bf16_f32 v199, v38, v39\n" \
    "v_accvgpr_write_b32 a196, v196\n" \
    "v_accvgpr_write_b32 a197, v197\n" \
    "v_accvgpr_write_b32 a198, v198\n" \
    "v_accvgpr_write_b32 a199, v199\n" \
    "v_cvt_pk_bf16_f32 v200, v24, v25\n" \
    "v_cvt_pk_bf16_f32 v201, v26, v27\n" \
    "v_cvt_pk_bf16_f32 v202, v40, v41\n" \
    "v_cvt_pk_bf16_f32 v203, v42, v43\n" \
    "v_accvgpr_write_b32 a200, v200\n" \
    "v_accvgpr_write_b32 a201, v201\n" \
    "v_accvgpr_write_b32 a202, v202\n" \
    "v_accvgpr_write_b32 a203, v203\n" \
    "v_cvt_pk_bf16_f32 v204, v28, v29\n" \
    "v_cvt_pk_bf16_f32 v205, v30, v31\n" \
    "v_cvt_pk_bf16_f32 v206, v44, v45\n" \
    "v_cvt_pk_bf16_f32 v207, v46, v47\n" \
    "v_accvgpr_write_b32 a204, v204\n" \
    "v_accvgpr_write_b32 a205, v205\n" \
    "v_accvgpr_write_b32 a206, v206\n" \
    "v_accvgpr_write_b32 a207, v207\n" \
    "v_cvt_pk_bf16_f32 v208, v48, v49\n" \
    "v_cvt_pk_bf16_f32 v209, v50, v51\n" \
    "v_cvt_pk_bf16_f32 v210, v64, v65\n" \
    "v_cvt_pk_bf16_f32 v211, v66, v67\n" \
    "v_accvgpr_write_b32 a208, v208\n" \
    "v_accvgpr_write_b32 a209, v209\n" \
    "v_accvgpr_write_b32 a210, v210\n" \
    "v_accvgpr_write_b32 a211, v211\n" \
    "v_cvt_pk_bf16_f32 v212, v52, v53\n" \
    "v_cvt_pk_bf16_f32 v213, v54, v55\n" \
    "v_cvt_pk_bf16_f32 v214, v68, v69\n" \
    "v_cvt_pk_bf16_f32 v215, v70, v71\n" \
    "v_accvgpr_write_b32 a212, v212\n" \
    "v_accvgpr_write_b32 a213, v213\n" \
    "v_accvgpr_write_b32 a214, v214\n" \
    "v_accvgpr_write_b32 a215, v215\n" \
    "v_cvt_pk_bf16_f32 v216, v56, v57\n" \
    "v_cvt_pk_bf16_f32 v217, v58, v59\n" \
    "v_cvt_pk_bf16_f32 v218, v72, v73\n" \
    "v_cvt_pk_bf16_f32 v219, v74, v75\n" \
    "v_accvgpr_write_b32 a216, v216\n" \
    "v_accvgpr_write_b32 a217, v217\n" \
    "v_accvgpr_write_b32 a218, v218\n" \
    "v_accvgpr_write_b32 a219, v219\n" \
    "v_cvt_pk_bf16_f32 v220, v60, v61\n" \
    "v_cvt_pk_bf16_f32 v221, v62, v63\n" \
    "v_cvt_pk_bf16_f32 v222, v76, v77\n" \
    "v_cvt_pk_bf16_f32 v223, v78, v79\n" \
    "v_accvgpr_write_b32 a220, v220\n" \
    "v_accvgpr_write_b32 a221, v221\n" \
    "v_accvgpr_write_b32 a222, v222\n" \
    "v_accvgpr_write_b32 a223, v223\n" \
    "v_cvt_pk_bf16_f32 v192, v80, v81\n" \
    "v_cvt_pk_bf16_f32 v193, v82, v83\n" \
    "v_cvt_pk_bf16_f32 v194, v96, v97\n" \
    "v_cvt_pk_bf16_f32 v195, v98, v99\n" \
    "v_accvgpr_write_b32 a224, v192\n" \
    "v_accvgpr_write_b32 a225, v193\n" \
    "v_accvgpr_write_b32 a226, v194\n" \
    "v_accvgpr_write_b32 a227, v195\n" \
    "v_cvt_pk_bf16_f32 v196, v84, v85\n" \
    "v_cvt_pk_bf16_f32 v197, v86, v87\n" \
    "v_cvt_pk_bf16_f32 v198, v100, v101\n" \
    "v_cvt_pk_bf16_f32 v199, v102, v103\n" \
    "v_accvgpr_write_b32 a228, v196\n" \
    "v_accvgpr_write_b32 a229, v197\n" \
    "v_accvgpr_write_b32 a230, v198\n" \
    "v_accvgpr_write_b32 a231, v199\n" \
    "v_cvt_pk_bf16_f32 v200, v88, v89\n" \
    "v_cvt_pk_bf16_f32 v201, v90, v91\n" \
    "v_cvt_pk_bf16_f32 v202, v104, v105\n" \
    "v_cvt_pk_bf16_f32 v203, v106, v107\n" \
    "v_accvgpr_write_b32 a232, v200\n" \
    "v_accvgpr_write_b32 a233, v201\n" \
    "v_accvgpr_write_b32 a234, v202\n" \
    "v_accvgpr_write_b32 a235, v203\n" \
    "v_cvt_pk_bf16_f32 v204, v92, v93\n" \
    "v_cvt_pk_bf16_f32 v205, v94, v95\n" \
    "v_cvt_pk_bf16_f32 v206, v108, v109\n" \
    "v_cvt_pk_bf16_f32 v207, v110, v111\n" \
    "v_accvgpr_write_b32 a236, v204\n" \
    "v_accvgpr_write_b32 a237, v205\n" \
    "v_accvgpr_write_b32 a238, v206\n" \
    "v_accvgpr_write_b32 a239, v207\n" \
    "v_cvt_pk_bf16_f32 v208, v112, v113\n" \
    "v_cvt_pk_bf16_f32 v209, v114, v115\n" \
    "v_cvt_pk_bf16_f32 v210, v128, v129\n" \
    "v_cvt_pk_bf16_f32 v211, v130, v131\n" \
    "v_accvgpr_write_b32 a240, v208\n" \
    "v_accvgpr_write_b32 a241, v209\n" \
    "v_accvgpr_write_b32 a242, v210\n" \
    "v_accvgpr_write_b32 a243, v211\n" \
    "v_cvt_pk_bf16_f32 v212, v116, v117\n" \
    "v_cvt_pk_bf16_f32 v213, v118, v119\n" \
    "v_cvt_pk_bf16_f32 v214, v132, v133\n" \
    "v_cvt_pk_bf16_f32 v215, v134, v135\n" \
    "v_accvgpr_write_b32 a244, v212\n" \
    "v_accvgpr_write_b32 a245, v213\n" \
    "v_accvgpr_write_b32 a246, v214\n" \
    "v_accvgpr_write_b32 a247, v215\n" \
    "v_cvt_pk_bf16_f32 v216, v120, v121\n" \
    "v_cvt_pk_bf16_f32 v217, v122, v123\n" \
    "v_cvt_pk_bf16_f32 v218, v136, v137\n" \
    "v_cvt_pk_bf16_f32 v219, v138, v139\n" \
    "v_accvgpr_write_b32 a248, v216\n" \
    "v_accvgpr_write_b32 a249, v217\n" \
    "v_accvgpr_write_b32 a250, v218\n" \
    "v_accvgpr_write_b32 a251, v219\n" \
    "v_cvt_pk_bf16_f32 v220, v124, v125\n" \
    "v_cvt_pk_bf16_f32 v221, v126, v127\n" \
    "v_cvt_pk_bf16_f32 v222, v140, v141\n" \
    "v_cvt_pk_bf16_f32 v223, v142, v143\n" \
    "v_accvgpr_write_b32 a252, v220\n" \
    "v_accvgpr_write_b32 a253, v221\n" \
    "v_accvgpr_write_b32 a254, v222\n" \
    "v_accvgpr_write_b32 a255, v223\n" \
    "s_branch Lg8_end%=\n" \
    "Lg81%=_sw:\n" \
    "s_cmp_eq_u32 s75, 3\n" \
    "s_cbranch_scc1 Lg81%=_sw2\n" \
    "buffer_atomic_add v252, v251, s[56:59], s67 offen\n" \
    "s_branch Lg81%=_swx\n" \
    "Lg81%=_sw2:\n" \
    "v_readlane_b32 s40, v255, 0\n" \
    "v_readlane_b32 s41, v255, 1\n" \
    "v_readlane_b32 s42, v255, 2\n" \
    "v_readlane_b32 s44, v255, 3\n" \
    "v_readlane_b32 s45, v255, 4\n" \
    "v_readlane_b32 s46, v255, 5\n" \
    "v_readlane_b32 s61, v255, 6\n" \
    "v_readlane_b32 s73, v255, 7\n" \
    "s_lshr_b32 s73, s73, 16\n" \
    "s_lshl_b32 s68, s61, 4\n" \
    "s_mul_i32 s69, s61, 12\n" \
    "s_lshr_b32 s70, s61, 1\n" \
    "v_mad_u32_u24 v14, v247, s61, v248\n" \
    "s_nop 0\n" \
    "s_bitcmp0_b32 s62, 7\n" \
    "s_cselect_b32 s42, s42, 0\n" \
    "s_cselect_b32 s46, s46, 0\n" \
    "s_branch Lg81%=_swx\n" \
    "Lg8_end%=:\n" \
    "v_readlane_b32 s48, v254, 8\n" \
    "v_readlane_b32 s49, v254, 9\n" \
    "v_readlane_b32 s50, v254, 10\n" \
    "v_readlane_b32 s66, v254, 11\n" \
    "v_readlane_b32 s63, v254, 12\n" \
    "v_readlane_b32 s64, v254, 13\n" \
    "v_readlane_b32 s65, v254, 14\n" \
    "v_readlane_b32 s67, v254, 15\n" \
    "s_bitcmp1_b32 s62, 8\n" \
    "s_cselect_b64 vcc, -1, 0\n" \
    "s_mov_b32 s58, s54\n" \
    "v_cndmask_b32 v15, v249, v250, vcc\n" \
    "v_mov_b32 v254, v255\n" \
    "s_bitcmp0_b32 s62, 6\n" \
    "s_cbranch_scc1 Lg8_tile%=\n" \
    "s_bitcmp0_b32 s62, 5\n" \
    "s_cbranch_scc1 Lg8_fl%=\n" \
    "buffer_store_dwordx4 a[192:195], v15, s[48:51], s66 offen\n" \
    "s_add_u32 s80, s66, s65\n" \
    "buffer_store_dwordx4 a[196:199], v15, s[48:51], s80 offen\n" \
    "s_add_u32 s80, s66, s64\n" \
    "buffer_store_dwordx4 a[200:203], v15, s[48:51], s80 offen\n" \
    "s_add_u32 s80, s80, s65\n" \
    "buffer_store_dwordx4 a[204:207], v15, s[48:51], s80 offen\n" \
    "s_add_u32 s66, s66, s63\n" \
    "buffer_store_dwordx4 a[208:211], v15, s[48:51], s66 offen\n" \
    "s_add_u32 s80, s66, s65\n" \
    "buffer_store_dwordx4 a[212:215], v15, s[48:51], s80 offen\n" \
    "s_add_u32 s80, s66, s64\n" \
    "buffer_store_dwordx4 a[216:219], v15, s[48:51], s80 offen\n" \
    "s_add_u32 s80, s80, s65\n" \
    "buffer_store_dwordx4 a[220:223], v15, s[48:51], s80 offen\n" \
    "s_add_u32 s66, s66, s63\n" \
    "buffer_store_dwordx4 a[224:227], v15, s[48:51], s66 offen\n" \
    "s_add_u32 s80, s66, s65\n" \
    "buffer_store_dwordx4 a[228:231], v15, s[48:51], s80 offen\n" \
    "s_add_u32 s80, s66, s64\n" \
    "buffer_store_dwordx4 a[232:235], v15, s[48:51], s80 offen\n" \
    "s_add_u32 s80, s80, s65\n" \
    "buffer_store_dwordx4 a[236:239], v15, s[48:51], s80 offen\n" \
    "s_add_u32 s66, s66, s63\n" \
    "buffer_store_dwordx4 a[240:243], v15, s[48:51], s66 offen\n" \
    "s_add_u32 s80, s66, s65\n" \
    "buffer_store_dwordx4 a[244:247], v15, s[48:51], s80 offen\n" \
    "s_add_u32 s80, s66, s64\n" \
    "buffer_store_dwordx4 a[248:251], v15, s[48:51], s80 offen\n" \
    "s_add_u32 s80, s80, s65\n" \
    "buffer_store_dwordx4 a[252:255], v15, s[48:51], s80 offen\n" \
    "s_add_u32 s66, s66, s63\n" \
    "s_branch Lg8_fx%=\n" \
    "Lg8_fl%=:\n" \
    "buffer_store_dwordx4 a[192:195], v15, s[48:51], s66 offen sc1\n" \
    "s_add_u32 s80, s66, s65\n" \
    "buffer_store_dwordx4 a[196:199], v15, s[48:51], s80 offen sc1\n" \
    "s_add_u32 s80, s66, s64\n" \
    "buffer_store_dwordx4 a[200:203], v15, s[48:51], s80 offen sc1\n" \
    "s_add_u32 s80, s80, s65\n" \
    "buffer_store_dwordx4 a[204:207], v15, s[48:51], s80 offen sc1\n" \
    "s_add_u32 s66, s66, s63\n" \
    "buffer_store_dwordx4 a[208:211], v15, s[48:51], s66 offen sc1\n" \
    "s_add_u32 s80, s66, s65\n" \
    "buffer_store_dwordx4 a[212:215], v15, s[48:51], s80 offen sc1\n" \
    "s_add_u32 s80, s66, s64\n" \
    "buffer_store_dwordx4 a[216:219], v15, s[48:51], s80 offen sc1\n" \
    "s_add_u32 s80, s80, s65\n" \
    "buffer_store_dwordx4 a[220:223], v15, s[48:51], s80 offen sc1\n" \
    "s_add_u32 s66, s66, s63\n" \
    "buffer_store_dwordx4 a[224:227], v15, s[48:51], s66 offen sc1\n" \
    "s_add_u32 s80, s66, s65\n" \
    "buffer_store_dwordx4 a[228:231], v15, s[48:51], s80 offen sc1\n" \
    "s_add_u32 s80, s66, s64\n" \
    "buffer_store_dwordx4 a[232:235], v15, s[48:51], s80 offen sc1\n" \
    "s_add_u32 s80, s80, s65\n" \
    "buffer_store_dwordx4 a[236:239], v15, s[48:51], s80 offen sc1\n" \
    "s_add_u32 s66, s66, s63\n" \
    "buffer_store_dwordx4 a[240:243], v15, s[48:51], s66 offen sc1\n" \
    "s_add_u32 s80, s66, s65\n" \
    "buffer_store_dwordx4 a[244:247], v15, s[48:51], s80 offen sc1\n" \
    "s_add_u32 s80, s66, s64\n" \
    "buffer_store_dwordx4 a[248:251], v15, s[48:51], s80 offen sc1\n" \
    "s_add_u32 s80, s80, s65\n" \
    "buffer_store_dwordx4 a[252:255], v15, s[48:51], s80 offen sc1\n" \
    "s_add_u32 s66, s66, s63\n" \
    "Lg8_fx%=:\n" \
    "s_waitcnt vmcnt(0) lgkmcnt(0)\n" \
    "s_bitcmp1_b32 s62, 7\n" \
    "s_cbranch_scc1 Lg8_ret%=\n" \
    "s_barrier\n" \
    "buffer_atomic_add v252, v251, s[52:55], s67 offen\n" \
    "s_mov_b32 s50, 0\n" \
    "s_mov_b32 s58, 0\n" \
    "s_branch Lg8_tile%=\n" \
    "Lg8_ret%=:\n"
#define GEMM4_ASM \
    "ds_read_b32 v254, v253\n" \
    "s_mov_b64 s[84:85], s[52:53]\n" \
    "s_mov_b32 s86, 65536\n" \
    "s_mov_b32 s87, 0x20000\n" \
    "s_mov_b32 s82, 57344\n" \
    "buffer_load_dword v100, off, s[84:87], s82 sc1\n" \
    "s_mov_b32 s77, -98304\n" \
    "s_mov_b32 s81, 20488\n" \
    "s_lshl_b32 s78, s60, 10\n" \
    "s_lshl_b32 s79, s60, 12\n" \
    "s_add_u32 s79, s79, 32768\n" \
    "s_mov_b32 s43, 0x20000\n" \
    "s_mov_b32 s47, 0x20000\n" \
    "s_mov_b32 s51, 0x20000\n" \
    "s_mov_b32 s59, 0x20000\n" \
    "s_mov_b64 s[48:49], s[52:53]\n" \
    "s_mov_b32 s50, 0\n" \
    "s_mov_b64 s[56:57], s[52:53]\n" \
    "s_mov_b32 s58, 0\n" \
    "s_mov_b32 s63, 0\n" \
    "s_mov_b32 s64, 0\n" \
    "s_mov_b32 s65, 0\n" \
    "s_mov_b32 s66, 0\n" \
    "s_mov_b32 s67, 0\n" \
    "s_mov_b32 s74, 0\n" \
    "v_mov_b32 v15, v249\n" \
    "s_waitcnt lgkmcnt(0)\n" \
    "v_readlane_b32 s40, v254, 0\n" \
    "v_readlane_b32 s41, v254, 1\n" \
    "v_readlane_b32 s42, v254, 2\n" \
    "v_readlane_b32 s44, v254, 3\n" \
    "v_readlane_b32 s45, v254, 4\n" \
    "v_readlane_b32 s46, v254, 5\n" \
    "v_readlane_b32 s61, v254, 6\n" \
    "v_readlane_b32 s73, v254, 7\n" \
    "s_lshr_b32 s73, s73, 16\n" \
    "s_lshl_b32 s68, s61, 4\n" \
    "s_mul_i32 s69, s61, 12\n" \
    "s_lshr_b32 s70, s61, 1\n" \
    "v_mad_u32_u24 v14, v247, s61, v248\n" \
    "s_nop 0\n" \
    "s_add_u32 m0, s74, s79\n" \
    "s_mul_i32 s72, s73, s70\n" \
    "s_nop 0\n" \
    "buffer_load_dwordx4 v14, s[44:47], s72 offen lds\n" \
    "s_add_u32 m0, m0, 1024\n" \
    "s_add_u32 s72, s72, s68\n" \
    "s_nop 0\n" \
    "buffer_load_dwordx4 v14, s[44:47], s72 offen lds\n" \
    "s_add_u32 m0, m0, 1024\n" \
    "s_sub_u32 s72, s72, s69\n" \
    "s_nop 0\n" \
    "buffer_load_dwordx4 v14, s[44:47], s72 offen lds\n" \
    "s_add_u32 m0, m0, 1024\n" \
    "s_add_u32 s72, s72, s68\n" \
    "s_nop 0\n" \
    "buffer_load_dwordx4 v14, s[44:47], s72 offen lds\n" \
    "s_add_u32 m0, s74, s78\n" \
    "s_mov_b32 s71, s73\n" \
    "s_add_u32 s73, s73, 128\n" \
    "s_and_b32 s73, s73, 2047\n" \
    "s_nop 0\n" \
    "buffer_load_dwordx4 v246, s[40:43], s71 offen sc1 lds\n" \
    "s_add_u32 m0, m0, 4096\n" \
    "s_add_u32 s71, s71, 65536\n" \
    "s_nop 0\n" \
    "buffer_load_dwordx4 v246, s[40:43], s71 offen sc1 lds\n" \
    "s_add_u32 m0, m0, 4096\n" \
    "s_add_u32 s71, s71, 65536\n" \
    "s_nop 0\n" \
    "buffer_load_dwordx4 v246, s[40:43], s71 offen sc1 lds\n" \
    "s_add_u32 m0, m0, 4096\n" \
    "s_add_u32 s71, s71, 65536\n" \
    "s_nop 0\n" \
    "buffer_load_dwordx4 v246, s[40:43], s71 offen sc1 lds\n" \
    "s_add_u32 s74, s74, 49152\n" \
    "s_cmp_eq_u32 s74, 147456\n" \
    "s_cselect_b32 s74, 0, s74\n" \
    "s_add_u32 m0, s74, s79\n" \
    "s_mul_i32 s72, s73, s70\n" \
    "s_nop 0\n" \
    "buffer_load_dwordx4 v14, s[44:47], s72 offen lds\n" \
    "s_add_u32 m0, m0, 1024\n" \
    "s_add_u32 s72, s72, s68\n" \
    "s_nop 0\n" \
    "buffer_load_dwordx4 v14, s[44:47], s72 offen lds\n" \
    "s_add_u32 m0, m0, 1024\n" \
    "s_sub_u32 s72, s72, s69\n" \
    "s_nop 0\n" \
    "buffer_load_dwordx4 v14, s[44:47], s72 offen lds\n" \
    "s_add_u32 m0, m0, 1024\n" \
    "s_add_u32 s72, s72, s68\n" \
    "s_nop 0\n" \
    "buffer_load_dwordx4 v14, s[44:47], s72 offen lds\n" \
    "s_add_u32 m0, s74, s78\n" \
    "s_mov_b32 s71, s73\n" \
    "s_add_u32 s73, s73, 128\n" \
    "s_and_b32 s73, s73, 2047\n" \
    "s_nop 0\n" \
    "buffer_load_dwordx4 v246, s[40:43], s71 offen sc1 lds\n" \
    "s_add_u32 m0, m0, 4096\n" \
    "s_add_u32 s71, s71, 65536\n" \
    "s_nop 0\n" \
    "buffer_load_dwordx4 v246, s[40:43], s71 offen sc1 lds\n" \
    "s_add_u32 m0, m0, 4096\n" \
    "s_add_u32 s71, s71, 65536\n" \
    "s_nop 0\n" \
    "buffer_load_dwordx4 v246, s[40:43], s71 offen sc1 lds\n" \
    "s_add_u32 m0, m0, 4096\n" \
    "s_add_u32 s71, s71, 65536\n" \
    "s_nop 0\n" \
    "buffer_load_dwordx4 v246, s[40:43], s71 offen sc1 lds\n" \
    "s_add_u32 s74, s74, 49152\n" \
    "s_cmp_eq_u32 s74, 147456\n" \
    "s_cselect_b32 s74, 0, s74\n" \
    "s_add_u32 m0, s74, s79\n" \
    "s_mul_i32 s72, s73, s70\n" \
    "s_nop 0\n" \
    "buffer_load_dwordx4 v14, s[44:47], s72 offen lds\n" \
    "s_add_u32 m0, m0, 1024\n" \
    "s_add_u32 s72, s72, s68\n" \
    "s_nop 0\n" \
    "buffer_load_dwordx4 v14, s[44:47], s72 offen lds\n" \
    "s_add_u32 m0, m0, 1024\n" \
    "s_sub_u32 s72, s72, s69\n" \
    "s_nop 0\n" \
    "buffer_load_dwordx4 v14, s[44:47], s72 offen lds\n" \
    "s_add_u32 m0, m0, 1024\n" \
    "s_add_u32 s72, s72, s68\n" \
    "s_nop 0\n" \
    "buffer_load_dwordx4 v14, s[44:47], s72 offen lds\n" \
    "s_add_u32 m0, s74, s78\n" \
    "s_mov_b32 s71, s73\n" \
    "s_add_u32 s73, s73, 128\n" \
    "s_and_b32 s73, s73, 2047\n" \
    "s_add_u32 s74, s74, 49152\n" \
    "s_cmp_eq_u32 s74, 147456\n" \
    "s_cselect_b32 s74, 0, s74\n" \
    "s_waitcnt vmcnt(12)\n" \
    "s_barrier\n" \
    "ds_read_b64_tr_b16 v[176:177], v242 offset:0\n" \
    "ds_read_b64_tr_b16 v[178:179], v242 offset:2048\n" \
    "ds_read_b64_tr_b16 v[180:181], v243 offset:0\n" \
    "ds_read_b64_tr_b16 v[182:183], v243 offset:2048\n" \
    "ds_read_b64_tr_b16 v[184:185], v244 offset:0\n" \
    "ds_read_b64_tr_b16 v[186:187], v244 offset:2048\n" \
    "ds_read_b64_tr_b16 v[188:189], v245 offset:0\n" \
    "ds_read_b64_tr_b16 v[190:191], v245 offset:2048\n" \
    "ds_read_b128 v[144:147], v240 offset:0\n" \
    "ds_read_b128 v[148:151], v240 offset:2048\n" \
    "ds_read_b128 v[152:155], v240 offset:4096\n" \
    "ds_read_b128 v[156:159], v240 offset:6144\n" \
    "Lg4_tile%=:\n" \
    "v_readlane_b32 s62, v254, 7\n" \
    "ds_read_b32 v255, v253 offset:256\n" \
    "v_add_u32 v253, 256, v253\n" \
    "s_mov_b32 s75, 16\n" \
    "s_bitcmp1_b32 s62, 0\n" \
    "s_cbranch_scc1 Lg41_in%=\n" \
    "Lg40_in%=:\n" \
    "s_waitcnt lgkmcnt(0)\n" \
    "v_mfma_f32_16x16x32_bf16 v[16:19], v[176:179], v[144:147], 0\n" \
    "ds_read_b64_tr_b16 v[224:225], v242 offset:4096\n" \
    "buffer_load_dwordx4 v246, s[40:43], s71 offen sc1 lds\n" \
    "v_mfma_f32_16x16x32_bf16 v[20:23], v[180:183], v[144:147], 0\n" \
    "ds_read_b64_tr_b16 v[226:227], v242 offset:6144\n" \
    "ds_read_b64_tr_b16 v[228:229], v243 offset:4096\n" \
    "v_mfma_f32_16x16x32_bf16 v[24:27], v[184:187], v[144:147], 0\n" \
    "s_add_u32 m0, m0, 4096\n" \
    "s_add_u32 s71, s71, 65536\n" \
    "v_mfma_f32_16x16x32_bf16 v[28:31], v[188:191], v[144:147], 0\n" \
    "ds_read_b64_tr_b16 v[230:231], v243 offset:6144\n" \
    "ds_read_b64_tr_b16 v[232:233], v244 offset:4096\n" \
    "v_mfma_f32_16x16x32_bf16 v[32:35], v[176:179], v[148:151], 0\n" \
    "ds_read_b64_tr_b16 v[234:235], v244 offset:6144\n" \
    "buffer_load_dwordx4 v246, s[40:43], s71 offen sc1 lds\n" \
    "v_mfma_f32_16x16x32_bf16 v[36:39], v[180:183], v[148:151], 0\n" \
    "ds_read_b64_tr_b16 v[236:237], v245 offset:4096\n" \
    "ds_read_b64_tr_b16 v[238:239], v245 offset:6144\n" \
    "v_mfma_f32_16x16x32_bf16 v[40:43], v[184:187], v[148:151], 0\n" \
    "s_add_u32 m0, m0, 4096\n" \
    "s_add_u32 s71, s71, 65536\n" \
    "v_mfma_f32_16x16x32_bf16 v[44:47], v[188:191], v[148:151], 0\n" \
    "ds_read_b128 v[192:195], v241 offset:0\n" \
    "ds_read_b128 v[196:199], v241 offset:2048\n" \
    "v_mfma_f32_16x16x32_bf16 v[48:51], v[176:179], v[152:155], 0\n" \
    "ds_read_b128 v[200:203], v241 offset:4096\n" \
    "buffer_load_dwordx4 v246, s[40:43], s71 offen sc1 lds\n" \
    "v_mfma_f32_16x16x32_bf16 v[52:55], v[180:183], v[152:155], 0\n" \
    "ds_read_b128 v[204:207], v241 offset:6144\n" \
    "v_mfma_f32_16x16x32_bf16 v[56:59], v[184:187], v[152:155], 0\n" \
    "s_add_u32 m0, m0, 4096\n" \
    "s_add_u32 s71, s71, 65536\n" \
    "v_mfma_f32_16x16x32_bf16 v[60:63], v[188:191], v[152:155], 0\n" \
    "s_cmp_eq_u32 s74, 98304\n" \
    "s_cselect_b32 s76, s77, 49152\n" \
    "v_mfma_f32_16x16x32_bf16 v[64:67], v[176:179], v[156:159], 0\n" \
    "buffer_load_dwordx4 v246, s[40:43], s71 offen sc1 lds\n" \
    "v_mfma_f32_16x16x32_bf16 v[68:71], v[180:183], v[156:159], 0\n" \
    "v_add_u32 v242, s76, v242\n" \
    "v_add_u32 v243, s76, v243\n" \
    "v_mfma_f32_16x16x32_bf16 v[72:75], v[184:187], v[156:159], 0\n" \
    "v_add_u32 v244, s76, v244\n" \
    "v_add_u32 v245, s76, v245\n" \
    "v_mfma_f32_16x16x32_bf16 v[76:79], v[188:191], v[156:159], 0\n" \
    "v_add_u32 v240, s76, v240\n" \
    "v_add_u32 v241, s76, v241\n" \
    "s_branch Lg40_p2%=\n" \
    "Lg40_p1%=:\n" \
    "s_waitcnt lgkmcnt(0)\n" \
    "v_mfma_f32_16x16x32_bf16 v[16:19], v[176:179], v[144:147], v[16:19]\n" \
    "ds_read_b64_tr_b16 v[224:225], v242 offset:4096\n" \
    "buffer_load_dwordx4 v246, s[40:43], s71 offen sc1 lds\n" \
    "v_mfma_f32_16x16x32_bf16 v[20:23], v[180:183], v[144:147], v[20:23]\n" \
    "ds_read_b64_tr_b16 v[226:227], v242 offset:6144\n" \
    "ds_read_b64_tr_b16 v[228:229], v243 offset:4096\n" \
    "v_mfma_f32_16x16x32_bf16 v[24:27], v[184:187], v[144:147], v[24:27]\n" \
    "s_add_u32 m0, m0, 4096\n" \
    "s_add_u32 s71, s71, 65536\n" \
    "v_mfma_f32_16x16x32_bf16 v[28:31], v[188:191], v[144:147], v[28:31]\n" \
    "ds_read_b64_tr_b16 v[230:231], v243 offset:6144\n" \
    "ds_read_b64_tr_b16 v[232:233], v244 offset:4096\n" \
    "v_mfma_f32_16x16x32_bf16 v[32:35], v[176:179], v[148:151], v[32:35]\n" \
    "ds_read_b64_tr_b16 v[234:235], v244 offset:6144\n" \
    "buffer_load_dwordx4 v246, s[40:43], s71 offen sc1 lds\n" \
    "v_mfma_f32_16x16x32_bf16 v[36:39], v[180:183], v[148:151], v[36:39]\n" \
    "ds_read_b64_tr_b16 v[236:237], v245 offset:4096\n" \
    "ds_read_b64_tr_b16 v[238:239], v245 offset:6144\n" \
    "v_mfma_f32_16x16x32_bf16 v[40:43], v[184:187], v[148:151], v[40:43]\n" \
    "s_add_u32 m0, m0, 4096\n" \
    "s_add_u32 s71, s71, 65536\n" \
    "v_mfma_f32_16x16x32_bf16 v[44:47], v[188:191], v[148:151], v[44:47]\n" \
    "ds_read_b128 v[192:195], v241 offset:0\n" \
    "ds_read_b128 v[196:199], v241 offset:2048\n" \
    "v_mfma_f32_16x16x32_bf16 v[48:51], v[176:179], v[152:155], v[48:51]\n" \
    "ds_read_b128 v[200:203], v241 offset:4096\n" \
    "buffer_load_dwordx4 v246, s[40:43], s71 offen sc1 lds\n" \
    "v_mfma_f32_16x16x32_bf16 v[52:55], v[180:183], v[152:155], v[52:55]\n" \
    "ds_read_b128 v[204:207], v241 offset:6144\n" \
    "v_mfma_f32_16x16x32_bf16 v[56:59], v[184:187], v[152:155], v[56:59]\n" \
    "s_add_u32 m0, m0, 4096\n" \
    "s_add_u32 s71, s71, 65536\n" \
    "v_mfma_f32_16x16x32_bf16 v[60:63], v[188:191], v[152:155], v[60:63]\n" \
    "s_cmp_eq_u32 s74, 98304\n" \
    "s_cselect_b32 s76, s77, 49152\n" \
    "v_mfma_f32_16x16x32_bf16 v[64:67], v[176:179], v[156:159], v[64:67]\n" \
    "buffer_load_dwordx4 v246, s[40:43], s71 offen sc1 lds\n" \
    "v_mfma_f32_16x16x32_bf16 v[68:71], v[180:183], v[156:159], v[68:71]\n" \
    "v_add_u32 v242, s76, v242\n" \
    "v_add_u32 v243, s76, v243\n" \
    "v_mfma_f32_16x16x32_bf16 v[72:75], v[184:187], v[156:159], v[72:75]\n" \
    "v_add_u32 v244, s76, v244\n" \
    "v_add_u32 v245, s76, v245\n" \
    "v_mfma_f32_16x16x32_bf16 v[76:79], v[188:191], v[156:159], v[76:79]\n" \
    "v_add_u32 v240, s76, v240\n" \
    "v_add_u32 v241, s76, v241\n" \
    "Lg40_p2%=:\n" \
    "s_waitcnt vmcnt(8) lgkmcnt(0)\n" \
    "v_mfma_f32_16x16x32_bf16 v[16:19], v[224:227], v[192:195], v[16:19]\n" \
    "s_barrier\n" \
    "s_bitcmp1_b32 s81, s75\n" \
    "s_cbranch_scc1 Lg40%=_sw\n" \
    "Lg40%=_swx:\n" \
    "v_mfma_f32_16x16x32_bf16 v[20:23], v[228:231], v[192:195], v[20:23]\n" \
    "ds_read_b64_tr_b16 v[176:177], v242 offset:0\n" \
    "s_add_u32 m0, s74, s79\n" \
    "s_mul_i32 s72, s73, s70\n" \
    "v_mfma_f32_16x16x32_bf16 v[24:27], v[232:235], v[192:195], v[24:27]\n" \
    "ds_read_b64_tr_b16 v[178:179], v242 offset:2048\n" \
    "buffer_load_dwordx4 v14, s[44:47], s72 offen lds\n" \
    "v_mfma_f32_16x16x32_bf16 v[28:31], v[236:239], v[192:195], v[28:31]\n" \
    "ds_read_b64_tr_b16 v[180:181], v243 offset:0\n" \
    "ds_read_b64_tr_b16 v[182:183], v243 offset:2048\n" \
    "v_mfma_f32_16x16x32_bf16 v[32:35], v[224:227], v[196:199], v[32:35]\n" \
    "ds_read_b64_tr_b16 v[184:185], v244 offset:0\n" \
    "s_add_u32 m0, m0, 1024\n" \
    "s_add_u32 s72, s72, s68\n" \
    "v_mfma_f32_16x16x32_bf16 v[36:39], v[228:231], v[196:199], v[36:39]\n" \
    "ds_read_b64_tr_b16 v[186:187], v244 offset:2048\n" \
    "buffer_load_dwordx4 v14, s[44:47], s72 offen lds\n" \
    "v_mfma_f32_16x16x32_bf16 v[40:43], v[232:235], v[196:199], v[40:43]\n" \
    "ds_read_b64_tr_b16 v[188:189], v245 offset:0\n" \
    "ds_read_b64_tr_b16 v[190:191], v245 offset:2048\n" \
    "v_mfma_f32_16x16x32_bf16 v[44:47], v[236:239], v[196:199], v[44:47]\n" \
    "ds_read_b128 v[144:147], v240 offset:0\n" \
    "s_add_u32 m0, m0, 1024\n" \
    "s_sub_u32 s72, s72, s69\n" \
    "v_mfma_f32_16x16x32_bf16 v[48:51], v[224:227], v[200:203], v[48:51]\n" \
    "ds_read_b128 v[148:151], v240 offset:2048\n" \
    "buffer_load_dwordx4 v14, s[44:47], s72 offen lds\n" \
    "v_mfma_f32_16x16x32_bf16 v[52:55], v[228:231], v[200:203], v[52:55]\n" \
    "ds_read_b128 v[152:155], v240 offset:4096\n" \
    "ds_read_b128 v[156:159], v240 offset:6144\n" \
    "v_mfma_f32_16x16x32_bf16 v[56:59], v[232:235], v[200:203], v[56:59]\n" \
    "s_add_u32 m0, m0, 1024\n" \
    "s_add_u32 s72, s72, s68\n" \
    "v_mfma_f32_16x16x32_bf16 v[60:63], v[236:239], v[200:203], v[60:63]\n" \
    "buffer_load_dwordx4 v14, s[44:47], s72 offen lds\n" \
    "v_mfma_f32_16x16x32_bf16 v[64:67], v[224:227], v[204:207], v[64:67]\n" \
    "s_add_u32 m0, s74, s78\n" \
    "s_mov_b32 s71, s73\n" \
    "v_mfma_f32_16x16x32_bf16 v[68:71], v[228:231], v[204:207], v[68:71]\n" \
    "s_add_u32 s73, s73, 128\n" \
    "s_and_b32 s73, s73, 2047\n" \
    "v_mfma_f32_16x16x32_bf16 v[72:75], v[232:235], v[204:207], v[72:75]\n" \
    "s_add_u32 s74, s74, s76\n" \
    "s_sub_u32 s75, s75, 1\n" \
    "v_mfma_f32_16x16x32_bf16 v[76:79], v[236:239], v[204:207], v[76:79]\n" \
    "s_cmp_lg_u32 s75, 0\n" \
    "s_cbranch_scc1 Lg40_p1%=\n" \
    "v_cvt_pk_bf16_f32 v192, v16, v17\n" \
    "v_cvt_pk_bf16_f32 v193, v18, v19\n" \
    "v_cvt_pk_bf16_f32 v194, v20, v21\n" \
    "v_cvt_pk_bf16_f32 v195, v22, v23\n" \
    "v_accvgpr_write_b32 a192, v192\n" \
    "v_accvgpr_write_b32 a193, v193\n" \
    "v_accvgpr_write_b32 a194, v194\n" \
    "v_accvgpr_write_b32 a195, v195\n" \
    "v_cvt_pk_bf16_f32 v196, v24, v25\n" \
    "v_cvt_pk_bf16_f32 v197, v26, v27\n" \
    "v_cvt_pk_bf16_f32 v198, v28, v29\n" \
    "v_cvt_pk_bf16_f32 v199, v30, v31\n" \
    "v_accvgpr_write_b32 a196, v196\n" \
    "v_accvgpr_write_b32 a197, v197\n" \
    "v_accvgpr_write_b32 a198, v198\n" \
    "v_accvgpr_write_b32 a199, v199\n" \
    "v_cvt_pk_bf16_f32 v200, v32, v33\n" \
    "v_cvt_pk_bf16_f32 v201, v34, v35\n" \
    "v_cvt_pk_bf16_f32 v202, v36, v37\n" \
    "v_cvt_pk_bf16_f32 v203, v38, v39\n" \
    "v_accvgpr_write_b32 a200, v200\n" \
    "v_accvgpr_write_b32 a201, v201\n" \
    "v_accvgpr_write_b32 a202, v202\n" \
    "v_accvgpr_write_b32 a203, v203\n" \
    "v_cvt_pk_bf16_f32 v204, v40, v41\n" \
    "v_cvt_pk_bf16_f32 v205, v42, v43\n" \
    "v_cvt_pk_bf16_f32 v206, v44, v45\n" \
    "v_cvt_pk_bf16_f32 v207, v46, v47\n" \
    "v_accvgpr_write_b32 a204, v204\n" \
    "v_accvgpr_write_b32 a205, v205\n" \
    "v_accvgpr_write_b32 a206, v206\n" \
    "v_accvgpr_write_b32 a207, v207\n" \
    "v_cvt_pk_bf16_f32 v208, v48, v49\n" \
    "v_cvt_pk_bf16_f32 v209, v50, v51\n" \
    "v_cvt_pk_bf16_f32 v210, v52, v53\n" \
    "v_cvt_pk_bf16_f32 v211, v54, v55\n" \
    "v_accvgpr_write_b32 a208, v208\n" \
    "v_accvgpr_write_b32 a209, v209\n" \
    "v_accvgpr_write_b32 a210, v210\n" \
    "v_accvgpr_write_b32 a211, v211\n" \
    "v_cvt_pk_bf16_f32 v212, v56, v57\n" \
    "v_cvt_pk_bf16_f32 v213, v58, v59\n" \
    "v_cvt_pk_bf16_f32 v214, v60, v61\n" \
    "v_cvt_pk_bf16_f32 v215, v62, v63\n" \
    "v_accvgpr_write_b32 a212, v212\n" \
    "v_accvgpr_write_b32 a213, v213\n" \
    "v_accvgpr_write_b32 a214, v214\n" \
    "v_accvgpr_write_b32 a215, v215\n" \
    "v_cvt_pk_bf16_f32 v216, v64, v65\n" \
    "v_cvt_pk_bf16_f32 v217, v66, v67\n" \
    "v_cvt_pk_bf16_f32 v218, v68, v69\n" \
    "v_cvt_pk_bf16_f32 v219, v70, v71\n" \
    "v_accvgpr_write_b32 a216, v216\n" \
    "v_accvgpr_write_b32 a217, v217\n" \
    "v_accvgpr_write_b32 a218, v218\n" \
    "v_accvgpr_write_b32 a219, v219\n" \
    "v_cvt_pk_bf16_f32 v220, v72, v73\n" \
    "v_cvt_pk_bf16_f32 v221, v74, v75\n" \
    "v_cvt_pk_bf16_f32 v222, v76, v77\n" \
    "v_cvt_pk_bf16_f32 v223, v78, v79\n" \
    "v_accvgpr_write_b32 a220, v220\n" \
    "v_accvgpr_write_b32 a221, v221\n" \
    "v_accvgpr_write_b32 a222, v222\n" \
    "v_accvgpr_write_b32 a223, v223\n" \
    "s_branch Lg4_end%=\n" \
    "Lg40%=_sw:\n" \
    "s_cmp_eq_u32 s75, 3\n" \
    "s_cbranch_scc1 Lg40%=_sw2\n" \
    "s_cmp_eq_u32 s75, 12\n" \
    "s_cbranch_scc1 Lg40%=_ik\n" \
    "buffer_load_dword v101, off, s[84:87], s82 sc1\n" \
    "s_branch Lg40%=_swx\n" \
    "Lg40%=_il:\n" \
    "s_cmp_eq_u32 s88, 0\n" \
    "s_cbranch_scc1 Lg40%=_ix\n" \
    "s_sub_u32 s88, s88, 1\n" \
    "buffer_load_dword v101, off, s[84:87], s82 sc1\n" \
    "s_waitcnt vmcnt(0)\n" \
    "Lg40%=_ik:\n" \
    "v_cmp_eq_u32 vcc, v100, v101\n" \
    "s_cbranch_vccnz Lg40%=_il\n" \
    "Lg40%=_ix:\n" \
    "s_mov_b32 s83, 32896\n" \
    "buffer_atomic_add v252, v251, s[52:55], s83 offen\n" \
    "s_branch Lg40%=_swx\n" \
    "Lg40%=_sw2:\n" \
    "v_readlane_b32 s40, v255, 0\n" \
    "v_readlane_b32 s41, v255, 1\n" \
    "v_readlane_b32 s42, v255, 2\n" \
    "v_readlane_b32 s44, v255, 3\n" \
    "v_readlane_b32 s45, v255, 4\n" \
    "v_readlane_b32 s46, v255, 5\n" \
    "v_readlane_b32 s61, v255, 6\n" \
    "v_readlane_b32 s73, v255, 7\n" \
    "s_lshr_b32 s73, s73, 16\n" \
    "s_lshl_b32 s68, s61, 4\n" \
    "s_mul_i32 s69, s61, 12\n" \
    "s_lshr_b32 s70, s61, 1\n" \
    "v_mad_u32_u24 v14, v247, s61, v248\n" \
    "s_nop 0\n" \
    "s_bitcmp0_b32 s62, 7\n" \
    "s_cselect_b32 s42, s42, 0\n" \
    "s_cselect_b32 s46, s46, 0\n" \
    "s_branch Lg40%=_swx\n" \
    "Lg41_in%=:\n" \
    "s_waitcnt lgkmcnt(0)\n" \
    "v_mfma_f32_16x16x32_bf16 v[16:19], v[144:147], v[176:179], 0\n" \
    "ds_read_b64_tr_b16 v[224:225], v242 offset:4096\n" \
    "buffer_load_dwordx4 v246, s[40:43], s71 offen sc1 lds\n" \
    "v_mfma_f32_16x16x32_bf16 v[20:23], v[144:147], v[180:183], 0\n" \
    "ds_read_b64_tr_b16 v[226:227], v242 offset:6144\n" \
    "ds_read_b64_tr_b16 v[228:229], v243 offset:4096\n" \
    "v_mfma_f32_16x16x32_bf16 v[24:27], v[144:147], v[184:187], 0\n" \
    "s_add_u32 m0, m0, 4096\n" \
    "s_add_u32 s71, s71, 65536\n" \
    "v_mfma_f32_16x16x32_bf16 v[28:31], v[144:147], v[188:191], 0\n" \
    "ds_read_b64_tr_b16 v[230:231], v243 offset:6144\n" \
    "ds_read_b64_tr_b16 v[232:233], v244 offset:4096\n" \
    "v_mfma_f32_16x16x32_bf16 v[32:35], v[148:151], v[176:179], 0\n" \
    "ds_read_b64_tr_b16 v[234:235], v244 offset:6144\n" \
    "buffer_load_dwordx4 v246, s[40:43], s71 offen sc1 lds\n" \
    "v_mfma_f32_16x16x32_bf16 v[36:39], v[148:151], v[180:183], 0\n" \
    "ds_read_b64_tr_b16 v[236:237], v245 offset:4096\n" \
    "ds_read_b64_tr_b16 v[238:239], v245 offset:6144\n" \
    "v_mfma_f32_16x16x32_bf16 v[40:43], v[148:151], v[184:187], 0\n" \
    "s_add_u32 m0, m0, 4096\n" \
    "s_add_u32 s71, s71, 65536\n" \
    "v_mfma_f32_16x16x32_bf16 v[44:47], v[148:151], v[188:191], 0\n" \
    "ds_read_b128 v[192:195], v241 offset:0\n" \
    "ds_read_b128 v[196:199], v241 offset:2048\n" \
    "v_mfma_f32_16x16x32_bf16 v[48:51], v[152:155], v[176:179], 0\n" \
    "ds_read_b128 v[200:203], v241 offset:4096\n" \
    "buffer_load_dwordx4 v246, s[40:43], s71 offen sc1 lds\n" \
    "v_mfma_f32_16x16x32_bf16 v[52:55], v[152:155], v[180:183], 0\n" \
    "ds_read_b128 v[204:207], v241 offset:6144\n" \
    "v_mfma_f32_16x16x32_bf16 v[56:59], v[152:155], v[184:187], 0\n" \
    "s_add_u32 m0, m0, 4096\n" \
    "s_add_u32 s71, s71, 65536\n" \
    "v_mfma_f32_16x16x32_bf16 v[60:63], v[152:155], v[188:191], 0\n" \
    "s_cmp_eq_u32 s74, 98304\n" \
    "s_cselect_b32 s76, s77, 49152\n" \
    "v_mfma_f32_16x16x32_bf16 v[64:67], v[156:159], v[176:179], 0\n" \
    "buffer_load_dwordx4 v246, s[40:43], s71 offen sc1 lds\n" \
    "v_mfma_f32_16x16x32_bf16 v[68:71], v[156:159], v[180:183], 0\n" \
    "v_add_u32 v242, s76, v242\n" \
    "v_add_u32 v243, s76, v243\n" \
    "v_mfma_f32_16x16x32_bf16 v[72:75], v[156:159], v[184:187], 0\n" \
    "v_add_u32 v244, s76, v244\n" \
    "v_add_u32 v245, s76, v245\n" \
    "v_mfma_f32_16x16x32_bf16 v[76:79], v[156:159], v[188:191], 0\n" \
    "v_add_u32 v240, s76, v240\n" \
    "v_add_u32 v241, s76, v241\n" \
    "s_branch Lg41_p2%=\n" \
    "Lg41_p1%=:\n" \
    "s_waitcnt lgkmcnt(0)\n" \
    "v_mfma_f32_16x16x32_bf16 v[16:19], v[144:147], v[176:179], v[16:19]\n" \
    "ds_read_b64_tr_b16 v[224:225], v242 offset:4096\n" \
    "buffer_load_dwordx4 v246, s[40:43], s71 offen sc1 lds\n" \
    "v_mfma_f32_16x16x32_bf16 v[20:23], v[144:147], v[180:183], v[20:23]\n" \
    "ds_read_b64_tr_b16 v[226:227], v242 offset:6144\n" \
    "ds_read_b64_tr_b16 v[228:229], v243 offset:4096\n" \
    "v_mfma_f32_16x16x32_bf16 v[24:27], v[144:147], v[184:187], v[24:27]\n" \
    "s_add_u32 m0, m0, 4096\n" \
    "s_add_u32 s71, s71, 65536\n" \
    "v_mfma_f32_16x16x32_bf16 v[28:31], v[144:147], v[188:191], v[28:31]\n" \
    "ds_read_b64_tr_b16 v[230:231], v243 offset:6144\n" \
    "ds_read_b64_tr_b16 v[232:233], v244 offset:4096\n" \
    "v_mfma_f32_16x16x32_bf16 v[32:35], v[148:151], v[176:179], v[32:35]\n" \
    "ds_read_b64_tr_b16 v[234:235], v244 offset:6144\n" \
    "buffer_load_dwordx4 v246, s[40:43], s71 offen sc1 lds\n" \
    "v_mfma_f32_16x16x32_bf16 v[36:39], v[148:151], v[180:183], v[36:39]\n" \
    "ds_read_b64_tr_b16 v[236:237], v245 offset:4096\n" \
    "ds_read_b64_tr_b16 v[238:239], v245 offset:6144\n" \
    "v_mfma_f32_16x16x32_bf16 v[40:43], v[148:151], v[184:187], v[40:43]\n" \
    "s_add_u32 m0, m0, 4096\n" \
    "s_add_u32 s71, s71, 65536\n" \
    "v_mfma_f32_16x16x32_bf16 v[44:47], v[148:151], v[188:191], v[44:47]\n" \
    "ds_read_b128 v[192:195], v241 offset:0\n" \
    "ds_read_b128 v[196:199], v241 offset:2048\n" \
    "v_mfma_f32_16x16x32_bf16 v[48:51], v[152:155], v[176:179], v[48:51]\n" \
    "ds_read_b128 v[200:203], v241 offset:4096\n" \
    "buffer_load_dwordx4 v246, s[40:43], s71 offen sc1 lds\n" \
    "v_mfma_f32_16x16x32_bf16 v[52:55], v[152:155], v[180:183], v[52:55]\n" \
    "ds_read_b128 v[204:207], v241 offset:6144\n" \
    "v_mfma_f32_16x16x32_bf16 v[56:59], v[152:155], v[184:187], v[56:59]\n" \
    "s_add_u32 m0, m0, 4096\n" \
    "s_add_u32 s71, s71, 65536\n" \
    "v_mfma_f32_16x16x32_bf16 v[60:63], v[152:155], v[188:191], v[60:63]\n" \
    "s_cmp_eq_u32 s74, 98304\n" \
    "s_cselect_b32 s76, s77, 49152\n" \
    "v_mfma_f32_16x16x32_bf16 v[64:67], v[156:159], v[176:179], v[64:67]\n" \
    "buffer_load_dwordx4 v246, s[40:43], s71 offen sc1 lds\n" \
    "v_mfma_f32_16x16x32_bf16 v[68:71], v[156:159], v[180:183], v[68:71]\n" \
    "v_add_u32 v242, s76, v242\n" \
    "v_add_u32 v243, s76, v243\n" \
    "v_mfma_f32_16x16x32_bf16 v[72:75], v[156:159], v[184:187], v[72:75]\n" \
    "v_add_u32 v244, s76, v244\n" \
    "v_add_u32 v245, s76, v245\n" \
    "v_mfma_f32_16x16x32_bf16 v[76:79], v[156:159], v[188:191], v[76:79]\n" \
    "v_add_u32 v240, s76, v240\n" \
    "v_add_u32 v241, s76, v241\n" \
    "Lg41_p2%=:\n" \
    "s_waitcnt vmcnt(8) lgkmcnt(0)\n" \
    "v_mfma_f32_16x16x32_bf16 v[16:19], v[192:195], v[224:227], v[16:19]\n" \
    "s_barrier\n" \
    "s_bitcmp1_b32 s81, s75\n" \
    "s_cbranch_scc1 Lg41%=_sw\n" \
    "Lg41%=_swx:\n" \
    "v_mfma_f32_16x16x32_bf16 v[20:23], v[192:195], v[228:231], v[20:23]\n" \
    "ds_read_b64_tr_b16 v[176:177], v242 offset:0\n" \
    "s_add_u32 m0, s74, s79\n" \
    "s_mul_i32 s72, s73, s70\n" \
    "v_mfma_f32_16x16x32_bf16 v[24:27], v[192:195], v[232:235], v[24:27]\n" \
    "ds_read_b64_tr_b16 v[178:179], v242 offset:2048\n" \
    "buffer_load_dwordx4 v14, s[44:47], s72 offen lds\n" \
    "v_mfma_f32_16x16x32_bf16 v[28:31], v[192:195], v[236:239], v[28:31]\n" \
    "ds_read_b64_tr_b16 v[180:181], v243 offset:0\n" \
    "ds_read_b64_tr_b16 v[182:183], v243 offset:2048\n" \
    "v_mfma_f32_16x16x32_bf16 v[32:35], v[196:199], v[224:227], v[32:35]\n" \
    "ds_read_b64_tr_b16 v[184:185], v244 offset:0\n" \
    "s_add_u32 m0, m0, 1024\n" \
    "s_add_u32 s72, s72, s68\n" \
    "v_mfma_f32_16x16x32_bf16 v[36:39], v[196:199], v[228:231], v[36:39]\n" \
    "ds_read_b64_tr_b16 v[186:187], v244 offset:2048\n" \
    "buffer_load_dwordx4 v14, s[44:47], s72 offen lds\n" \
    "v_mfma_f32_16x16x32_bf16 v[40:43], v[196:199], v[232:235], v[40:43]\n" \
    "ds_read_b64_tr_b16 v[188:189], v245 offset:0\n" \
    "ds_read_b64_tr_b16 v[190:191], v245 offset:2048\n" \
    "v_mfma_f32_16x16x32_bf16 v[44:47], v[196:199], v[236:239], v[44:47]\n" \
    "ds_read_b128 v[144:147], v240 offset:0\n" \
    "s_add_u32 m0, m0, 1024\n" \
    "s_sub_u32 s72, s72, s69\n" \
    "v_mfma_f32_16x16x32_bf16 v[48:51], v[200:203], v[224:227], v[48:51]\n" \
    "ds_read_b128 v[148:151], v240 offset:2048\n" \
    "buffer_load_dwordx4 v14, s[44:47], s72 offen lds\n" \
    "v_mfma_f32_16x16x32_bf16 v[52:55], v[200:203], v[228:231], v[52:55]\n" \
    "ds_read_b128 v[152:155], v240 offset:4096\n" \
    "ds_read_b128 v[156:159], v240 offset:6144\n" \
    "v_mfma_f32_16x16x32_bf16 v[56:59], v[200:203], v[232:235], v[56:59]\n" \
    "s_add_u32 m0, m0, 1024\n" \
    "s_add_u32 s72, s72, s68\n" \
    "v_mfma_f32_16x16x32_bf16 v[60:63], v[200:203], v[236:239], v[60:63]\n" \
    "buffer_load_dwordx4 v14, s[44:47], s72 offen lds\n" \
    "v_mfma_f32_16x16x32_bf16 v[64:67], v[204:207], v[224:227], v[64:67]\n" \
    "s_add_u32 m0, s74, s78\n" \
    "s_mov_b32 s71, s73\n" \
    "v_mfma_f32_16x16x32_bf16 v[68:71], v[204:207], v[228:231], v[68:71]\n" \
    "s_add_u32 s73, s73, 128\n" \
    "s_and_b32 s73, s73, 2047\n" \
    "v_mfma_f32_16x16x32_bf16 v[72:75], v[204:207], v[232:235], v[72:75]\n" \
    "s_add_u32 s74, s74, s76\n" \
    "s_sub_u32 s75, s75, 1\n" \
    "v_mfma_f32_16x16x32_bf16 v[76:79], v[204:207], v[236:239], v[76:79]\n" \
    "s_cmp_lg_u32 s75, 0\n" \
    "s_cbranch_scc1 Lg41_p1%=\n" \
    "v_cvt_pk_bf16_f32 v192, v16, v17\n" \
    "v_cvt_pk_bf16_f32 v193, v18, v19\n" \
    "v_cvt_pk_bf16_f32 v194, v32, v33\n" \
    "v_cvt_pk_bf16_f32 v195, v34, v35\n" \
    "v_accvgpr_write_b32 a192, v192\n" \
    "v_accvgpr_write_b32 a193, v193\n" \
    "v_accvgpr_write_b32 a194, v194\n" \
    "v_accvgpr_write_b32 a195, v195\n" \
    "v_cvt_pk_bf16_f32 v196, v20, v21\n" \
    "v_cvt_pk_bf16_f32 v197, v22, v23\n" \
    "v_cvt_pk_bf16_f32 v198, v36, v37\n" \
    "v_cvt_pk_bf16_f32 v199, v38, v39\n" \
    "v_accvgpr_write_b32 a196, v196\n" \
    "v_accvgpr_write_b32 a197, v197\n" \
    "v_accvgpr_write_b32 a198, v198\n" \
    "v_accvgpr_write_b32 a199, v199\n" \
    "v_cvt_pk_bf16_f32 v200, v24, v25\n" \
    "v_cvt_pk_bf16_f32 v201, v26, v27\n" \
    "v_cvt_pk_bf16_f32 v202, v40, v41\n" \
    "v_cvt_pk_bf16_f32 v203, v42, v43\n" \
    "v_accvgpr_write_b32 a200, v200\n" \
    "v_accvgpr_write_b32 a201, v201\n" \
    "v_accvgpr_write_b32 a202, v202\n" \
    "v_accvgpr_write_b32 a203, v203\n" \
    "v_cvt_pk_bf16_f32 v204, v28, v29\n" \
    "v_cvt_pk_bf16_f32 v205, v30, v31\n" \
    "v_cvt_pk_bf16_f32 v206, v44, v45\n" \
    "v_cvt_pk_bf16_f32 v207, v46, v47\n" \
    "v_accvgpr_write_b32 a204, v204\n" \
    "v_accvgpr_write_b32 a205, v205\n" \
    "v_accvgpr_write_b32 a206, v206\n" \
    "v_accvgpr_write_b32 a207, v207\n" \
    "v_cvt_pk_bf16_f32 v208, v48, v49\n" \
    "v_cvt_pk_bf16_f32 v209, v50, v51\n" \
    "v_cvt_pk_bf16_f32 v210, v64, v65\n" \
    "v_cvt_pk_bf16_f32 v211, v66, v67\n" \
    "v_accvgpr_write_b32 a208, v208\n" \
    "v_accvgpr_write_b32 a209, v209\n" \
    "v_accvgpr_write_b32 a210, v210\n" \
    "v_accvgpr_write_b32 a211, v211\n" \
    "v_cvt_pk_bf16_f32 v212, v52, v53\n" \
    "v_cvt_pk_bf16_f32 v213, v54, v55\n" \
    "v_cvt_pk_bf16_f32 v214, v68, v69\n" \
    "v_cvt_pk_bf16_f32 v215, v70, v71\n" \
    "v_accvgpr_write_b32 a212, v212\n" \
    "v_accvgpr_write_b32 a213, v213\n" \
    "v_accvgpr_write_b32 a214, v214\n" \
    "v_accvgpr_write_b32 a215, v215\n" \
    "v_cvt_pk_bf16_f32 v216, v56, v57\n" \
    "v_cvt_pk_bf16_f32 v217, v58, v59\n" \
    "v_cvt_pk_bf16_f32 v218, v72, v73\n" \
    "v_cvt_pk_bf16_f32 v219, v74, v75\n" \
    "v_accvgpr_write_b32 a216, v216\n" \
    "v_accvgpr_write_b32 a217, v217\n" \
    "v_accvgpr_write_b32 a218, v218\n" \
    "v_accvgpr_write_b32 a219, v219\n" \
    "v_cvt_pk_bf16_f32 v220, v60, v61\n" \
    "v_cvt_pk_bf16_f32 v221, v62, v63\n" \
    "v_cvt_pk_bf16_f32 v222, v76, v77\n" \
    "v_cvt_pk_bf16_f32 v223, v78, v79\n" \
    "v_accvgpr_write_b32 a220, v220\n" \
    "v_accvgpr_write_b32 a221, v221\n" \
    "v_accvgpr_write_b32 a222, v222\n" \
    "v_accvgpr_write_b32 a223, v223\n" \
    "s_branch Lg4_end%=\n" \
    "Lg41%=_sw:\n" \
    "s_cmp_eq_u32 s75, 3\n" \
    "s_cbranch_scc1 Lg41%=_sw2\n" \
    "s_cmp_eq_u32 s75, 12\n" \
    "s_cbranch_scc1 Lg41%=_ik\n" \
    "buffer_load_dword v101, off, s[84:87], s82 sc1\n" \
    "s_branch Lg41%=_swx\n" \
    "Lg41%=_il:\n" \
    "s_cmp_eq_u32 s88, 0\n" \
    "s_cbranch_scc1 Lg41%=_ix\n" \
    "s_sub_u32 s88, s88, 1\n" \
    "buffer_load_dword v101, off, s[84:87], s82 sc1\n" \
    "s_waitcnt vmcnt(0)\n" \
    "Lg41%=_ik:\n" \
    "v_cmp_eq_u32 vcc, v100, v101\n" \
    "s_cbranch_vccnz Lg41%=_il\n" \
    "Lg41%=_ix:\n" \
    "s_mov_b32 s83, 32896\n" \
    "buffer_atomic_add v252, v251, s[52:55], s83 offen\n" \
    "s_branch Lg41%=_swx\n" \
    "Lg41%=_sw2:\n" \
    "v_readlane_b32 s40, v255, 0\n" \
    "v_readlane_b32 s41, v255, 1\n" \
    "v_readlane_b32 s42, v255, 2\n" \
    "v_readlane_b32 s44, v255, 3\n" \
    "v_readlane_b32 s45, v255, 4\n" \
    "v_readlane_b32 s46, v255, 5\n" \
    "v_readlane_b32 s61, v255, 6\n" \
    "v_readlane_b32 s73, v255, 7\n" \
    "s_lshr_b32 s73, s73, 16\n" \
    "s_lshl_b32 s68, s61, 4\n" \
    "s_mul_i32 s69, s61, 12\n" \
    "s_lshr_b32 s70, s61, 1\n" \
    "v_mad_u32_u24 v14, v247, s61, v248\n" \
    "s_nop 0\n" \
    "s_bitcmp0_b32 s62, 7\n" \
    "s_cselect_b32 s42, s42, 0\n" \
    "s_cselect_b32 s46, s46, 0\n" \
    "s_branch Lg41%=_swx\n" \
    "Lg4_end%=:\n" \
    "v_readlane_b32 s48, v254, 8\n" \
    "v_readlane_b32 s49, v254, 9\n" \
    "v_readlane_b32 s50, v254, 10\n" \
    "v_readlane_b32 s66, v254, 11\n" \
    "v_readlane_b32 s63, v254, 12\n" \
    "v_readlane_b32 s64, v254, 13\n" \
    "v_readlane_b32 s65, v254, 14\n" \
    "v_readlane_b32 s67, v254, 15\n" \
    "s_bitcmp1_b32 s62, 8\n" \
    "s_cselect_b64 vcc, -1, 0\n" \
    "s_mov_b32 s58, s54\n" \
    "v_cndmask_b32 v15, v249, v250, vcc\n" \
    "v_mov_b32 v254, v255\n" \
    "s_nop 1\n" \
    "buffer_store_dwordx4 a[192:195], v15, s[48:51], s66 offen sc1\n" \
    "s_add_u32 s80, s66, s65\n" \
    "buffer_store_dwordx4 a[196:199], v15, s[48:51], s80 offen sc1\n" \
    "s_add_u32 s80, s66, s64\n" \
    "buffer_store_dwordx4 a[200:203], v15, s[48:51], s80 offen sc1\n" \
    "s_add_u32 s80, s80, s65\n" \
    "buffer_store_dwordx4 a[204:207], v15, s[48:51], s80 offen sc1\n" \
    "s_add_u32 s66, s66, s63\n" \
    "buffer_store_dwordx4 a[208:211], v15, s[48:51], s66 offen sc1\n" \
    "s_add_u32 s80, s66, s65\n" \
    "buffer_store_dwordx4 a[212:215], v15, s[48:51], s80 offen sc1\n" \
    "s_add_u32 s80, s66, s64\n" \
    "buffer_store_dwordx4 a[216:219], v15, s[48:51], s80 offen sc1\n" \
    "s_add_u32 s80, s80, s65\n" \
    "buffer_store_dwordx4 a[220:223], v15, s[48:51], s80 offen sc1\n" \
    "s_add_u32 s66, s66, s63\n" \
    "Lg4_fx%=:\n" \
    "s_waitcnt vmcnt(0) lgkmcnt(0)\n"
#define GEMM_CLOB "v14", "v15", "v16", "v17", "v18", "v19", "v20", "v21", "v22", "v23", "v24", "v25", "v26", "v27", "v28", "v29", "v30", "v31", "v32", "v33", "v34", "v35", "v36", "v37", "v38", "v39", "v40", "v41", "v42", "v43", "v44", "v45", "v46", "v47", "v48", "v49", "v50", "v51", "v52", "v53", "v54", "v55", "v56", "v57", "v58", "v59", "v60", "v61", "v62", "v63", "v64", "v65", "v66", "v67", "v68", "v69", "v70", "v71", "v72", "v73", "v74", "v75", "v76", "v77", "v78", "v79", "v80", "v81", "v82", "v83", "v84", "v85", "v86", "v87", "v88", "v89", "v90", "v91", "v92", "v93", "v94", "v95", "v96", "v97", "v98", "v99", "v100", "v101", "v102", "v103", "v104", "v105", "v106", "v107", "v108", "v109", "v110", "v111", "v112", "v113", "v114", "v115", "v116", "v117", "v118", "v119", "v120", "v121", "v122", "v123", "v124", "v125", "v126", "v127", "v128", "v129", "v130", "v131", "v132", "v133", "v134", "v135", "v136", "v137", "v138", "v139", "v140", "v141", "v142", "v143", "v144", "v145", "v146", "v147", "v148", "v149", "v150", "v151", "v152", "v153", "v154", "v155", "v156", "v157", "v158", "v159", "v160", "v161", "v162", "v163", "v164", "v165", "v166", "v167", "v168", "v169", "v170", "v171", "v172", "v173", "v174", "v175", "v176", "v177", "v178", "v179", "v180", "v181", "v182", "v183", "v184", "v185", "v186", "v187", "v188", "v189", "v190", "v191", "v192", "v193", "v194", "v195", "v196", "v197", "v198", "v199", "v200", "v201", "v202", "v203", "v204", "v205", "v206", "v207", "v208", "v209", "v210", "v211", "v212", "v213", "v214", "v215", "v216", "v217", "v218", "v219", "v220", "v221", "v222", "v223", "v224", "v225", "v226", "v227", "v228", "v229", "v230", "v231", "v232", "v233", "v234", "v235", "v236", "v237", "v238", "v239", "a192", "a193", "a194", "a195", "a196", "a197", "a198", "a199", "a200", "a201", "a202", "a203", "a204", "a205", "a206", "a207", "a208", "a209", "a210", "a211", "a212", "a213", "a214", "a215", "a216", "a217", "a218", "a219", "a220", "a221", "a222", "a223", "a224", "a225", "a226", "a227", "a228", "a229", "a230", "a231", "a232", "a233", "a234", "a235", "a236", "a237", "a238", "a239", "a240", "a241", "a242", "a243", "a244", "a245", "a246", "a247", "a248", "a249", "a250", "a251", "a252", "a253", "a254", "a255", "s40", "s41", "s42", "s43", "s44", "s45", "s46", "s47", "s48", "s49", "s50", "s51", "s56", "s57", "s58", "s59", "s61", "s62", "s63", "s64", "s65", "s66", "s68", "s69", "s70", "s71", "s72", "s73", "s74", "s75", "s76", "s77", "s78", "s79", "s80", "s81", "s82", "s83", "s84", "s85", "s86", "s87", "vcc", "scc", "memory"
#define ATTN_ASM \
    "s_nop 4\n" \
    "s_lshl_b32 s57, s49, 12\n" \
    "s_mov_b32 s58, 0\n" \
    "s_mov_b32 m0, s57\n" \
    "buffer_load_dwordx4 v[16:19], v240, s[40:43], s47 offen sc1\n" \
    "s_add_u32 s47, s47, 65536\n" \
    "buffer_load_dwordx4 v[20:23], v240, s[40:43], s47 offen sc1\n" \
    "s_add_u32 s47, s47, 65536\n" \
    "buffer_load_dwordx4 a[0:3], v240, s[40:43], s44 offen offset:0 sc1\n" \
    "buffer_load_dwordx4 a[4:7], v240, s[40:43], s44 offen offset:1024 sc1\n" \
    "s_add_u32 s44, s44, 131072\n" \
    "buffer_load_dwordx4 a[8:11], v240, s[40:43], s44 offen offset:0 sc1\n" \
    "buffer_load_dwordx4 a[12:15], v240, s[40:43], s44 offen offset:1024 sc1\n" \
    "s_add_u32 s44, s44, 131072\n" \
    "buffer_load_dwordx4 a[16:19], v240, s[40:43], s44 offen offset:0 sc1\n" \
    "buffer_load_dwordx4 a[20:23], v240, s[40:43], s44 offen offset:1024 sc1\n" \
    "s_add_u32 s44, s44, 131072\n" \
    "buffer_load_dwordx4 a[24:27], v240, s[40:43], s44 offen offset:0 sc1\n" \
    "buffer_load_dwordx4 a[28:31], v240, s[40:43], s44 offen offset:1024 sc1\n" \
    "s_add_u32 s44, s44, 131072\n" \
    "buffer_load_dwordx4 v240, s[40:43], s46 offen offset:0 sc1 lds\n" \
    "buffer_load_dwordx4 v240, s[40:43], s46 offen offset:1024 sc1 lds\n" \
    "buffer_load_dwordx4 v240, s[40:43], s46 offen offset:2048 sc1 lds\n" \
    "buffer_load_dwordx4 v240, s[40:43], s46 offen offset:3072 sc1 lds\n" \
    "s_add_u32 s46, s46, 4096\n" \
    "s_add_u32 s58, s58, 0x4000\n" \
    "s_and_b32 s58, s58, 0xffff\n" \
    "s_add_u32 m0, s58, s57\n" \
    "s_nop 0\n" \
    "buffer_load_dwordx4 v240, s[40:43], s46 offen offset:0 sc1 lds\n" \
    "buffer_load_dwordx4 v240, s[40:43], s46 offen offset:1024 sc1 lds\n" \
    "buffer_load_dwordx4 v240, s[40:43], s46 offen offset:2048 sc1 lds\n" \
    "buffer_load_dwordx4 v240, s[40:43], s46 offen offset:3072 sc1 lds\n" \
    "s_add_u32 s46, s46, 4096\n" \
    "s_add_u32 s58, s58, 0x4000\n" \
    "s_and_b32 s58, s58, 0xffff\n" \
    "s_add_u32 m0, s58, s57\n" \
    "s_nop 0\n" \
    "buffer_load_dwordx4 v240, s[40:43], s46 offen offset:0 sc1 lds\n" \
    "buffer_load_dwordx4 v240, s[40:43], s46 offen offset:1024 sc1 lds\n" \
    "buffer_load_dwordx4 v240, s[40:43], s46 offen offset:2048 sc1 lds\n" \
    "buffer_load_dwordx4 v240, s[40:43], s46 offen offset:3072 sc1 lds\n" \
    "s_add_u32 s46, s46, 4096\n" \
    "s_add_u32 s58, s58, 0x4000\n" \
    "s_and_b32 s58, s58, 0xffff\n" \
    "s_add_u32 m0, s58, s57\n" \
    "s_nop 0\n" \
    "v_mov_b32 v32, v244\n" \
    "v_mov_b32 v33, v244\n" \
    "v_mov_b32 v34, v245\n" \
    "v_mov_b32 v35, v245\n" \
    "v_mov_b64 v[164:165], 0\n" \
    "v_mov_b64 v[166:167], 0\n" \
    "v_mov_b64 v[168:169], 0\n" \
    "v_mov_b64 v[170:171], 0\n" \
    "v_mov_b64 v[172:173], 0\n" \
    "v_mov_b64 v[174:175], 0\n" \
    "v_mov_b64 v[176:177], 0\n" \
    "v_mov_b64 v[178:179], 0\n" \
    "v_mov_b64 v[84:85], 0\n" \
    "v_mov_b64 v[86:87], 0\n" \
    "v_mov_b64 v[180:181], 0\n" \
    "v_mov_b64 v[182:183], 0\n" \
    "v_mov_b64 v[184:185], 0\n" \
    "v_mov_b64 v[186:187], 0\n" \
    "v_mov_b64 v[188:189], 0\n" \
    "v_mov_b64 v[190:191], 0\n" \
    "v_mov_b64 v[192:193], 0\n" \
    "v_mov_b64 v[194:195], 0\n" \
    "v_mov_b64 v[88:89], 0\n" \
    "v_mov_b64 v[90:91], 0\n" \
    "v_mov_b64 v[196:197], 0\n" \
    "v_mov_b64 v[198:199], 0\n" \
    "v_mov_b64 v[200:201], 0\n" \
    "v_mov_b64 v[202:203], 0\n" \
    "v_mov_b64 v[204:205], 0\n" \
    "v_mov_b64 v[206:207], 0\n" \
    "v_mov_b64 v[208:209], 0\n" \
    "v_mov_b64 v[210:211], 0\n" \
    "v_mov_b64 v[92:93], 0\n" \
    "v_mov_b64 v[94:95], 0\n" \
    "v_mov_b64 v[212:213], 0\n" \
    "v_mov_b64 v[214:215], 0\n" \
    "v_mov_b64 v[216:217], 0\n" \
    "v_mov_b64 v[218:219], 0\n" \
    "v_mov_b64 v[220:221], 0\n" \
    "v_mov_b64 v[222:223], 0\n" \
    "v_mov_b64 v[224:225], 0\n" \
    "v_mov_b64 v[226:227], 0\n" \
    "v_mov_b64 v[96:97], 0\n" \
    "v_mov_b64 v[98:99], 0\n" \
    "v_mov_b32 v247, v240\n" \
    "s_waitcnt vmcnt(19)\n" \
    "v_mfma_f32_32x32x16_bf16 v[36:51], v[16:19], a[0:3], 0\n" \
    "s_waitcnt vmcnt(18)\n" \
    "v_mfma_f32_32x32x16_bf16 v[36:51], v[20:23], a[4:7], v[36:51]\n" \
    "s_nop 0\n" \
    "s_nop 0\n" \
    "s_nop 0\n" \
    "s_nop 0\n" \
    "s_nop 0\n" \
    "s_nop 0\n" \
    "s_nop 0\n" \
    "s_nop 0\n" \
    "s_nop 0\n" \
    "s_nop 0\n" \
    "s_nop 0\n" \
    "s_nop 0\n" \
    "v_max3_f32 v248, v248, v36, v37\n" \
    "v_max3_f32 v248, v248, v38, v39\n" \
    "v_max3_f32 v248, v248, v40, v41\n" \
    "v_max3_f32 v248, v248, v42, v43\n" \
    "v_max3_f32 v248, v248, v44, v45\n" \
    "v_max3_f32 v248, v248, v46, v47\n" \
    "v_max3_f32 v248, v248, v48, v49\n" \
    "v_max3_f32 v248, v248, v50, v51\n" \
    "v_mov_b32 v228, v248\n" \
    "s_nop 1\n" \
    "v_permlane32_swap_b32 v248, v228\n" \
    "v_max_f32 v248, v248, v228\n" \
    "v_sub_f32 v100, s45, v248\n" \
    "v_mov_b32 v101, v100\n" \
    "v_mov_b64 v[102:103], v[100:101]\n" \
    "v_mov_b64 v[104:105], v[100:101]\n" \
    "v_mov_b64 v[106:107], v[100:101]\n" \
    "v_mov_b64 v[108:109], v[100:101]\n" \
    "v_mov_b64 v[110:111], v[100:101]\n" \
    "v_mov_b64 v[112:113], v[100:101]\n" \
    "v_mov_b64 v[114:115], v[100:101]\n" \
    "s_waitcnt vmcnt(17)\n" \
    "v_mfma_f32_32x32x16_bf16 v[52:67], v[16:19], a[8:11], 0\n" \
    "s_waitcnt vmcnt(16)\n" \
    "v_mfma_f32_32x32x16_bf16 v[52:67], v[20:23], a[12:15], v[52:67]\n" \
    "s_nop 0\n" \
    "s_nop 0\n" \
    "s_nop 0\n" \
    "s_nop 0\n" \
    "s_nop 0\n" \
    "s_nop 0\n" \
    "s_nop 0\n" \
    "s_nop 0\n" \
    "s_nop 0\n" \
    "s_nop 0\n" \
    "s_nop 0\n" \
    "s_nop 0\n" \
    "v_max3_f32 v249, v249, v52, v53\n" \
    "v_max3_f32 v249, v249, v54, v55\n" \
    "v_max3_f32 v249, v249, v56, v57\n" \
    "v_max3_f32 v249, v249, v58, v59\n" \
    "v_max3_f32 v249, v249, v60, v61\n" \
    "v_max3_f32 v249, v249, v62, v63\n" \
    "v_max3_f32 v249, v249, v64, v65\n" \
    "v_max3_f32 v249, v249, v66, v67\n" \
    "v_mov_b32 v228, v249\n" \
    "s_nop 1\n" \
    "v_permlane32_swap_b32 v249, v228\n" \
    "v_max_f32 v249, v249, v228\n" \
    "v_sub_f32 v116, s45, v249\n" \
    "v_mov_b32 v117, v116\n" \
    "v_mov_b64 v[118:119], v[116:117]\n" \
    "v_mov_b64 v[120:121], v[116:117]\n" \
    "v_mov_b64 v[122:123], v[116:117]\n" \
    "v_mov_b64 v[124:125], v[116:117]\n" \
    "v_mov_b64 v[126:127], v[116:117]\n" \
    "v_mov_b64 v[128:129], v[116:117]\n" \
    "v_mov_b64 v[130:131], v[116:117]\n" \
    "s_waitcnt vmcnt(15)\n" \
    "v_mfma_f32_32x32x16_bf16 v[36:51], v[16:19], a[16:19], 0\n" \
    "s_waitcnt vmcnt(14)\n" \
    "v_mfma_f32_32x32x16_bf16 v[36:51], v[20:23], a[20:23], v[36:51]\n" \
    "s_nop 0\n" \
    "s_nop 0\n" \
    "s_nop 0\n" \
    "s_nop 0\n" \
    "s_nop 0\n" \
    "s_nop 0\n" \
    "s_nop 0\n" \
    "s_nop 0\n" \
    "s_nop 0\n" \
    "s_nop 0\n" \
    "s_nop 0\n" \
    "s_nop 0\n" \
    "v_max3_f32 v250, v250, v36, v37\n" \
    "v_max3_f32 v250, v250, v38, v39\n" \
    "v_max3_f32 v250, v250, v40, v41\n" \
    "v_max3_f32 v250, v250, v42, v43\n" \
    "v_max3_f32 v250, v250, v44, v45\n" \
    "v_max3_f32 v250, v250, v46, v47\n" \
    "v_max3_f32 v250, v250, v48, v49\n" \
    "v_max3_f32 v250, v250, v50, v51\n" \
    "v_mov_b32 v228, v250\n" \
    "s_nop 1\n" \
    "v_permlane32_swap_b32 v250, v228\n" \
    "v_max_f32 v250, v250, v228\n" \
    "v_sub_f32 v132, s45, v250\n" \
    "v_mov_b32 v133, v132\n" \
    "v_mov_b64 v[134:135], v[132:133]\n" \
    "v_mov_b64 v[136:137], v[132:133]\n" \
    "v_mov_b64 v[138:139], v[132:133]\n" \
    "v_mov_b64 v[140:141], v[132:133]\n" \
    "v_mov_b64 v[142:143], v[132:133]\n" \
    "v_mov_b64 v[144:145], v[132:133]\n" \
    "v_mov_b64 v[146:147], v[132:133]\n" \
    "s_waitcnt vmcnt(13)\n" \
    "v_mfma_f32_32x32x16_bf16 v[52:67], v[16:19], a[24:27], 0\n" \
    "s_waitcnt vmcnt(12)\n" \
    "v_mfma_f32_32x32x16_bf16 v[52:67], v[20:23], a[28:31], v[52:67]\n" \
    "s_nop 0\n" \
    "s_nop 0\n" \
    "s_nop 0\n" \
    "s_nop 0\n" \
    "s_nop 0\n" \
    "s_nop 0\n" \
    "s_nop 0\n" \
    "s_nop 0\n" \
    "s_nop 0\n" \
    "s_nop 0\n" \
    "s_nop 0\n" \
    "s_nop 0\n" \
    "v_max3_f32 v251, v251, v52, v53\n" \
    "v_max3_f32 v251, v251, v54, v55\n" \
    "v_max3_f32 v251, v251, v56, v57\n" \
    "v_max3_f32 v251, v251, v58, v59\n" \
    "v_max3_f32 v251, v251, v60, v61\n" \
    "v_max3_f32 v251, v251, v62, v63\n" \
    "v_max3_f32 v251, v251, v64, v65\n" \
    "v_max3_f32 v251, v251, v66, v67\n" \
    "v_mov_b32 v228, v251\n" \
    "s_nop 1\n" \
    "v_permlane32_swap_b32 v251, v228\n" \
    "v_max_f32 v251, v251, v228\n" \
    "v_sub_f32 v148, s45, v251\n" \
    "v_mov_b32 v149, v148\n" \
    "v_mov_b64 v[150:151], v[148:149]\n" \
    "v_mov_b64 v[152:153], v[148:149]\n" \
    "v_mov_b64 v[154:155], v[148:149]\n" \
    "v_mov_b64 v[156:157], v[148:149]\n" \
    "v_mov_b64 v[158:159], v[148:149]\n" \
    "v_mov_b64 v[160:161], v[148:149]\n" \
    "v_mov_b64 v[162:163], v[148:149]\n" \
    "v_mfma_f32_32x32x16_bf16 v[36:51], v[16:19], a[0:3], v[100:115]\n" \
    "v_mfma_f32_32x32x16_bf16 v[36:51], v[20:23], a[4:7], v[36:51]\n" \
    "v_mfma_f32_32x32x16_bf16 v[52:67], v[16:19], a[8:11], v[116:131]\n" \
    "v_mfma_f32_32x32x16_bf16 v[52:67], v[20:23], a[12:15], v[52:67]\n" \
    "s_nop 4\n" \
    "v_exp_f32 v36, v36\n" \
    "v_exp_f32 v37, v37\n" \
    "v_exp_f32 v38, v38\n" \
    "v_exp_f32 v39, v39\n" \
    "v_exp_f32 v40, v40\n" \
    "v_exp_f32 v41, v41\n" \
    "v_exp_f32 v42, v42\n" \
    "v_exp_f32 v43, v43\n" \
    "v_exp_f32 v44, v44\n" \
    "v_exp_f32 v45, v45\n" \
    "v_exp_f32 v46, v46\n" \
    "v_exp_f32 v47, v47\n" \
    "v_exp_f32 v48, v48\n" \
    "v_exp_f32 v49, v49\n" \
    "v_exp_f32 v50, v50\n" \
    "v_exp_f32 v51, v51\n" \
    "v_cvt_pk_bf16_f32 v68, v36, v37\n" \
    "v_cvt_pk_bf16_f32 v69, v38, v39\n" \
    "v_cvt_pk_bf16_f32 v70, v40, v41\n" \
    "v_cvt_pk_bf16_f32 v71, v42, v43\n" \
    "v_cvt_pk_bf16_f32 v72, v44, v45\n" \
    "v_cvt_pk_bf16_f32 v73, v46, v47\n" \
    "v_cvt_pk_bf16_f32 v74, v48, v49\n" \
    "v_cvt_pk_bf16_f32 v75, v50, v51\n" \
    "s_mov_b32 s56, 16\n" \
    "s_mov_b32 s59, s51\n" \
    "s_waitcnt vmcnt(8)\n" \
    "s_barrier\n" \
    "ds_read_b128 v[24:27], v247 offset:8192\n" \
    "ds_read_b128 v[28:31], v247 offset:12288\n" \
    "s_waitcnt lgkmcnt(0)\n" \
    "La_loop%=:\n" \
    "v_mfma_f32_32x32x16_bf16 v[36:51], v[16:19], a[16:19], v[132:147]\n" \
    "s_waitcnt lgkmcnt(0)\n" \
    "v_exp_f32 v52, v52\n" \
    "v_exp_f32 v53, v53\n" \
    "v_exp_f32 v54, v54\n" \
    "v_mfma_f32_32x32x16_bf16 v[164:179], v[24:27], v[68:71], v[164:179]\n" \
    "v_exp_f32 v55, v55\n" \
    "v_cvt_pk_bf16_f32 v76, v52, v53\n" \
    "v_exp_f32 v56, v56\n" \
    "v_exp_f32 v57, v57\n" \
    "v_mfma_f32_32x32x16_bf16 v[36:51], v[20:23], a[20:23], v[36:51]\n" \
    "buffer_load_dwordx4 v240, s[40:43], s46 offen offset:0 sc1 lds\n" \
    "v_cvt_pk_bf16_f32 v77, v54, v55\n" \
    "v_exp_f32 v58, v58\n" \
    "v_exp_f32 v59, v59\n" \
    "v_cvt_pk_bf16_f32 v78, v56, v57\n" \
    "v_exp_f32 v60, v60\n" \
    "v_mfma_f32_32x32x16_bf16 v[164:179], v[28:31], v[72:75], v[164:179]\n" \
    "v_exp_f32 v61, v61\n" \
    "v_cvt_pk_bf16_f32 v79, v58, v59\n" \
    "v_exp_f32 v62, v62\n" \
    "v_exp_f32 v63, v63\n" \
    "v_cvt_pk_bf16_f32 v80, v60, v61\n" \
    "v_exp_f32 v64, v64\n" \
    "v_smfmac_f32_16x16x64_bf16 v[84:87], v[32:35], v[68:75], v246\n" \
    "v_cvt_pk_bf16_f32 v81, v62, v63\n" \
    "v_exp_f32 v65, v65\n" \
    "v_exp_f32 v66, v66\n" \
    "v_exp_f32 v67, v67\n" \
    "v_cvt_pk_bf16_f32 v82, v64, v65\n" \
    "v_cvt_pk_bf16_f32 v83, v66, v67\n" \
    "v_mfma_f32_32x32x16_bf16 v[52:67], v[16:19], a[24:27], v[148:163]\n" \
    "ds_read_b128 v[16:19], v247 offset:0\n" \
    "v_exp_f32 v36, v36\n" \
    "v_exp_f32 v37, v37\n" \
    "v_exp_f32 v38, v38\n" \
    "v_mfma_f32_32x32x16_bf16 v[180:195], v[24:27], v[76:79], v[180:195]\n" \
    "v_exp_f32 v39, v39\n" \
    "v_cvt_pk_bf16_f32 v68, v36, v37\n" \
    "v_exp_f32 v40, v40\n" \
    "v_exp_f32 v41, v41\n" \
    "v_mfma_f32_32x32x16_bf16 v[52:67], v[20:23], a[28:31], v[52:67]\n" \
    "ds_read_b128 v[20:23], v247 offset:4096\n" \
    "v_cvt_pk_bf16_f32 v69, v38, v39\n" \
    "v_exp_f32 v42, v42\n" \
    "v_exp_f32 v43, v43\n" \
    "v_cvt_pk_bf16_f32 v70, v40, v41\n" \
    "v_exp_f32 v44, v44\n" \
    "v_mfma_f32_32x32x16_bf16 v[180:195], v[28:31], v[80:83], v[180:195]\n" \
    "v_exp_f32 v45, v45\n" \
    "v_cvt_pk_bf16_f32 v71, v42, v43\n" \
    "v_exp_f32 v46, v46\n" \
    "v_exp_f32 v47, v47\n" \
    "v_cvt_pk_bf16_f32 v72, v44, v45\n" \
    "v_exp_f32 v48, v48\n" \
    "v_smfmac_f32_16x16x64_bf16 v[88:91], v[32:35], v[76:83], v246\n" \
    "s_waitcnt lgkmcnt(0)\n" \
    "v_cvt_pk_bf16_f32 v73, v46, v47\n" \
    "v_exp_f32 v49, v49\n" \
    "v_exp_f32 v50, v50\n" \
    "v_exp_f32 v51, v51\n" \
    "v_cvt_pk_bf16_f32 v74, v48, v49\n" \
    "v_cvt_pk_bf16_f32 v75, v50, v51\n" \
    "v_mfma_f32_32x32x16_bf16 v[36:51], v[16:19], a[0:3], v[100:115]\n" \
    "v_exp_f32 v52, v52\n" \
    "v_exp_f32 v53, v53\n" \
    "v_exp_f32 v54, v54\n" \
    "v_mfma_f32_32x32x16_bf16 v[196:211], v[24:27], v[68:71], v[196:211]\n" \
    "v_exp_f32 v55, v55\n" \
    "v_cvt_pk_bf16_f32 v76, v52, v53\n" \
    "v_exp_f32 v56, v56\n" \
    "v_exp_f32 v57, v57\n" \
    "v_mfma_f32_32x32x16_bf16 v[36:51], v[20:23], a[4:7], v[36:51]\n" \
    "v_cvt_pk_bf16_f32 v77, v54, v55\n" \
    "v_exp_f32 v58, v58\n" \
    "v_exp_f32 v59, v59\n" \
    "v_cvt_pk_bf16_f32 v78, v56, v57\n" \
    "v_exp_f32 v60, v60\n" \
    "v_mfma_f32_32x32x16_bf16 v[196:211], v[28:31], v[72:75], v[196:211]\n" \
    "v_exp_f32 v61, v61\n" \
    "v_cvt_pk_bf16_f32 v79, v58, v59\n" \
    "v_exp_f32 v62, v62\n" \
    "v_exp_f32 v63, v63\n" \
    "v_cvt_pk_bf16_f32 v80, v60, v61\n" \
    "v_exp_f32 v64, v64\n" \
    "v_smfmac_f32_16x16x64_bf16 v[92:95], v[32:35], v[68:75], v246\n" \
    "v_cvt_pk_bf16_f32 v81, v62, v63\n" \
    "v_exp_f32 v65, v65\n" \
    "v_exp_f32 v66, v66\n" \
    "v_exp_f32 v67, v67\n" \
    "v_cvt_pk_bf16_f32 v82, v64, v65\n" \
    "v_cvt_pk_bf16_f32 v83, v66, v67\n" \
    "v_mfma_f32_32x32x16_bf16 v[52:67], v[16:19], a[8:11], v[116:131]\n" \
    "v_exp_f32 v36, v36\n" \
    "v_exp_f32 v37, v37\n" \
    "v_exp_f32 v38, v38\n" \
    "v_mfma_f32_32x32x16_bf16 v[212:227], v[24:27], v[76:79], v[212:227]\n" \
    "ds_read_b128 v[24:27], v247 offset:9216\n" \
    "v_exp_f32 v39, v39\n" \
    "v_cvt_pk_bf16_f32 v68, v36, v37\n" \
    "v_exp_f32 v40, v40\n" \
    "v_exp_f32 v41, v41\n" \
    "v_mfma_f32_32x32x16_bf16 v[52:67], v[20:23], a[12:15], v[52:67]\n" \
    "v_cvt_pk_bf16_f32 v69, v38, v39\n" \
    "v_exp_f32 v42, v42\n" \
    "v_exp_f32 v43, v43\n" \
    "v_cvt_pk_bf16_f32 v70, v40, v41\n" \
    "v_exp_f32 v44, v44\n" \
    "v_mfma_f32_32x32x16_bf16 v[212:227], v[28:31], v[80:83], v[212:227]\n" \
    "ds_read_b128 v[28:31], v247 offset:13312\n" \
    "v_exp_f32 v45, v45\n" \
    "v_cvt_pk_bf16_f32 v71, v42, v43\n" \
    "v_exp_f32 v46, v46\n" \
    "v_exp_f32 v47, v47\n" \
    "v_cvt_pk_bf16_f32 v72, v44, v45\n" \
    "v_exp_f32 v48, v48\n" \
    "v_smfmac_f32_16x16x64_bf16 v[96:99], v[32:35], v[76:83], v246\n" \
    "v_cvt_pk_bf16_f32 v73, v46, v47\n" \
    "v_exp_f32 v49, v49\n" \
    "v_exp_f32 v50, v50\n" \
    "v_exp_f32 v51, v51\n" \
    "v_cvt_pk_bf16_f32 v74, v48, v49\n" \
    "v_cvt_pk_bf16_f32 v75, v50, v51\n" \
    "v_mfma_f32_32x32x16_bf16 v[36:51], v[16:19], a[16:19], v[132:147]\n" \
    "s_waitcnt lgkmcnt(0)\n" \
    "v_exp_f32 v52, v52\n" \
    "v_exp_f32 v53, v53\n" \
    "v_exp_f32 v54, v54\n" \
    "v_mfma_f32_32x32x16_bf16 v[164:179], v[24:27], v[68:71], v[164:179]\n" \
    "v_exp_f32 v55, v55\n" \
    "v_cvt_pk_bf16_f32 v76, v52, v53\n" \
    "v_exp_f32 v56, v56\n" \
    "v_exp_f32 v57, v57\n" \
    "v_mfma_f32_32x32x16_bf16 v[36:51], v[20:23], a[20:23], v[36:51]\n" \
    "buffer_load_dwordx4 v240, s[40:43], s46 offen offset:1024 sc1 lds\n" \
    "v_cvt_pk_bf16_f32 v77, v54, v55\n" \
    "v_exp_f32 v58, v58\n" \
    "v_exp_f32 v59, v59\n" \
    "v_cvt_pk_bf16_f32 v78, v56, v57\n" \
    "v_exp_f32 v60, v60\n" \
    "v_mfma_f32_32x32x16_bf16 v[164:179], v[28:31], v[72:75], v[164:179]\n" \
    "v_exp_f32 v61, v61\n" \
    "v_cvt_pk_bf16_f32 v79, v58, v59\n" \
    "v_exp_f32 v62, v62\n" \
    "v_exp_f32 v63, v63\n" \
    "v_cvt_pk_bf16_f32 v80, v60, v61\n" \
    "v_exp_f32 v64, v64\n" \
    "v_smfmac_f32_16x16x64_bf16 v[84:87], v[32:35], v[68:75], v246\n" \
    "v_cvt_pk_bf16_f32 v81, v62, v63\n" \
    "v_exp_f32 v65, v65\n" \
    "v_exp_f32 v66, v66\n" \
    "v_exp_f32 v67, v67\n" \
    "v_cvt_pk_bf16_f32 v82, v64, v65\n" \
    "v_cvt_pk_bf16_f32 v83, v66, v67\n" \
    "v_mfma_f32_32x32x16_bf16 v[52:67], v[16:19], a[24:27], v[148:163]\n" \
    "ds_read_b128 v[16:19], v247 offset:1024\n" \
    "v_exp_f32 v36, v36\n" \
    "v_exp_f32 v37, v37\n" \
    "v_exp_f32 v38, v38\n" \
    "v_mfma_f32_32x32x16_bf16 v[180:195], v[24:27], v[76:79], v[180:195]\n" \
    "v_exp_f32 v39, v39\n" \
    "v_cvt_pk_bf16_f32 v68, v36, v37\n" \
    "v_exp_f32 v40, v40\n" \
    "v_exp_f32 v41, v41\n" \
    "v_mfma_f32_32x32x16_bf16 v[52:67], v[20:23], a[28:31], v[52:67]\n" \
    "ds_read_b128 v[20:23], v247 offset:5120\n" \
    "v_cvt_pk_bf16_f32 v69, v38, v39\n" \
    "v_exp_f32 v42, v42\n" \
    "v_exp_f32 v43, v43\n" \
    "v_cvt_pk_bf16_f32 v70, v40, v41\n" \
    "v_exp_f32 v44, v44\n" \
    "v_mfma_f32_32x32x16_bf16 v[180:195], v[28:31], v[80:83], v[180:195]\n" \
    "v_exp_f32 v45, v45\n" \
    "v_cvt_pk_bf16_f32 v71, v42, v43\n" \
    "v_exp_f32 v46, v46\n" \
    "v_exp_f32 v47, v47\n" \
    "v_cvt_pk_bf16_f32 v72, v44, v45\n" \
    "v_exp_f32 v48, v48\n" \
    "v_smfmac_f32_16x16x64_bf16 v[88:91], v[32:35], v[76:83], v246\n" \
    "s_waitcnt lgkmcnt(0)\n" \
    "v_cvt_pk_bf16_f32 v73, v46, v47\n" \
    "v_exp_f32 v49, v49\n" \
    "v_exp_f32 v50, v50\n" \
    "v_exp_f32 v51, v51\n" \
    "v_cvt_pk_bf16_f32 v74, v48, v49\n" \
    "v_cvt_pk_bf16_f32 v75, v50, v51\n" \
    "v_mfma_f32_32x32x16_bf16 v[36:51], v[16:19], a[0:3], v[100:115]\n" \
    "v_exp_f32 v52, v52\n" \
    "v_exp_f32 v53, v53\n" \
    "v_exp_f32 v54, v54\n" \
    "v_mfma_f32_32x32x16_bf16 v[196:211], v[24:27], v[68:71], v[196:211]\n" \
    "v_exp_f32 v55, v55\n" \
    "v_cvt_pk_bf16_f32 v76, v52, v53\n" \
    "v_exp_f32 v56, v56\n" \
    "v_exp_f32 v57, v57\n" \
    "v_mfma_f32_32x32x16_bf16 v[36:51], v[20:23], a[4:7], v[36:51]\n" \
    "v_cvt_pk_bf16_f32 v77, v54, v55\n" \
    "v_exp_f32 v58, v58\n" \
    "v_exp_f32 v59, v59\n" \
    "v_cvt_pk_bf16_f32 v78, v56, v57\n" \
    "v_exp_f32 v60, v60\n" \
    "v_mfma_f32_32x32x16_bf16 v[196:211], v[28:31], v[72:75], v[196:211]\n" \
    "v_exp_f32 v61, v61\n" \
    "v_cvt_pk_bf16_f32 v79, v58, v59\n" \
    "v_exp_f32 v62, v62\n" \
    "v_exp_f32 v63, v63\n" \
    "v_cvt_pk_bf16_f32 v80, v60, v61\n" \
    "v_exp_f32 v64, v64\n" \
    "v_smfmac_f32_16x16x64_bf16 v[92:95], v[32:35], v[68:75], v246\n" \
    "v_cvt_pk_bf16_f32 v81, v62, v63\n" \
    "v_exp_f32 v65, v65\n" \
    "v_exp_f32 v66, v66\n" \
    "v_exp_f32 v67, v67\n" \
    "v_cvt_pk_bf16_f32 v82, v64, v65\n" \
    "v_cvt_pk_bf16_f32 v83, v66, v67\n" \
    "v_mfma_f32_32x32x16_bf16 v[52:67], v[16:19], a[8:11], v[116:131]\n" \
    "v_exp_f32 v36, v36\n" \
    "v_exp_f32 v37, v37\n" \
    "v_exp_f32 v38, v38\n" \
    "v_mfma_f32_32x32x16_bf16 v[212:227], v[24:27], v[76:79], v[212:227]\n" \
    "ds_read_b128 v[24:27], v247 offset:10240\n" \
    "v_exp_f32 v39, v39\n" \
    "v_cvt_pk_bf16_f32 v68, v36, v37\n" \
    "v_exp_f32 v40, v40\n" \
    "v_exp_f32 v41, v41\n" \
    "v_mfma_f32_32x32x16_bf16 v[52:67], v[20:23], a[12:15], v[52:67]\n" \
    "v_cvt_pk_bf16_f32 v69, v38, v39\n" \
    "v_exp_f32 v42, v42\n" \
    "v_exp_f32 v43, v43\n" \
    "v_cvt_pk_bf16_f32 v70, v40, v41\n" \
    "v_exp_f32 v44, v44\n" \
    "v_mfma_f32_32x32x16_bf16 v[212:227], v[28:31], v[80:83], v[212:227]\n" \
    "ds_read_b128 v[28:31], v247 offset:14336\n" \
    "v_exp_f32 v45, v45\n" \
    "v_cvt_pk_bf16_f32 v71, v42, v43\n" \
    "v_exp_f32 v46, v46\n" \
    "v_exp_f32 v47, v47\n" \
    "v_cvt_pk_bf16_f32 v72, v44, v45\n" \
    "v_exp_f32 v48, v48\n" \
    "v_smfmac_f32_16x16x64_bf16 v[96:99], v[32:35], v[76:83], v246\n" \
    "v_cvt_pk_bf16_f32 v73, v46, v47\n" \
    "v_exp_f32 v49, v49\n" \
    "v_exp_f32 v50, v50\n" \
    "v_exp_f32 v51, v51\n" \
    "v_cvt_pk_bf16_f32 v74, v48, v49\n" \
    "v_cvt_pk_bf16_f32 v75, v50, v51\n" \
    "v_mfma_f32_32x32x16_bf16 v[36:51], v[16:19], a[16:19], v[132:147]\n" \
    "s_waitcnt lgkmcnt(0)\n" \
    "v_exp_f32 v52, v52\n" \
    "v_exp_f32 v53, v53\n" \
    "v_exp_f32 v54, v54\n" \
    "v_mfma_f32_32x32x16_bf16 v[164:179], v[24:27], v[68:71], v[164:179]\n" \
    "v_exp_f32 v55, v55\n" \
    "v_cvt_pk_bf16_f32 v76, v52, v53\n" \
    "v_exp_f32 v56, v56\n" \
    "v_exp_f32 v57, v57\n" \
    "v_mfma_f32_32x32x16_bf16 v[36:51], v[20:23], a[20:23], v[36:51]\n" \
    "buffer_load_dwordx4 v240, s[40:43], s46 offen offset:2048 sc1 lds\n" \
    "v_cvt_pk_bf16_f32 v77, v54, v55\n" \
    "v_exp_f32 v58, v58\n" \
    "v_exp_f32 v59, v59\n" \
    "v_cvt_pk_bf16_f32 v78, v56, v57\n" \
    "v_exp_f32 v60, v60\n" \
    "v_mfma_f32_32x32x16_bf16 v[164:179], v[28:31], v[72:75], v[164:179]\n" \
    "v_exp_f32 v61, v61\n" \
    "v_cvt_pk_bf16_f32 v79, v58, v59\n" \
    "v_exp_f32 v62, v62\n" \
    "v_exp_f32 v63, v63\n" \
    "v_cvt_pk_bf16_f32 v80, v60, v61\n" \
    "v_exp_f32 v64, v64\n" \
    "v_smfmac_f32_16x16x64_bf16 v[84:87], v[32:35], v[68:75], v246\n" \
    "v_cvt_pk_bf16_f32 v81, v62, v63\n" \
    "v_exp_f32 v65, v65\n" \
    "v_exp_f32 v66, v66\n" \
    "v_exp_f32 v67, v67\n" \
    "v_cvt_pk_bf16_f32 v82, v64, v65\n" \
    "v_cvt_pk_bf16_f32 v83, v66, v67\n" \
    "v_mfma_f32_32x32x16_bf16 v[52:67], v[16:19], a[24:27], v[148:163]\n" \
    "ds_read_b128 v[16:19], v247 offset:2048\n" \
    "v_exp_f32 v36, v36\n" \
    "v_exp_f32 v37, v37\n" \
    "v_exp_f32 v38, v38\n" \
    "v_mfma_f32_32x32x16_bf16 v[180:195], v[24:27], v[76:79], v[180:195]\n" \
    "v_exp_f32 v39, v39\n" \
    "v_cvt_pk_bf16_f32 v68, v36, v37\n" \
    "v_exp_f32 v40, v40\n" \
    "v_exp_f32 v41, v41\n" \
    "v_mfma_f32_32x32x16_bf16 v[52:67], v[20:23], a[28:31], v[52:67]\n" \
    "ds_read_b128 v[20:23], v247 offset:6144\n" \
    "v_cvt_pk_bf16_f32 v69, v38, v39\n" \
    "v_exp_f32 v42, v42\n" \
    "v_exp_f32 v43, v43\n" \
    "v_cvt_pk_bf16_f32 v70, v40, v41\n" \
    "v_exp_f32 v44, v44\n" \
    "v_mfma_f32_32x32x16_bf16 v[180:195], v[28:31], v[80:83], v[180:195]\n" \
    "v_exp_f32 v45, v45\n" \
    "v_cvt_pk_bf16_f32 v71, v42, v43\n" \
    "v_exp_f32 v46, v46\n" \
    "v_exp_f32 v47, v47\n" \
    "v_cvt_pk_bf16_f32 v72, v44, v45\n" \
    "v_exp_f32 v48, v48\n" \
    "v_smfmac_f32_16x16x64_bf16 v[88:91], v[32:35], v[76:83], v246\n" \
    "s_waitcnt lgkmcnt(0)\n" \
    "v_cvt_pk_bf16_f32 v73, v46, v47\n" \
    "v_exp_f32 v49, v49\n" \
    "v_exp_f32 v50, v50\n" \
    "v_exp_f32 v51, v51\n" \
    "v_cvt_pk_bf16_f32 v74, v48, v49\n" \
    "v_cvt_pk_bf16_f32 v75, v50, v51\n" \
    "v_mfma_f32_32x32x16_bf16 v[36:51], v[16:19], a[0:3], v[100:115]\n" \
    "v_exp_f32 v52, v52\n" \
    "v_exp_f32 v53, v53\n" \
    "v_exp_f32 v54, v54\n" \
    "v_mfma_f32_32x32x16_bf16 v[196:211], v[24:27], v[68:71], v[196:211]\n" \
    "v_exp_f32 v55, v55\n" \
    "v_cvt_pk_bf16_f32 v76, v52, v53\n" \
    "v_exp_f32 v56, v56\n" \
    "v_exp_f32 v57, v57\n" \
    "v_mfma_f32_32x32x16_bf16 v[36:51], v[20:23], a[4:7], v[36:51]\n" \
    "v_cvt_pk_bf16_f32 v77, v54, v55\n" \
    "v_exp_f32 v58, v58\n" \
    "v_exp_f32 v59, v59\n" \
    "v_cvt_pk_bf16_f32 v78, v56, v57\n" \
    "v_exp_f32 v60, v60\n" \
    "v_mfma_f32_32x32x16_bf16 v[196:211], v[28:31], v[72:75], v[196:211]\n" \
    "v_exp_f32 v61, v61\n" \
    "v_cvt_pk_bf16_f32 v79, v58, v59\n" \
    "v_exp_f32 v62, v62\n" \
    "v_exp_f32 v63, v63\n" \
    "v_cvt_pk_bf16_f32 v80, v60, v61\n" \
    "v_exp_f32 v64, v64\n" \
    "v_smfmac_f32_16x16x64_bf16 v[92:95], v[32:35], v[68:75], v246\n" \
    "v_cvt_pk_bf16_f32 v81, v62, v63\n" \
    "v_exp_f32 v65, v65\n" \
    "v_exp_f32 v66, v66\n" \
    "v_exp_f32 v67, v67\n" \
    "v_cvt_pk_bf16_f32 v82, v64, v65\n" \
    "v_cvt_pk_bf16_f32 v83, v66, v67\n" \
    "v_mfma_f32_32x32x16_bf16 v[52:67], v[16:19], a[8:11], v[116:131]\n" \
    "v_exp_f32 v36, v36\n" \
    "v_exp_f32 v37, v37\n" \
    "v_exp_f32 v38, v38\n" \
    "v_mfma_f32_32x32x16_bf16 v[212:227], v[24:27], v[76:79], v[212:227]\n" \
    "ds_read_b128 v[24:27], v247 offset:11264\n" \
    "v_exp_f32 v39, v39\n" \
    "v_cvt_pk_bf16_f32 v68, v36, v37\n" \
    "v_exp_f32 v40, v40\n" \
    "v_exp_f32 v41, v41\n" \
    "v_mfma_f32_32x32x16_bf16 v[52:67], v[20:23], a[12:15], v[52:67]\n" \
    "v_cvt_pk_bf16_f32 v69, v38, v39\n" \
    "v_exp_f32 v42, v42\n" \
    "v_exp_f32 v43, v43\n" \
    "v_cvt_pk_bf16_f32 v70, v40, v41\n" \
    "v_exp_f32 v44, v44\n" \
    "v_mfma_f32_32x32x16_bf16 v[212:227], v[28:31], v[80:83], v[212:227]\n" \
    "ds_read_b128 v[28:31], v247 offset:15360\n" \
    "v_exp_f32 v45, v45\n" \
    "v_cvt_pk_bf16_f32 v71, v42, v43\n" \
    "v_exp_f32 v46, v46\n" \
    "v_exp_f32 v47, v47\n" \
    "v_cvt_pk_bf16_f32 v72, v44, v45\n" \
    "v_exp_f32 v48, v48\n" \
    "v_smfmac_f32_16x16x64_bf16 v[96:99], v[32:35], v[76:83], v246\n" \
    "v_cvt_pk_bf16_f32 v73, v46, v47\n" \
    "v_exp_f32 v49, v49\n" \
    "v_exp_f32 v50, v50\n" \
    "v_exp_f32 v51, v51\n" \
    "v_cvt_pk_bf16_f32 v74, v48, v49\n" \
    "v_cvt_pk_bf16_f32 v75, v50, v51\n" \
    "v_mfma_f32_32x32x16_bf16 v[36:51], v[16:19], a[16:19], v[132:147]\n" \
    "s_waitcnt lgkmcnt(0)\n" \
    "v_exp_f32 v52, v52\n" \
    "v_exp_f32 v53, v53\n" \
    "v_exp_f32 v54, v54\n" \
    "v_mfma_f32_32x32x16_bf16 v[164:179], v[24:27], v[68:71], v[164:179]\n" \
    "v_exp_f32 v55, v55\n" \
    "v_cvt_pk_bf16_f32 v76, v52, v53\n" \
    "v_exp_f32 v56, v56\n" \
    "v_exp_f32 v57, v57\n" \
    "v_mfma_f32_32x32x16_bf16 v[36:51], v[20:23], a[20:23], v[36:51]\n" \
    "buffer_load_dwordx4 v240, s[40:43], s46 offen offset:3072 sc1 lds\n" \
    "v_cvt_pk_bf16_f32 v77, v54, v55\n" \
    "v_exp_f32 v58, v58\n" \
    "v_exp_f32 v59, v59\n" \
    "v_cvt_pk_bf16_f32 v78, v56, v57\n" \
    "v_exp_f32 v60, v60\n" \
    "v_mfma_f32_32x32x16_bf16 v[164:179], v[28:31], v[72:75], v[164:179]\n" \
    "s_add_u32 s46, s46, 4096\n" \
    "s_add_u32 s58, s58, 0x4000\n" \
    "v_exp_f32 v61, v61\n" \
    "v_cvt_pk_bf16_f32 v79, v58, v59\n" \
    "v_exp_f32 v62, v62\n" \
    "v_exp_f32 v63, v63\n" \
    "v_cvt_pk_bf16_f32 v80, v60, v61\n" \
    "v_exp_f32 v64, v64\n" \
    "v_smfmac_f32_16x16x64_bf16 v[84:87], v[32:35], v[68:75], v246\n" \
    "s_and_b32 s58, s58, 0xffff\n" \
    "s_add_u32 m0, s58, s57\n" \
    "v_cvt_pk_bf16_f32 v81, v62, v63\n" \
    "v_exp_f32 v65, v65\n" \
    "v_exp_f32 v66, v66\n" \
    "v_exp_f32 v67, v67\n" \
    "v_cvt_pk_bf16_f32 v82, v64, v65\n" \
    "v_cvt_pk_bf16_f32 v83, v66, v67\n" \
    "v_mfma_f32_32x32x16_bf16 v[52:67], v[16:19], a[24:27], v[148:163]\n" \
    "ds_read_b128 v[16:19], v247 offset:3072\n" \
    "v_exp_f32 v36, v36\n" \
    "v_exp_f32 v37, v37\n" \
    "v_exp_f32 v38, v38\n" \
    "v_mfma_f32_32x32x16_bf16 v[180:195], v[24:27], v[76:79], v[180:195]\n" \
    "v_exp_f32 v39, v39\n" \
    "v_cvt_pk_bf16_f32 v68, v36, v37\n" \
    "v_exp_f32 v40, v40\n" \
    "v_exp_f32 v41, v41\n" \
    "v_mfma_f32_32x32x16_bf16 v[52:67], v[20:23], a[28:31], v[52:67]\n" \
    "ds_read_b128 v[20:23], v247 offset:7168\n" \
    "v_cvt_pk_bf16_f32 v69, v38, v39\n" \
    "v_exp_f32 v42, v42\n" \
    "v_exp_f32 v43, v43\n" \
    "v_cvt_pk_bf16_f32 v70, v40, v41\n" \
    "v_exp_f32 v44, v44\n" \
    "v_mfma_f32_32x32x16_bf16 v[180:195], v[28:31], v[80:83], v[180:195]\n" \
    "v_exp_f32 v45, v45\n" \
    "v_cvt_pk_bf16_f32 v71, v42, v43\n" \
    "v_exp_f32 v46, v46\n" \
    "v_exp_f32 v47, v47\n" \
    "v_cvt_pk_bf16_f32 v72, v44, v45\n" \
    "v_exp_f32 v48, v48\n" \
    "v_smfmac_f32_16x16x64_bf16 v[88:91], v[32:35], v[76:83], v246\n" \
    "s_waitcnt lgkmcnt(0)\n" \
    "v_cvt_pk_bf16_f32 v73, v46, v47\n" \
    "v_exp_f32 v49, v49\n" \
    "v_exp_f32 v50, v50\n" \
    "v_exp_f32 v51, v51\n" \
    "v_cvt_pk_bf16_f32 v74, v48, v49\n" \
    "v_cvt_pk_bf16_f32 v75, v50, v51\n" \
    "v_mfma_f32_32x32x16_bf16 v[36:51], v[16:19], a[0:3], v[100:115]\n" \
    "v_add_u32 v247, 0x4000, v247\n" \
    "v_exp_f32 v52, v52\n" \
    "v_exp_f32 v53, v53\n" \
    "v_exp_f32 v54, v54\n" \
    "v_mfma_f32_32x32x16_bf16 v[196:211], v[24:27], v[68:71], v[196:211]\n" \
    "v_and_b32 v247, 0xffff, v247\n" \
    "v_exp_f32 v55, v55\n" \
    "v_cvt_pk_bf16_f32 v76, v52, v53\n" \
    "v_exp_f32 v56, v56\n" \
    "v_exp_f32 v57, v57\n" \
    "v_mfma_f32_32x32x16_bf16 v[36:51], v[20:23], a[4:7], v[36:51]\n" \
    "s_waitcnt vmcnt(8)\n" \
    "s_barrier\n" \
    "v_cvt_pk_bf16_f32 v77, v54, v55\n" \
    "v_exp_f32 v58, v58\n" \
    "v_exp_f32 v59, v59\n" \
    "v_cvt_pk_bf16_f32 v78, v56, v57\n" \
    "v_exp_f32 v60, v60\n" \
    "v_mfma_f32_32x32x16_bf16 v[196:211], v[28:31], v[72:75], v[196:211]\n" \
    "v_max3_f32 v228, v84, v85, v88\n" \
    "v_max3_f32 v228, v228, v89, v96\n" \
    "v_exp_f32 v61, v61\n" \
    "v_cvt_pk_bf16_f32 v79, v58, v59\n" \
    "v_exp_f32 v62, v62\n" \
    "v_exp_f32 v63, v63\n" \
    "v_cvt_pk_bf16_f32 v80, v60, v61\n" \
    "v_exp_f32 v64, v64\n" \
    "v_smfmac_f32_16x16x64_bf16 v[92:95], v[32:35], v[68:75], v246\n" \
    "v_cvt_pk_bf16_f32 v81, v62, v63\n" \
    "v_exp_f32 v65, v65\n" \
    "v_exp_f32 v66, v66\n" \
    "v_exp_f32 v67, v67\n" \
    "v_cvt_pk_bf16_f32 v82, v64, v65\n" \
    "v_cvt_pk_bf16_f32 v83, v66, v67\n" \
    "v_mfma_f32_32x32x16_bf16 v[52:67], v[16:19], a[8:11], v[116:131]\n" \
    "s_sub_u32 s56, s56, 1\n" \
    "v_exp_f32 v36, v36\n" \
    "v_exp_f32 v37, v37\n" \
    "v_exp_f32 v38, v38\n" \
    "v_mfma_f32_32x32x16_bf16 v[212:227], v[24:27], v[76:79], v[212:227]\n" \
    "ds_read_b128 v[24:27], v247 offset:8192\n" \
    "v_exp_f32 v39, v39\n" \
    "v_cvt_pk_bf16_f32 v68, v36, v37\n" \
    "v_exp_f32 v40, v40\n" \
    "v_exp_f32 v41, v41\n" \
    "v_mfma_f32_32x32x16_bf16 v[52:67], v[20:23], a[12:15], v[52:67]\n" \
    "v_max3_f32 v228, v228, v97, v92\n" \
    "v_max_f32 v228, v228, v93\n" \
    "v_cmp_nge_f32 vcc, s59, v228\n" \
    "v_cvt_pk_bf16_f32 v69, v38, v39\n" \
    "v_exp_f32 v42, v42\n" \
    "v_exp_f32 v43, v43\n" \
    "v_cvt_pk_bf16_f32 v70, v40, v41\n" \
    "v_exp_f32 v44, v44\n" \
    "v_mfma_f32_32x32x16_bf16 v[212:227], v[28:31], v[80:83], v[212:227]\n" \
    "ds_read_b128 v[28:31], v247 offset:12288\n" \
    "v_exp_f32 v45, v45\n" \
    "v_cvt_pk_bf16_f32 v71, v42, v43\n" \
    "v_exp_f32 v46, v46\n" \
    "v_exp_f32 v47, v47\n" \
    "v_cvt_pk_bf16_f32 v72, v44, v45\n" \
    "v_exp_f32 v48, v48\n" \
    "v_smfmac_f32_16x16x64_bf16 v[96:99], v[32:35], v[76:83], v246\n" \
    "s_cmp_lg_u32 s56, 0\n" \
    "v_cvt_pk_bf16_f32 v73, v46, v47\n" \
    "v_exp_f32 v49, v49\n" \
    "v_exp_f32 v50, v50\n" \
    "v_exp_f32 v51, v51\n" \
    "v_cvt_pk_bf16_f32 v74, v48, v49\n" \
    "v_cvt_pk_bf16_f32 v75, v50, v51\n" \
    "s_cbranch_vccnz La_resc%=\n" \
    "La_resc_x%=:\n" \
    "s_cbranch_scc1 La_loop%=\n" \
    "v_mov_b32 v229, 0\n" \
    "v_mov_b32 v232, 1.0\n" \
    "v_add_f32 v84, v84, v85\n" \
    "v_fma_f32 v229, v84, 0, v229\n" \
    "v_rcp_f32 v228, v84\n" \
    "v_fma_f32 v229, v164, 0, v229\n" \
    "v_fma_f32 v229, v165, 0, v229\n" \
    "v_fma_f32 v229, v166, 0, v229\n" \
    "v_fma_f32 v229, v167, 0, v229\n" \
    "v_fma_f32 v229, v168, 0, v229\n" \
    "v_fma_f32 v229, v169, 0, v229\n" \
    "v_fma_f32 v229, v170, 0, v229\n" \
    "v_fma_f32 v229, v171, 0, v229\n" \
    "v_fma_f32 v229, v172, 0, v229\n" \
    "v_fma_f32 v229, v173, 0, v229\n" \
    "v_fma_f32 v229, v174, 0, v229\n" \
    "v_fma_f32 v229, v175, 0, v229\n" \
    "v_fma_f32 v229, v176, 0, v229\n" \
    "v_fma_f32 v229, v177, 0, v229\n" \
    "v_fma_f32 v229, v178, 0, v229\n" \
    "v_fma_f32 v229, v179, 0, v229\n" \
    "v_max_f32 v231, |v164|, |v165|\n" \
    "v_max3_f32 v231, v231, |v166|, |v167|\n" \
    "v_max3_f32 v231, v231, |v168|, |v169|\n" \
    "v_max3_f32 v231, v231, |v170|, |v171|\n" \
    "v_max3_f32 v231, v231, |v172|, |v173|\n" \
    "v_max3_f32 v231, v231, |v174|, |v175|\n" \
    "v_max3_f32 v231, v231, |v176|, |v177|\n" \
    "v_max3_f32 v231, v231, |v178|, |v179|\n" \
    "v_min_f32 v232, v232, v231\n" \
    "v_mul_f32 v164, v164, v228\n" \
    "v_mul_f32 v165, v165, v228\n" \
    "v_mul_f32 v166, v166, v228\n" \
    "v_mul_f32 v167, v167, v228\n" \
    "v_mul_f32 v168, v168, v228\n" \
    "v_mul_f32 v169, v169, v228\n" \
    "v_mul_f32 v170, v170, v228\n" \
    "v_mul_f32 v171, v171, v228\n" \
    "v_mul_f32 v172, v172, v228\n" \
    "v_mul_f32 v173, v173, v228\n" \
    "v_mul_f32 v174, v174, v228\n" \
    "v_mul_f32 v175, v175, v228\n" \
    "v_mul_f32 v176, v176, v228\n" \
    "v_mul_f32 v177, v177, v228\n" \
    "v_mul_f32 v178, v178, v228\n" \
    "v_mul_f32 v179, v179, v228\n" \
    "v_cvt_pk_bf16_f32 v68, v164, v165\n" \
    "v_cvt_pk_bf16_f32 v69, v166, v167\n" \
    "v_cvt_pk_bf16_f32 v70, v168, v169\n" \
    "v_cvt_pk_bf16_f32 v71, v170, v171\n" \
    "v_cvt_pk_bf16_f32 v72, v172, v173\n" \
    "v_cvt_pk_bf16_f32 v73, v174, v175\n" \
    "v_cvt_pk_bf16_f32 v74, v176, v177\n" \
    "v_cvt_pk_bf16_f32 v75, v178, v179\n" \
    "ds_write_b64 v241, v[68:69] offset:0\n" \
    "ds_write_b64 v241, v[70:71] offset:16\n" \
    "ds_write_b64 v241, v[72:73] offset:32\n" \
    "ds_write_b64 v241, v[74:75] offset:48\n" \
    "s_waitcnt lgkmcnt(4)\n" \
    "v_add_f32 v88, v88, v89\n" \
    "v_fma_f32 v229, v88, 0, v229\n" \
    "v_rcp_f32 v228, v88\n" \
    "v_fma_f32 v229, v180, 0, v229\n" \
    "v_fma_f32 v229, v181, 0, v229\n" \
    "v_fma_f32 v229, v182, 0, v229\n" \
    "v_fma_f32 v229, v183, 0, v229\n" \
    "v_fma_f32 v229, v184, 0, v229\n" \
    "v_fma_f32 v229, v185, 0, v229\n" \
    "v_fma_f32 v229, v186, 0, v229\n" \
    "v_fma_f32 v229, v187, 0, v229\n" \
    "v_fma_f32 v229, v188, 0, v229\n" \
    "v_fma_f32 v229, v189, 0, v229\n" \
    "v_fma_f32 v229, v190, 0, v229\n" \
    "v_fma_f32 v229, v191, 0, v229\n" \
    "v_fma_f32 v229, v192, 0, v229\n" \
    "v_fma_f32 v229, v193, 0, v229\n" \
    "v_fma_f32 v229, v194, 0, v229\n" \
    "v_fma_f32 v229, v195, 0, v229\n" \
    "v_max_f32 v231, |v180|, |v181|\n" \
    "v_max3_f32 v231, v231, |v182|, |v183|\n" \
    "v_max3_f32 v231, v231, |v184|, |v185|\n" \
    "v_max3_f32 v231, v231, |v186|, |v187|\n" \
    "v_max3_f32 v231, v231, |v188|, |v189|\n" \
    "v_max3_f32 v231, v231, |v190|, |v191|\n" \
    "v_max3_f32 v231, v231, |v192|, |v193|\n" \
    "v_max3_f32 v231, v231, |v194|, |v195|\n" \
    "v_min_f32 v232, v232, v231\n" \
    "v_mul_f32 v180, v180, v228\n" \
    "v_mul_f32 v181, v181, v228\n" \
    "v_mul_f32 v182, v182, v228\n" \
    "v_mul_f32 v183, v183, v228\n" \
    "v_mul_f32 v184, v184, v228\n" \
    "v_mul_f32 v185, v185, v228\n" \
    "v_mul_f32 v186, v186, v228\n" \
    "v_mul_f32 v187, v187, v228\n" \
    "v_mul_f32 v188, v188, v228\n" \
    "v_mul_f32 v189, v189, v228\n" \
    "v_mul_f32 v190, v190, v228\n" \
    "v_mul_f32 v191, v191, v228\n" \
    "v_mul_f32 v192, v192, v228\n" \
    "v_mul_f32 v193, v193, v228\n" \
    "v_mul_f32 v194, v194, v228\n" \
    "v_mul_f32 v195, v195, v228\n" \
    "v_cvt_pk_bf16_f32 v76, v180, v181\n" \
    "v_cvt_pk_bf16_f32 v77, v182, v183\n" \
    "v_cvt_pk_bf16_f32 v78, v184, v185\n" \
    "v_cvt_pk_bf16_f32 v79, v186, v187\n" \
    "v_cvt_pk_bf16_f32 v80, v188, v189\n" \
    "v_cvt_pk_bf16_f32 v81, v190, v191\n" \
    "v_cvt_pk_bf16_f32 v82, v192, v193\n" \
    "v_cvt_pk_bf16_f32 v83, v194, v195\n" \
    "ds_write_b64 v241, v[76:77] offset:64\n" \
    "ds_write_b64 v241, v[78:79] offset:80\n" \
    "ds_write_b64 v241, v[80:81] offset:96\n" \
    "ds_write_b64 v241, v[82:83] offset:112\n" \
    "s_waitcnt lgkmcnt(4)\n" \
    "v_add_f32 v92, v92, v93\n" \
    "v_fma_f32 v229, v92, 0, v229\n" \
    "v_rcp_f32 v228, v92\n" \
    "v_fma_f32 v229, v196, 0, v229\n" \
    "v_fma_f32 v229, v197, 0, v229\n" \
    "v_fma_f32 v229, v198, 0, v229\n" \
    "v_fma_f32 v229, v199, 0, v229\n" \
    "v_fma_f32 v229, v200, 0, v229\n" \
    "v_fma_f32 v229, v201, 0, v229\n" \
    "v_fma_f32 v229, v202, 0, v229\n" \
    "v_fma_f32 v229, v203, 0, v229\n" \
    "v_fma_f32 v229, v204, 0, v229\n" \
    "v_fma_f32 v229, v205, 0, v229\n" \
    "v_fma_f32 v229, v206, 0, v229\n" \
    "v_fma_f32 v229, v207, 0, v229\n" \
    "v_fma_f32 v229, v208, 0, v229\n" \
    "v_fma_f32 v229, v209, 0, v229\n" \
    "v_fma_f32 v229, v210, 0, v229\n" \
    "v_fma_f32 v229, v211, 0, v229\n" \
    "v_max_f32 v231, |v196|, |v197|\n" \
    "v_max3_f32 v231, v231, |v198|, |v199|\n" \
    "v_max3_f32 v231, v231, |v200|, |v201|\n" \
    "v_max3_f32 v231, v231, |v202|, |v203|\n" \
    "v_max3_f32 v231, v231, |v204|, |v205|\n" \
    "v_max3_f32 v231, v231, |v206|, |v207|\n" \
    "v_max3_f32 v231, v231, |v208|, |v209|\n" \
    "v_max3_f32 v231, v231, |v210|, |v211|\n" \
    "v_min_f32 v232, v232, v231\n" \
    "v_mul_f32 v196, v196, v228\n" \
    "v_mul_f32 v197, v197, v228\n" \
    "v_mul_f32 v198, v198, v228\n" \
    "v_mul_f32 v199, v199, v228\n" \
    "v_mul_f32 v200, v200, v228\n" \
    "v_mul_f32 v201, v201, v228\n" \
    "v_mul_f32 v202, v202, v228\n" \
    "v_mul_f32 v203, v203, v228\n" \
    "v_mul_f32 v204, v204, v228\n" \
    "v_mul_f32 v205, v205, v228\n" \
    "v_mul_f32 v206, v206, v228\n" \
    "v_mul_f32 v207, v207, v228\n" \
    "v_mul_f32 v208, v208, v228\n" \
    "v_mul_f32 v209, v209, v228\n" \
    "v_mul_f32 v210, v210, v228\n" \
    "v_mul_f32 v211, v211, v228\n" \
    "v_cvt_pk_bf16_f32 v68, v196, v197\n" \
    "v_cvt_pk_bf16_f32 v69, v198, v199\n" \
    "v_cvt_pk_bf16_f32 v70, v200, v201\n" \
    "v_cvt_pk_bf16_f32 v71, v202, v203\n" \
    "v_cvt_pk_bf16_f32 v72, v204, v205\n" \
    "v_cvt_pk_bf16_f32 v73, v206, v207\n" \
    "v_cvt_pk_bf16_f32 v74, v208, v209\n" \
    "v_cvt_pk_bf16_f32 v75, v210, v211\n" \
    "ds_write_b64 v241, v[68:69] offset:128\n" \
    "ds_write_b64 v241, v[70:71] offset:144\n" \
    "ds_write_b64 v241, v[72:73] offset:160\n" \
    "ds_write_b64 v241, v[74:75] offset:176\n" \
    "s_waitcnt lgkmcnt(4)\n" \
    "v_add_f32 v96, v96, v97\n" \
    "v_fma_f32 v229, v96, 0, v229\n" \
    "v_rcp_f32 v228, v96\n" \
    "v_fma_f32 v229, v212, 0, v229\n" \
    "v_fma_f32 v229, v213, 0, v229\n" \
    "v_fma_f32 v229, v214, 0, v229\n" \
    "v_fma_f32 v229, v215, 0, v229\n" \
    "v_fma_f32 v229, v216, 0, v229\n" \
    "v_fma_f32 v229, v217, 0, v229\n" \
    "v_fma_f32 v229, v218, 0, v229\n" \
    "v_fma_f32 v229, v219, 0, v229\n" \
    "v_fma_f32 v229, v220, 0, v229\n" \
    "v_fma_f32 v229, v221, 0, v229\n" \
    "v_fma_f32 v229, v222, 0, v229\n" \
    "v_fma_f32 v229, v223, 0, v229\n" \
    "v_fma_f32 v229, v224, 0, v229\n" \
    "v_fma_f32 v229, v225, 0, v229\n" \
    "v_fma_f32 v229, v226, 0, v229\n" \
    "v_fma_f32 v229, v227, 0, v229\n" \
    "v_max_f32 v231, |v212|, |v213|\n" \
    "v_max3_f32 v231, v231, |v214|, |v215|\n" \
    "v_max3_f32 v231, v231, |v216|, |v217|\n" \
    "v_max3_f32 v231, v231, |v218|, |v219|\n" \
    "v_max3_f32 v231, v231, |v220|, |v221|\n" \
    "v_max3_f32 v231, v231, |v222|, |v223|\n" \
    "v_max3_f32 v231, v231, |v224|, |v225|\n" \
    "v_max3_f32 v231, v231, |v226|, |v227|\n" \
    "v_min_f32 v232, v232, v231\n" \
    "v_mul_f32 v212, v212, v228\n" \
    "v_mul_f32 v213, v213, v228\n" \
    "v_mul_f32 v214, v214, v228\n" \
    "v_mul_f32 v215, v215, v228\n" \
    "v_mul_f32 v216, v216, v228\n" \
    "v_mul_f32 v217, v217, v228\n" \
    "v_mul_f32 v218, v218, v228\n" \
    "v_mul_f32 v219, v219, v228\n" \
    "v_mul_f32 v220, v220, v228\n" \
    "v_mul_f32 v221, v221, v228\n" \
    "v_mul_f32 v222, v222, v228\n" \
    "v_mul_f32 v223, v223, v228\n" \
    "v_mul_f32 v224, v224, v228\n" \
    "v_mul_f32 v225, v225, v228\n" \
    "v_mul_f32 v226, v226, v228\n" \
    "v_mul_f32 v227, v227, v228\n" \
    "v_cvt_pk_bf16_f32 v76, v212, v213\n" \
    "v_cvt_pk_bf16_f32 v77, v214, v215\n" \
    "v_cvt_pk_bf16_f32 v78, v216, v217\n" \
    "v_cvt_pk_bf16_f32 v79, v218, v219\n" \
    "v_cvt_pk_bf16_f32 v80, v220, v221\n" \
    "v_cvt_pk_bf16_f32 v81, v222, v223\n" \
    "v_cvt_pk_bf16_f32 v82, v224, v225\n" \
    "v_cvt_pk_bf16_f32 v83, v226, v227\n" \
    "ds_write_b64 v241, v[76:77] offset:192\n" \
    "ds_write_b64 v241, v[78:79] offset:208\n" \
    "ds_write_b64 v241, v[80:81] offset:224\n" \
    "ds_write_b64 v241, v[82:83] offset:240\n" \
    "s_waitcnt lgkmcnt(4)\n" \
    "v_cmp_u_f32 s[56:57], v229, v229\n" \
    "v_cmp_gt_f32 vcc, 0x06800000, v232\n" \
    "s_waitcnt lgkmcnt(0)\n" \
    "ds_read_b128 v[100:103], v242 offset:0\n" \
    "ds_read_b128 v[104:107], v242 offset:1088\n" \
    "ds_read_b128 v[108:111], v242 offset:2176\n" \
    "ds_read_b128 v[112:115], v242 offset:3264\n" \
    "ds_read_b128 v[116:119], v242 offset:4352\n" \
    "ds_read_b128 v[120:123], v242 offset:5440\n" \
    "ds_read_b128 v[124:127], v242 offset:6528\n" \
    "ds_read_b128 v[128:131], v242 offset:7616\n" \
    "s_or_b64 vcc, vcc, s[56:57]\n" \
    "s_cselect_b32 s50, 1, 0\n" \
    "s_waitcnt lgkmcnt(7)\n" \
    "buffer_store_dwordx4 v[100:103], v243, s[40:43], s48 offen sc1\n" \
    "s_add_u32 s48, s48, 8192\n" \
    "s_waitcnt lgkmcnt(6)\n" \
    "buffer_store_dwordx4 v[104:107], v243, s[40:43], s48 offen sc1\n" \
    "s_add_u32 s48, s48, 8192\n" \
    "s_waitcnt lgkmcnt(5)\n" \
    "buffer_store_dwordx4 v[108:111], v243, s[40:43], s48 offen sc1\n" \
    "s_add_u32 s48, s48, 8192\n" \
    "s_waitcnt lgkmcnt(4)\n" \
    "buffer_store_dwordx4 v[112:115], v243, s[40:43], s48 offen sc1\n" \
    "s_add_u32 s48, s48, 8192\n" \
    "s_waitcnt lgkmcnt(3)\n" \
    "buffer_store_dwordx4 v[116:119], v243, s[40:43], s48 offen sc1\n" \
    "s_add_u32 s48, s48, 8192\n" \
    "s_waitcnt lgkmcnt(2)\n" \
    "buffer_store_dwordx4 v[120:123], v243, s[40:43], s48 offen sc1\n" \
    "s_add_u32 s48, s48, 8192\n" \
    "s_waitcnt lgkmcnt(1)\n" \
    "buffer_store_dwordx4 v[124:127], v243, s[40:43], s48 offen sc1\n" \
    "s_add_u32 s48, s48, 8192\n" \
    "s_waitcnt lgkmcnt(0)\n" \
    "buffer_store_dwordx4 v[128:131], v243, s[40:43], s48 offen sc1\n" \
    "s_add_u32 s48, s48, 8192\n" \
    "s_waitcnt vmcnt(0)\n" \
    "s_branch La_end%=\n" \
    "La_resc%=:\n" \
    "s_mov_b32 s59, 0x4f800000\n" \
    "s_nop 13\n" \
    "v_add_f32 v228, v84, v85\n" \
    "v_frexp_exp_i32_f32 v228, v228\n" \
    "v_add_u32 v228, 96, v228\n" \
    "v_max_i32 v228, 0, v228\n" \
    "v_cvt_f32_i32 v229, v228\n" \
    "v_sub_u32 v228, 0, v228\n" \
    "v_ldexp_f32 v164, v164, v228\n" \
    "v_ldexp_f32 v165, v165, v228\n" \
    "v_ldexp_f32 v166, v166, v228\n" \
    "v_ldexp_f32 v167, v167, v228\n" \
    "v_ldexp_f32 v168, v168, v228\n" \
    "v_ldexp_f32 v169, v169, v228\n" \
    "v_ldexp_f32 v170, v170, v228\n" \
    "v_ldexp_f32 v171, v171, v228\n" \
    "v_ldexp_f32 v172, v172, v228\n" \
    "v_ldexp_f32 v173, v173, v228\n" \
    "v_ldexp_f32 v174, v174, v228\n" \
    "v_ldexp_f32 v175, v175, v228\n" \
    "v_ldexp_f32 v176, v176, v228\n" \
    "v_ldexp_f32 v177, v177, v228\n" \
    "v_ldexp_f32 v178, v178, v228\n" \
    "v_ldexp_f32 v179, v179, v228\n" \
    "v_ldexp_f32 v84, v84, v228\n" \
    "v_ldexp_f32 v85, v85, v228\n" \
    "v_ldexp_f32 v86, v86, v228\n" \
    "v_ldexp_f32 v87, v87, v228\n" \
    "v_sub_f32 v100, v100, v229\n" \
    "v_sub_f32 v101, v101, v229\n" \
    "v_sub_f32 v102, v102, v229\n" \
    "v_sub_f32 v103, v103, v229\n" \
    "v_sub_f32 v104, v104, v229\n" \
    "v_sub_f32 v105, v105, v229\n" \
    "v_sub_f32 v106, v106, v229\n" \
    "v_sub_f32 v107, v107, v229\n" \
    "v_sub_f32 v108, v108, v229\n" \
    "v_sub_f32 v109, v109, v229\n" \
    "v_sub_f32 v110, v110, v229\n" \
    "v_sub_f32 v111, v111, v229\n" \
    "v_sub_f32 v112, v112, v229\n" \
    "v_sub_f32 v113, v113, v229\n" \
    "v_sub_f32 v114, v114, v229\n" \
    "v_sub_f32 v115, v115, v229\n" \
    "v_ldexp_f32 v36, v36, v228\n" \
    "v_ldexp_f32 v37, v37, v228\n" \
    "v_ldexp_f32 v38, v38, v228\n" \
    "v_ldexp_f32 v39, v39, v228\n" \
    "v_ldexp_f32 v40, v40, v228\n" \
    "v_ldexp_f32 v41, v41, v228\n" \
    "v_ldexp_f32 v42, v42, v228\n" \
    "v_ldexp_f32 v43, v43, v228\n" \
    "v_ldexp_f32 v44, v44, v228\n" \
    "v_ldexp_f32 v45, v45, v228\n" \
    "v_ldexp_f32 v46, v46, v228\n" \
    "v_ldexp_f32 v47, v47, v228\n" \
    "v_ldexp_f32 v48, v48, v228\n" \
    "v_ldexp_f32 v49, v49, v228\n" \
    "v_ldexp_f32 v50, v50, v228\n" \
    "v_ldexp_f32 v51, v51, v228\n" \
    "v_cvt_pk_bf16_f32 v68, v36, v37\n" \
    "v_cvt_pk_bf16_f32 v69, v38, v39\n" \
    "v_cvt_pk_bf16_f32 v70, v40, v41\n" \
    "v_cvt_pk_bf16_f32 v71, v42, v43\n" \
    "v_cvt_pk_bf16_f32 v72, v44, v45\n" \
    "v_cvt_pk_bf16_f32 v73, v46, v47\n" \
    "v_cvt_pk_bf16_f32 v74, v48, v49\n" \
    "v_cvt_pk_bf16_f32 v75, v50, v51\n" \
    "v_add_f32 v228, v88, v89\n" \
    "v_frexp_exp_i32_f32 v228, v228\n" \
    "v_add_u32 v228, 96, v228\n" \
    "v_max_i32 v228, 0, v228\n" \
    "v_cvt_f32_i32 v229, v228\n" \
    "v_sub_u32 v228, 0, v228\n" \
    "v_ldexp_f32 v180, v180, v228\n" \
    "v_ldexp_f32 v181, v181, v228\n" \
    "v_ldexp_f32 v182, v182, v228\n" \
    "v_ldexp_f32 v183, v183, v228\n" \
    "v_ldexp_f32 v184, v184, v228\n" \
    "v_ldexp_f32 v185, v185, v228\n" \
    "v_ldexp_f32 v186, v186, v228\n" \
    "v_ldexp_f32 v187, v187, v228\n" \
    "v_ldexp_f32 v188, v188, v228\n" \
    "v_ldexp_f32 v189, v189, v228\n" \
    "v_ldexp_f32 v190, v190, v228\n" \
    "v_ldexp_f32 v191, v191, v228\n" \
    "v_ldexp_f32 v192, v192, v228\n" \
    "v_ldexp_f32 v193, v193, v228\n" \
    "v_ldexp_f32 v194, v194, v228\n" \
    "v_ldexp_f32 v195, v195, v228\n" \
    "v_ldexp_f32 v88, v88, v228\n" \
    "v_ldexp_f32 v89, v89, v228\n" \
    "v_ldexp_f32 v90, v90, v228\n" \
    "v_ldexp_f32 v91, v91, v228\n" \
    "v_sub_f32 v116, v116, v229\n" \
    "v_sub_f32 v117, v117, v229\n" \
    "v_sub_f32 v118, v118, v229\n" \
    "v_sub_f32 v119, v119, v229\n" \
    "v_sub_f32 v120, v120, v229\n" \
    "v_sub_f32 v121, v121, v229\n" \
    "v_sub_f32 v122, v122, v229\n" \
    "v_sub_f32 v123, v123, v229\n" \
    "v_sub_f32 v124, v124, v229\n" \
    "v_sub_f32 v125, v125, v229\n" \
    "v_sub_f32 v126, v126, v229\n" \
    "v_sub_f32 v127, v127, v229\n" \
    "v_sub_f32 v128, v128, v229\n" \
    "v_sub_f32 v129, v129, v229\n" \
    "v_sub_f32 v130, v130, v229\n" \
    "v_sub_f32 v131, v131, v229\n" \
    "v_sub_f32 v52, v52, v229\n" \
    "v_sub_f32 v53, v53, v229\n" \
    "v_sub_f32 v54, v54, v229\n" \
    "v_sub_f32 v55, v55, v229\n" \
    "v_sub_f32 v56, v56, v229\n" \
    "v_sub_f32 v57, v57, v229\n" \
    "v_sub_f32 v58, v58, v229\n" \
    "v_sub_f32 v59, v59, v229\n" \
    "v_sub_f32 v60, v60, v229\n" \
    "v_sub_f32 v61, v61, v229\n" \
    "v_sub_f32 v62, v62, v229\n" \
    "v_sub_f32 v63, v63, v229\n" \
    "v_sub_f32 v64, v64, v229\n" \
    "v_sub_f32 v65, v65, v229\n" \
    "v_sub_f32 v66, v66, v229\n" \
    "v_sub_f32 v67, v67, v229\n" \
    "v_add_f32 v228, v92, v93\n" \
    "v_frexp_exp_i32_f32 v228, v228\n" \
    "v_add_u32 v228, 96, v228\n" \
    "v_max_i32 v228, 0, v228\n" \
    "v_cvt_f32_i32 v229, v228\n" \
    "v_sub_u32 v228, 0, v228\n" \
    "v_ldexp_f32 v196, v196, v228\n" \
    "v_ldexp_f32 v197, v197, v228\n" \
    "v_ldexp_f32 v198, v198, v228\n" \
    "v_ldexp_f32 v199, v199, v228\n" \
    "v_ldexp_f32 v200, v200, v228\n" \
    "v_ldexp_f32 v201, v201, v228\n" \
    "v_ldexp_f32 v202, v202, v228\n" \
    "v_ldexp_f32 v203, v203, v228\n" \
    "v_ldexp_f32 v204, v204, v228\n" \
    "v_ldexp_f32 v205, v205, v228\n" \
    "v_ldexp_f32 v206, v206, v228\n" \
    "v_ldexp_f32 v207, v207, v228\n" \
    "v_ldexp_f32 v208, v208, v228\n" \
    "v_ldexp_f32 v209, v209, v228\n" \
    "v_ldexp_f32 v210, v210, v228\n" \
    "v_ldexp_f32 v211, v211, v228\n" \
    "v_ldexp_f32 v92, v92, v228\n" \
    "v_ldexp_f32 v93, v93, v228\n" \
    "v_ldexp_f32 v94, v94, v228\n" \
    "v_ldexp_f32 v95, v95, v228\n" \
    "v_sub_f32 v132, v132, v229\n" \
    "v_sub_f32 v133, v133, v229\n" \
    "v_sub_f32 v134, v134, v229\n" \
    "v_sub_f32 v135, v135, v229\n" \
    "v_sub_f32 v136, v136, v229\n" \
    "v_sub_f32 v137, v137, v229\n" \
    "v_sub_f32 v138, v138, v229\n" \
    "v_sub_f32 v139, v139, v229\n" \
    "v_sub_f32 v140, v140, v229\n" \
    "v_sub_f32 v141, v141, v229\n" \
    "v_sub_f32 v142, v142, v229\n" \
    "v_sub_f32 v143, v143, v229\n" \
    "v_sub_f32 v144, v144, v229\n" \
    "v_sub_f32 v145, v145, v229\n" \
    "v_sub_f32 v146, v146, v229\n" \
    "v_sub_f32 v147, v147, v229\n" \
    "v_add_f32 v228, v96, v97\n" \
    "v_frexp_exp_i32_f32 v228, v228\n" \
    "v_add_u32 v228, 96, v228\n" \
    "v_max_i32 v228, 0, v228\n" \
    "v_cvt_f32_i32 v229, v228\n" \
    "v_sub_u32 v228, 0, v228\n" \
    "v_ldexp_f32 v212, v212, v228\n" \
    "v_ldexp_f32 v213, v213, v228\n" \
    "v_ldexp_f32 v214, v214, v228\n" \
    "v_ldexp_f32 v215, v215, v228\n" \
    "v_ldexp_f32 v216, v216, v228\n" \
    "v_ldexp_f32 v217, v217, v228\n" \
    "v_ldexp_f32 v218, v218, v228\n" \
    "v_ldexp_f32 v219, v219, v228\n" \
    "v_ldexp_f32 v220, v220, v228\n" \
    "v_ldexp_f32 v221, v221, v228\n" \
    "v_ldexp_f32 v222, v222, v228\n" \
    "v_ldexp_f32 v223, v223, v228\n" \
    "v_ldexp_f32 v224, v224, v228\n" \
    "v_ldexp_f32 v225, v225, v228\n" \
    "v_ldexp_f32 v226, v226, v228\n" \
    "v_ldexp_f32 v227, v227, v228\n" \
    "v_ldexp_f32 v96, v96, v228\n" \
    "v_ldexp_f32 v97, v97, v228\n" \
    "v_ldexp_f32 v98, v98, v228\n" \
    "v_ldexp_f32 v99, v99, v228\n" \
    "v_sub_f32 v148, v148, v229\n" \
    "v_sub_f32 v149, v149, v229\n" \
    "v_sub_f32 v150, v150, v229\n" \
    "v_sub_f32 v151, v151, v229\n" \
    "v_sub_f32 v152, v152, v229\n" \
    "v_sub_f32 v153, v153, v229\n" \
    "v_sub_f32 v154, v154, v229\n" \
    "v_sub_f32 v155, v155, v229\n" \
    "v_sub_f32 v156, v156, v229\n" \
    "v_sub_f32 v157, v157, v229\n" \
    "v_sub_f32 v158, v158, v229\n" \
    "v_sub_f32 v159, v159, v229\n" \
    "v_sub_f32 v160, v160, v229\n" \
    "v_sub_f32 v161, v161, v229\n" \
    "v_sub_f32 v162, v162, v229\n" \
    "v_sub_f32 v163, v163, v229\n" \
    "s_nop 1\n" \
    "s_branch La_resc_x%=\n" \
    "La_end%=:\n"
#define ATTN_CLOB "v16", "v17", "v18", "v19", "v20", "v21", "v22", "v23", "v24", "v25", "v26", "v27", "v28", "v29", "v30", "v31", "v32", "v33", "v34", "v35", "v36", "v37", "v38", "v39", "v40", "v41", "v42", "v43", "v44", "v45", "v46", "v47", "v48", "v49", "v50", "v51", "v52", "v53", "v54", "v55", "v56", "v57", "v58", "v59", "v60", "v61", "v62", "v63", "v64", "v65", "v66", "v67", "v68", "v69", "v70", "v71", "v72", "v73", "v74", "v75", "v76", "v77", "v78", "v79", "v80", "v81", "v82", "v83", "v84", "v85", "v86", "v87", "v88", "v89", "v90", "v91", "v92", "v93", "v94", "v95", "v96", "v97", "v98", "v99", "v100", "v101", "v102", "v103", "v104", "v105", "v106", "v107", "v108", "v109", "v110", "v111", "v112", "v113", "v114", "v115", "v116", "v117", "v118", "v119", "v120", "v121", "v122", "v123", "v124", "v125", "v126", "v127", "v128", "v129", "v130", "v131", "v132", "v133", "v134", "v135", "v136", "v137", "v138", "v139", "v140", "v141", "v142", "v143", "v144", "v145", "v146", "v147", "v148", "v149", "v150", "v151", "v152", "v153", "v154", "v155", "v156", "v157", "v158", "v159", "v160", "v161", "v162", "v163", "v164", "v165", "v166", "v167", "v168", "v169", "v170", "v171", "v172", "v173", "v174", "v175", "v176", "v177", "v178", "v179", "v180", "v181", "v182", "v183", "v184", "v185", "v186", "v187", "v188", "v189", "v190", "v191", "v192", "v193", "v194", "v195", "v196", "v197", "v198", "v199", "v200", "v201", "v202", "v203", "v204", "v205", "v206", "v207", "v208", "v209", "v210", "v211", "v212", "v213", "v214", "v215", "v216", "v217", "v218", "v219", "v220", "v221", "v222", "v223", "v224", "v225", "v226", "v227", "v228", "v229", "v230", "v231", "v232", "a0", "a1", "a2", "a3", "a4", "a5", "a6", "a7", "a8", "a9", "a10", "a11", "a12", "a13", "a14", "a15", "a16", "a17", "a18", "a19", "a20", "a21", "a22", "a23", "a24", "a25", "a26", "a27", "a28", "a29", "a30", "a31", "s56", "s57", "s58", "s59", "vcc", "scc", "memory"
#define ATTN_LIFT 96.f
#define FA_ASM \
    "s_nop 4\n" \
    "s_mov_b32 s46, -1\n" \
    "s_mov_b32 s47, 0\n" \
    "s_mov_b32 s48, 0x3e0293ee\n" \
    "s_mov_b32 s49, 0x3e0293ee\n" \
    "v_cmp_gt_i32 s[50:51], 0, v245\n" \
    "v_cmp_gt_i32 s[52:53], 1, v245\n" \
    "v_cmp_gt_i32 s[54:55], 2, v245\n" \
    "v_cmp_gt_i32 s[56:57], 3, v245\n" \
    "v_mov_b32 v4, 0xff800000\n" \
    "v_mov_b32 v5, 1\n" \
    "v_mov_b32 v12, 0x3f803f80\n" \
    "v_mov_b32 v13, 0x3f803f80\n" \
    "v_mov_b32 v14, 0x3f803f80\n" \
    "v_mov_b32 v15, 0x3f803f80\n" \
    "s_mov_b32 s68, 33024\n" \
    "s_branch La_s_i\n" \
    "La_s:\n" \
    "s_sleep 2\n" \
    "La_s_i:\n" \
    "s_cmp_eq_u32 s90, 0\n" \
    "s_cbranch_scc1 La_s_x\n" \
    "s_sub_u32 s90, s90, 1\n" \
    "buffer_load_dword v0, off, s[40:43], s68 sc1\n" \
    "s_waitcnt vmcnt(0)\n" \
    "v_cmp_gt_u32 vcc, 1, v0\n" \
    "s_cbranch_vccnz La_s\n" \
    "La_s_x:\n" \
    "v_readfirstlane_b32 s68, v0\n" \
    "s_and_b32 s68, s68, 0x7000\n" \
    "s_xor_b32 s68, s68, 0x1000\n" \
    "s_lshr_b32 s60, s44, 8\n" \
    "s_lshl_b32 s60, s60, 9\n" \
    "s_and_b32 s61, s44, 31\n" \
    "s_lshl_b32 s61, s61, 4\n" \
    "s_add_u32 s60, s60, s61\n" \
    "s_add_u32 s64, s60, 40960\n" \
    "s_branch La_t0_i\n" \
    "La_t0:\n" \
    "s_sleep 2\n" \
    "La_t0_i:\n" \
    "s_cmp_eq_u32 s90, 0\n" \
    "s_cbranch_scc1 La_t0_x\n" \
    "s_sub_u32 s90, s90, 1\n" \
    "buffer_load_dwordx3 v[0:2], off, s[40:43], s64 sc1\n" \
    "s_waitcnt vmcnt(0)\n" \
    "v_min3_u32 v0, v0, v1, v2\n" \
    "v_cmp_gt_u32 vcc, 2, v0\n" \
    "s_cbranch_vccnz La_t0\n" \
    "La_t0_x:\n" \
    "s_lshl_b32 s60, s44, 13\n" \
    "s_add_u32 s61, s60, 12582912\n" \
    "buffer_load_dwordx4 v[48:51], v240, s[40:43], s61 offen offset:0 sc1\n" \
    "buffer_load_dwordx4 v[52:55], v240, s[40:43], s61 offen offset:1024 sc1\n" \
    "buffer_load_dwordx4 v[56:59], v240, s[40:43], s61 offen offset:2048 sc1\n" \
    "buffer_load_dwordx4 v[60:63], v240, s[40:43], s61 offen offset:3072 sc1\n" \
    "s_add_u32 s61, s60, 12587008\n" \
    "buffer_load_dwordx4 v[64:67], v240, s[40:43], s61 offen offset:0 sc1\n" \
    "buffer_load_dwordx4 v[68:71], v240, s[40:43], s61 offen offset:1024 sc1\n" \
    "buffer_load_dwordx4 v[72:75], v240, s[40:43], s61 offen offset:2048 sc1\n" \
    "buffer_load_dwordx4 v[76:79], v240, s[40:43], s61 offen offset:3072 sc1\n" \
    "s_add_u32 s61, s60, 46137344\n" \
    "buffer_load_dwordx4 v[16:19], v240, s[40:43], s61 offen offset:0 sc1\n" \
    "buffer_load_dwordx4 v[20:23], v240, s[40:43], s61 offen offset:1024 sc1\n" \
    "buffer_load_dwordx4 v[24:27], v240, s[40:43], s61 offen offset:2048 sc1\n" \
    "buffer_load_dwordx4 v[28:31], v240, s[40:43], s61 offen offset:3072 sc1\n" \
    "s_add_u32 s61, s60, 46141440\n" \
    "buffer_load_dwordx4 v[32:35], v240, s[40:43], s61 offen offset:0 sc1\n" \
    "buffer_load_dwordx4 v[36:39], v240, s[40:43], s61 offen offset:1024 sc1\n" \
    "buffer_load_dwordx4 v[40:43], v240, s[40:43], s61 offen offset:2048 sc1\n" \
    "buffer_load_dwordx4 v[44:47], v240, s[40:43], s61 offen offset:3072 sc1\n" \
    "s_add_u32 s61, s60, 29360128\n" \
    "buffer_load_dwordx4 v[80:83], v243, s[40:43], s61 offen offset:0 sc1\n" \
    "buffer_load_dwordx4 v[84:87], v243, s[40:43], s61 offen offset:64 sc1\n" \
    "buffer_load_dwordx4 v[88:91], v243, s[40:43], s61 offen offset:2048 sc1\n" \
    "buffer_load_dwordx4 v[92:95], v243, s[40:43], s61 offen offset:2112 sc1\n" \
    "s_add_u32 s61, s60, 29364224\n" \
    "buffer_load_dwordx4 v[96:99], v243, s[40:43], s61 offen offset:0 sc1\n" \
    "buffer_load_dwordx4 v[100:103], v243, s[40:43], s61 offen offset:64 sc1\n" \
    "buffer_load_dwordx4 v[104:107], v243, s[40:43], s61 offen offset:2048 sc1\n" \
    "buffer_load_dwordx4 v[108:111], v243, s[40:43], s61 offen offset:2112 sc1\n" \
    "La_loop:\n" \
    "s_add_u32 s63, s44, s45\n" \
    "s_min_u32 s62, s63, 2047\n" \
    "s_lshr_b32 s60, s62, 8\n" \
    "s_lshl_b32 s60, s60, 9\n" \
    "s_and_b32 s61, s62, 31\n" \
    "s_lshl_b32 s61, s61, 4\n" \
    "s_add_u32 s60, s60, s61\n" \
    "s_add_u32 s64, s60, 40960\n" \
    "s_and_b32 s60, s44, 31\n" \
    "s_lshl_b32 s60, s60, 7\n" \
    "s_add_u32 s60, s60, s68\n" \
    "s_lshr_b32 s61, s44, 5\n" \
    "s_add_u32 s61, s61, 1\n" \
    "s_branch La_p_i\n" \
    "La_p:\n" \
    "s_sleep 2\n" \
    "La_p_i:\n" \
    "s_cmp_eq_u32 s90, 0\n" \
    "s_cbranch_scc1 La_p_x\n" \
    "s_sub_u32 s90, s90, 1\n" \
    "buffer_load_dwordx3 v[0:2], off, s[40:43], s64 sc1\n" \
    "buffer_load_dword v3, off, s[40:43], s60 sc1\n" \
    "s_waitcnt vmcnt(0)\n" \
    "v_min3_u32 v0, v0, v1, v2\n" \
    "v_cmp_gt_u32 s[66:67], s61, v3\n" \
    "v_cmp_gt_u32 vcc, 2, v0\n" \
    "s_or_b64 vcc, vcc, s[66:67]\n" \
    "s_cbranch_scc1 La_p\n" \
    "La_p_x:\n" \
    "s_waitcnt vmcnt(0)\n" \
    "s_lshl_b32 s60, s44, 11\n" \
    "s_add_u32 s61, s60, 7340032\n" \
    "s_add_u32 s60, s60, 2097152\n" \
    "buffer_load_dwordx4 v[112:115], v242, s[40:43], s61 offen offset:0 sc1\n" \
    "buffer_load_dwordx4 v[116:119], v242, s[40:43], s61 offen offset:512 sc1\n" \
    "buffer_load_dwordx4 v[120:123], v242, s[40:43], s61 offen offset:1024 sc1\n" \
    "buffer_load_dwordx4 v[124:127], v242, s[40:43], s61 offen offset:1536 sc1\n" \
    "buffer_load_dwordx4 v[128:131], v244, s[40:43], s60 offen offset:0 sc1\n" \
    "buffer_load_dwordx4 v[132:135], v244, s[40:43], s60 offen offset:512 sc1\n" \
    "buffer_load_dwordx4 v[136:139], v244, s[40:43], s60 offen offset:1024 sc1\n" \
    "buffer_load_dwordx4 v[140:143], v244, s[40:43], s60 offen offset:1536 sc1\n" \
    "s_lshl_b32 s60, s44, 13\n" \
    "s_add_u32 s60, s60, 62914560\n" \
    "v_mfma_f32_16x16x32_bf16 v[148:151], v[48:51], v[16:19], 0\n" \
    "v_mfma_f32_16x16x32_bf16 v[148:151], v[52:55], v[20:23], v[148:151]\n" \
    "v_mfma_f32_16x16x32_bf16 v[148:151], v[56:59], v[24:27], v[148:151]\n" \
    "v_mfma_f32_16x16x32_bf16 v[148:151], v[60:63], v[28:31], v[148:151]\n" \
    "s_waitcnt vmcnt(7)\n" \
    "v_mfma_f32_16x16x32_bf16 v[144:147], v[112:115], v[16:19], 0\n" \
    "s_waitcnt vmcnt(6)\n" \
    "v_mfma_f32_16x16x32_bf16 v[144:147], v[116:119], v[20:23], v[144:147]\n" \
    "s_waitcnt vmcnt(5)\n" \
    "v_mfma_f32_16x16x32_bf16 v[144:147], v[120:123], v[24:27], v[144:147]\n" \
    "s_waitcnt vmcnt(4)\n" \
    "v_mfma_f32_16x16x32_bf16 v[144:147], v[124:127], v[28:31], v[144:147]\n" \
    "v_cndmask_b32 v148, v148, v4, s[50:51]\n" \
    "v_cndmask_b32 v149, v149, v4, s[52:53]\n" \
    "v_cndmask_b32 v150, v150, v4, s[54:55]\n" \
    "v_cndmask_b32 v151, v151, v4, s[56:57]\n" \
    "s_nop 0\n" \
    "s_nop 0\n" \
    "s_nop 0\n" \
    "s_nop 0\n" \
    "v_cndmask_b32 v144, v144, v4, s[46:47]\n" \
    "v_cndmask_b32 v145, v145, v4, s[46:47]\n" \
    "v_cndmask_b32 v146, v146, v4, s[46:47]\n" \
    "v_cndmask_b32 v147, v147, v4, s[46:47]\n" \
    "v_max3_f32 v7, v144, v145, v146\n" \
    "v_max3_f32 v7, v7, v147, v148\n" \
    "v_max3_f32 v7, v7, v149, v150\n" \
    "v_max_f32 v8, v7, v151\n" \
    "v_max_f32 v7, v7, v151\n" \
    "s_nop 1\n" \
    "v_permlane32_swap_b32 v7, v8\n" \
    "v_max_f32 v9, v7, v8\n" \
    "v_max_f32 v7, v7, v8\n" \
    "s_nop 1\n" \
    "v_permlane16_swap_b32 v7, v9\n" \
    "v_max_f32 v7, v7, v9\n" \
    "v_mul_f32 v6, 0xbe0293ee, v7\n" \
    "v_pk_fma_f32 v[144:145], v[144:145], s[48:49], v[6:7] op_sel_hi:[1,0,0]\n" \
    "v_pk_fma_f32 v[146:147], v[146:147], s[48:49], v[6:7] op_sel_hi:[1,0,0]\n" \
    "v_pk_fma_f32 v[148:149], v[148:149], s[48:49], v[6:7] op_sel_hi:[1,0,0]\n" \
    "v_pk_fma_f32 v[150:151], v[150:151], s[48:49], v[6:7] op_sel_hi:[1,0,0]\n" \
    "v_exp_f32 v144, v144\n" \
    "v_exp_f32 v145, v145\n" \
    "v_exp_f32 v146, v146\n" \
    "v_exp_f32 v147, v147\n" \
    "v_exp_f32 v148, v148\n" \
    "v_exp_f32 v149, v149\n" \
    "v_exp_f32 v150, v150\n" \
    "v_exp_f32 v151, v151\n" \
    "v_cvt_pk_bf16_f32 v160, v144, v145\n" \
    "v_cvt_pk_bf16_f32 v161, v146, v147\n" \
    "v_cvt_pk_bf16_f32 v156, v148, v149\n" \
    "v_cvt_pk_bf16_f32 v157, v150, v151\n" \
    "s_nop 1\n" \
    "v_mfma_f32_16x16x16_bf16 v[164:167], v[12:13], v[156:157], 0\n" \
    "v_mfma_f32_16x16x16_bf16 v[168:171], v[80:81], v[156:157], 0\n" \
    "v_mfma_f32_16x16x16_bf16 v[172:175], v[84:85], v[156:157], 0\n" \
    "v_mfma_f32_16x16x16_bf16 v[176:179], v[88:89], v[156:157], 0\n" \
    "v_mfma_f32_16x16x16_bf16 v[180:183], v[92:93], v[156:157], 0\n" \
    "v_mfma_f32_16x16x16_bf16 v[184:187], v[96:97], v[156:157], 0\n" \
    "v_mfma_f32_16x16x16_bf16 v[188:191], v[100:101], v[156:157], 0\n" \
    "v_mfma_f32_16x16x16_bf16 v[192:195], v[104:105], v[156:157], 0\n" \
    "v_mfma_f32_16x16x16_bf16 v[196:199], v[108:109], v[156:157], 0\n" \
    "v_mfma_f32_16x16x16_bf16 v[164:167], v[12:13], v[160:161], v[164:167]\n" \
    "s_waitcnt vmcnt(3)\n" \
    "v_mfma_f32_16x16x16_bf16 v[168:171], v[128:129], v[160:161], v[168:171]\n" \
    "v_mfma_f32_16x16x16_bf16 v[172:175], v[130:131], v[160:161], v[172:175]\n" \
    "s_waitcnt vmcnt(2)\n" \
    "v_mfma_f32_16x16x16_bf16 v[176:179], v[132:133], v[160:161], v[176:179]\n" \
    "v_mfma_f32_16x16x16_bf16 v[180:183], v[134:135], v[160:161], v[180:183]\n" \
    "s_waitcnt vmcnt(1)\n" \
    "v_mfma_f32_16x16x16_bf16 v[184:187], v[136:137], v[160:161], v[184:187]\n" \
    "v_mfma_f32_16x16x16_bf16 v[188:191], v[138:139], v[160:161], v[188:191]\n" \
    "s_waitcnt vmcnt(0)\n" \
    "v_mfma_f32_16x16x16_bf16 v[192:195], v[140:141], v[160:161], v[192:195]\n" \
    "v_mfma_f32_16x16x16_bf16 v[196:199], v[142:143], v[160:161], v[196:199]\n" \
    "v_rcp_f32 v6, v164\n" \
    "s_nop 0\n" \
    "v_pk_mul_f32 v[168:169], v[168:169], v[6:7] op_sel_hi:[1,0]\n" \
    "v_pk_mul_f32 v[170:171], v[170:171], v[6:7] op_sel_hi:[1,0]\n" \
    "v_cvt_pk_bf16_f32 v200, v168, v169\n" \
    "v_cvt_pk_bf16_f32 v201, v170, v171\n" \
    "v_pk_mul_f32 v[172:173], v[172:173], v[6:7] op_sel_hi:[1,0]\n" \
    "v_pk_mul_f32 v[174:175], v[174:175], v[6:7] op_sel_hi:[1,0]\n" \
    "v_cvt_pk_bf16_f32 v202, v172, v173\n" \
    "v_cvt_pk_bf16_f32 v203, v174, v175\n" \
    "buffer_store_dwordx4 v[200:203], v240, s[40:43], s60 offen offset:0 sc1\n" \
    "v_pk_mul_f32 v[176:177], v[176:177], v[6:7] op_sel_hi:[1,0]\n" \
    "v_pk_mul_f32 v[178:179], v[178:179], v[6:7] op_sel_hi:[1,0]\n" \
    "v_cvt_pk_bf16_f32 v204, v176, v177\n" \
    "v_cvt_pk_bf16_f32 v205, v178, v179\n" \
    "v_pk_mul_f32 v[180:181], v[180:181], v[6:7] op_sel_hi:[1,0]\n" \
    "v_pk_mul_f32 v[182:183], v[182:183], v[6:7] op_sel_hi:[1,0]\n" \
    "v_cvt_pk_bf16_f32 v206, v180, v181\n" \
    "v_cvt_pk_bf16_f32 v207, v182, v183\n" \
    "buffer_store_dwordx4 v[204:207], v240, s[40:43], s60 offen offset:1024 sc1\n" \
    "v_pk_mul_f32 v[184:185], v[184:185], v[6:7] op_sel_hi:[1,0]\n" \
    "v_pk_mul_f32 v[186:187], v[186:187], v[6:7] op_sel_hi:[1,0]\n" \
    "v_cvt_pk_bf16_f32 v208, v184, v185\n" \
    "v_cvt_pk_bf16_f32 v209, v186, v187\n" \
    "v_pk_mul_f32 v[188:189], v[188:189], v[6:7] op_sel_hi:[1,0]\n" \
    "v_pk_mul_f32 v[190:191], v[190:191], v[6:7] op_sel_hi:[1,0]\n" \
    "v_cvt_pk_bf16_f32 v210, v188, v189\n" \
    "v_cvt_pk_bf16_f32 v211, v190, v191\n" \
    "buffer_store_dwordx4 v[208:211], v240, s[40:43], s60 offen offset:2048 sc1\n" \
    "v_pk_mul_f32 v[192:193], v[192:193], v[6:7] op_sel_hi:[1,0]\n" \
    "v_pk_mul_f32 v[194:195], v[194:195], v[6:7] op_sel_hi:[1,0]\n" \
    "v_cvt_pk_bf16_f32 v212, v192, v193\n" \
    "v_cvt_pk_bf16_f32 v213, v194, v195\n" \
    "v_pk_mul_f32 v[196:197], v[196:197], v[6:7] op_sel_hi:[1,0]\n" \
    "v_pk_mul_f32 v[198:199], v[198:199], v[6:7] op_sel_hi:[1,0]\n" \
    "v_cvt_pk_bf16_f32 v214, v196, v197\n" \
    "v_cvt_pk_bf16_f32 v215, v198, v199\n" \
    "buffer_store_dwordx4 v[212:215], v240, s[40:43], s60 offen offset:3072 sc1\n" \
    "s_add_u32 s60, s60, 4096\n" \
    "v_mfma_f32_16x16x32_bf16 v[148:151], v[48:51], v[32:35], 0\n" \
    "v_mfma_f32_16x16x32_bf16 v[152:155], v[64:67], v[32:35], 0\n" \
    "v_mfma_f32_16x16x32_bf16 v[148:151], v[52:55], v[36:39], v[148:151]\n" \
    "v_mfma_f32_16x16x32_bf16 v[152:155], v[68:71], v[36:39], v[152:155]\n" \
    "v_mfma_f32_16x16x32_bf16 v[148:151], v[56:59], v[40:43], v[148:151]\n" \
    "v_mfma_f32_16x16x32_bf16 v[152:155], v[72:75], v[40:43], v[152:155]\n" \
    "v_mfma_f32_16x16x32_bf16 v[148:151], v[60:63], v[44:47], v[148:151]\n" \
    "v_mfma_f32_16x16x32_bf16 v[152:155], v[76:79], v[44:47], v[152:155]\n" \
    "v_mfma_f32_16x16x32_bf16 v[144:147], v[112:115], v[32:35], 0\n" \
    "v_mfma_f32_16x16x32_bf16 v[144:147], v[116:119], v[36:39], v[144:147]\n" \
    "v_mfma_f32_16x16x32_bf16 v[144:147], v[120:123], v[40:43], v[144:147]\n" \
    "v_mfma_f32_16x16x32_bf16 v[144:147], v[124:127], v[44:47], v[144:147]\n" \
    "v_cndmask_b32 v152, v152, v4, s[50:51]\n" \
    "v_cndmask_b32 v153, v153, v4, s[52:53]\n" \
    "v_cndmask_b32 v154, v154, v4, s[54:55]\n" \
    "v_cndmask_b32 v155, v155, v4, s[56:57]\n" \
    "s_nop 0\n" \
    "s_nop 0\n" \
    "s_nop 0\n" \
    "s_nop 0\n" \
    "v_cndmask_b32 v144, v144, v4, s[46:47]\n" \
    "v_cndmask_b32 v145, v145, v4, s[46:47]\n" \
    "v_cndmask_b32 v146, v146, v4, s[46:47]\n" \
    "v_cndmask_b32 v147, v147, v4, s[46:47]\n" \
    "v_max3_f32 v7, v144, v145, v146\n" \
    "v_max3_f32 v7, v7, v147, v148\n" \
    "v_max3_f32 v7, v7, v149, v150\n" \
    "v_max3_f32 v7, v7, v151, v152\n" \
    "v_max3_f32 v7, v7, v153, v154\n" \
    "v_max_f32 v8, v7, v155\n" \
    "v_max_f32 v7, v7, v155\n" \
    "s_nop 1\n" \
    "v_permlane32_swap_b32 v7, v8\n" \
    "v_max_f32 v9, v7, v8\n" \
    "v_max_f32 v7, v7, v8\n" \
    "s_nop 1\n" \
    "v_permlane16_swap_b32 v7, v9\n" \
    "v_max_f32 v7, v7, v9\n" \
    "v_mul_f32 v6, 0xbe0293ee, v7\n" \
    "v_pk_fma_f32 v[144:145], v[144:145], s[48:49], v[6:7] op_sel_hi:[1,0,0]\n" \
    "v_pk_fma_f32 v[146:147], v[146:147], s[48:49], v[6:7] op_sel_hi:[1,0,0]\n" \
    "v_pk_fma_f32 v[148:149], v[148:149], s[48:49], v[6:7] op_sel_hi:[1,0,0]\n" \
    "v_pk_fma_f32 v[150:151], v[150:151], s[48:49], v[6:7] op_sel_hi:[1,0,0]\n" \
    "v_pk_fma_f32 v[152:153], v[152:153], s[48:49], v[6:7] op_sel_hi:[1,0,0]\n" \
    "v_pk_fma_f32 v[154:155], v[154:155], s[48:49], v[6:7] op_sel_hi:[1,0,0]\n" \
    "v_exp_f32 v144, v144\n" \
    "v_exp_f32 v145, v145\n" \
    "v_exp_f32 v146, v146\n" \
    "v_exp_f32 v147, v147\n" \
    "v_exp_f32 v148, v148\n" \
    "v_exp_f32 v149, v149\n" \
    "v_exp_f32 v150, v150\n" \
    "v_exp_f32 v151, v151\n" \
    "v_exp_f32 v152, v152\n" \
    "v_exp_f32 v153, v153\n" \
    "v_exp_f32 v154, v154\n" \
    "v_exp_f32 v155, v155\n" \
    "v_cvt_pk_bf16_f32 v160, v144, v145\n" \
    "v_cvt_pk_bf16_f32 v161, v146, v147\n" \
    "v_cvt_pk_bf16_f32 v156, v148, v149\n" \
    "v_cvt_pk_bf16_f32 v157, v150, v151\n" \
    "v_cvt_pk_bf16_f32 v158, v152, v153\n" \
    "v_cvt_pk_bf16_f32 v159, v154, v155\n" \
    "s_nop 1\n" \
    "v_mfma_f32_16x16x32_bf16 v[164:167], v[12:15], v[156:159], 0\n" \
    "v_mfma_f32_16x16x32_bf16 v[168:171], v[80:83], v[156:159], 0\n" \
    "v_mfma_f32_16x16x32_bf16 v[172:175], v[84:87], v[156:159], 0\n" \
    "v_mfma_f32_16x16x32_bf16 v[176:179], v[88:91], v[156:159], 0\n" \
    "v_mfma_f32_16x16x32_bf16 v[180:183], v[92:95], v[156:159], 0\n" \
    "v_mfma_f32_16x16x32_bf16 v[184:187], v[96:99], v[156:159], 0\n" \
    "v_mfma_f32_16x16x32_bf16 v[188:191], v[100:103], v[156:159], 0\n" \
    "v_mfma_f32_16x16x32_bf16 v[192:195], v[104:107], v[156:159], 0\n" \
    "v_mfma_f32_16x16x32_bf16 v[196:199], v[108:111], v[156:159], 0\n" \
    "v_mfma_f32_16x16x16_bf16 v[164:167], v[12:13], v[160:161], v[164:167]\n" \
    "v_mfma_f32_16x16x16_bf16 v[168:171], v[128:129], v[160:161], v[168:171]\n" \
    "v_mfma_f32_16x16x16_bf16 v[172:175], v[130:131], v[160:161], v[172:175]\n" \
    "v_mfma_f32_16x16x16_bf16 v[176:179], v[132:133], v[160:161], v[176:179]\n" \
    "v_mfma_f32_16x16x16_bf16 v[180:183], v[134:135], v[160:161], v[180:183]\n" \
    "v_mfma_f32_16x16x16_bf16 v[184:187], v[136:137], v[160:161], v[184:187]\n" \
    "v_mfma_f32_16x16x16_bf16 v[188:191], v[138:139], v[160:161], v[188:191]\n" \
    "v_mfma_f32_16x16x16_bf16 v[192:195], v[140:141], v[160:161], v[192:195]\n" \
    "v_mfma_f32_16x16x16_bf16 v[196:199], v[142:143], v[160:161], v[196:199]\n" \
    "v_rcp_f32 v6, v164\n" \
    "s_nop 0\n" \
    "v_pk_mul_f32 v[168:169], v[168:169], v[6:7] op_sel_hi:[1,0]\n" \
    "v_pk_mul_f32 v[170:171], v[170:171], v[6:7] op_sel_hi:[1,0]\n" \
    "v_cvt_pk_bf16_f32 v216, v168, v169\n" \
    "v_cvt_pk_bf16_f32 v217, v170, v171\n" \
    "v_pk_mul_f32 v[172:173], v[172:173], v[6:7] op_sel_hi:[1,0]\n" \
    "v_pk_mul_f32 v[174:175], v[174:175], v[6:7] op_sel_hi:[1,0]\n" \
    "v_cvt_pk_bf16_f32 v218, v172, v173\n" \
    "v_cvt_pk_bf16_f32 v219, v174, v175\n" \
    "buffer_store_dwordx4 v[216:219], v240, s[40:43], s60 offen offset:0 sc1\n" \
    "v_pk_mul_f32 v[176:177], v[176:177], v[6:7] op_sel_hi:[1,0]\n" \
    "v_pk_mul_f32 v[178:179], v[178:179], v[6:7] op_sel_hi:[1,0]\n" \
    "v_cvt_pk_bf16_f32 v220, v176, v177\n" \
    "v_cvt_pk_bf16_f32 v221, v178, v179\n" \
    "v_pk_mul_f32 v[180:181], v[180:181], v[6:7] op_sel_hi:[1,0]\n" \
    "v_pk_mul_f32 v[182:183], v[182:183], v[6:7] op_sel_hi:[1,0]\n" \
    "v_cvt_pk_bf16_f32 v222, v180, v181\n" \
    "v_cvt_pk_bf16_f32 v223, v182, v183\n" \
    "buffer_store_dwordx4 v[220:223], v240, s[40:43], s60 offen offset:1024 sc1\n" \
    "v_pk_mul_f32 v[184:185], v[184:185], v[6:7] op_sel_hi:[1,0]\n" \
    "v_pk_mul_f32 v[186:187], v[186:187], v[6:7] op_sel_hi:[1,0]\n" \
    "v_cvt_pk_bf16_f32 v224, v184, v185\n" \
    "v_cvt_pk_bf16_f32 v225, v186, v187\n" \
    "v_pk_mul_f32 v[188:189], v[188:189], v[6:7] op_sel_hi:[1,0]\n" \
    "v_pk_mul_f32 v[190:191], v[190:191], v[6:7] op_sel_hi:[1,0]\n" \
    "v_cvt_pk_bf16_f32 v226, v188, v189\n" \
    "v_cvt_pk_bf16_f32 v227, v190, v191\n" \
    "buffer_store_dwordx4 v[224:227], v240, s[40:43], s60 offen offset:2048 sc1\n" \
    "v_pk_mul_f32 v[192:193], v[192:193], v[6:7] op_sel_hi:[1,0]\n" \
    "v_pk_mul_f32 v[194:195], v[194:195], v[6:7] op_sel_hi:[1,0]\n" \
    "v_cvt_pk_bf16_f32 v228, v192, v193\n" \
    "v_cvt_pk_bf16_f32 v229, v194, v195\n" \
    "v_pk_mul_f32 v[196:197], v[196:197], v[6:7] op_sel_hi:[1,0]\n" \
    "v_pk_mul_f32 v[198:199], v[198:199], v[6:7] op_sel_hi:[1,0]\n" \
    "v_cvt_pk_bf16_f32 v230, v196, v197\n" \
    "v_cvt_pk_bf16_f32 v231, v198, v199\n" \
    "buffer_store_dwordx4 v[228:231], v240, s[40:43], s60 offen offset:3072 sc1\n" \
    "s_add_u32 s60, s60, 4096\n" \
    "s_lshr_b32 s65, s44, 5\n" \
    "s_lshl_b32 s60, s65, 6\n" \
    "s_add_u32 s60, s60, 45056\n" \
    "v_mov_b32 v0, 1\n" \
    "s_waitcnt vmcnt(0)\n" \
    "buffer_atomic_add v0, v241, s[40:43], s60 offen sc0\n" \
    "s_cmp_ge_u32 s63, s69\n" \
    "s_cbranch_scc1 La_last\n" \
    "s_lshl_b32 s60, s62, 13\n" \
    "s_add_u32 s61, s60, 12582912\n" \
    "buffer_load_dwordx4 v[48:51], v240, s[40:43], s61 offen offset:0 sc1\n" \
    "buffer_load_dwordx4 v[52:55], v240, s[40:43], s61 offen offset:1024 sc1\n" \
    "buffer_load_dwordx4 v[56:59], v240, s[40:43], s61 offen offset:2048 sc1\n" \
    "buffer_load_dwordx4 v[60:63], v240, s[40:43], s61 offen offset:3072 sc1\n" \
    "s_add_u32 s61, s60, 12587008\n" \
    "buffer_load_dwordx4 v[64:67], v240, s[40:43], s61 offen offset:0 sc1\n" \
    "buffer_load_dwordx4 v[68:71], v240, s[40:43], s61 offen offset:1024 sc1\n" \
    "buffer_load_dwordx4 v[72:75], v240, s[40:43], s61 offen offset:2048 sc1\n" \
    "buffer_load_dwordx4 v[76:79], v240, s[40:43], s61 offen offset:3072 sc1\n" \
    "s_add_u32 s61, s60, 46137344\n" \
    "buffer_load_dwordx4 v[16:19], v240, s[40:43], s61 offen offset:0 sc1\n" \
    "buffer_load_dwordx4 v[20:23], v240, s[40:43], s61 offen offset:1024 sc1\n" \
    "buffer_load_dwordx4 v[24:27], v240, s[40:43], s61 offen offset:2048 sc1\n" \
    "buffer_load_dwordx4 v[28:31], v240, s[40:43], s61 offen offset:3072 sc1\n" \
    "s_add_u32 s61, s60, 46141440\n" \
    "buffer_load_dwordx4 v[32:35], v240, s[40:43], s61 offen offset:0 sc1\n" \
    "buffer_load_dwordx4 v[36:39], v240, s[40:43], s61 offen offset:1024 sc1\n" \
    "buffer_load_dwordx4 v[40:43], v240, s[40:43], s61 offen offset:2048 sc1\n" \
    "buffer_load_dwordx4 v[44:47], v240, s[40:43], s61 offen offset:3072 sc1\n" \
    "s_add_u32 s61, s60, 29360128\n" \
    "buffer_load_dwordx4 v[80:83], v243, s[40:43], s61 offen offset:0 sc1\n" \
    "buffer_load_dwordx4 v[84:87], v243, s[40:43], s61 offen offset:64 sc1\n" \
    "buffer_load_dwordx4 v[88:91], v243, s[40:43], s61 offen offset:2048 sc1\n" \
    "buffer_load_dwordx4 v[92:95], v243, s[40:43], s61 offen offset:2112 sc1\n" \
    "s_add_u32 s61, s60, 29364224\n" \
    "buffer_load_dwordx4 v[96:99], v243, s[40:43], s61 offen offset:0 sc1\n" \
    "buffer_load_dwordx4 v[100:103], v243, s[40:43], s61 offen offset:64 sc1\n" \
    "buffer_load_dwordx4 v[104:107], v243, s[40:43], s61 offen offset:2048 sc1\n" \
    "buffer_load_dwordx4 v[108:111], v243, s[40:43], s61 offen offset:2112 sc1\n" \
    "s_waitcnt vmcnt(24)\n" \
    "s_branch La_l2\n" \
    "La_last:\n" \
    "s_waitcnt vmcnt(0)\n" \
    "La_l2:\n" \
    "s_lshl_b32 s65, s65, 7\n" \
    "s_add_u32 s65, s65, 49152\n" \
    "v_readfirstlane_b32 s64, v0\n" \
    "s_cmp_lg_u32 s64, 31\n" \
    "s_cbranch_scc1 La_nl\n" \
    "buffer_store_dword v5, v241, s[40:43], s65 offen sc1\n" \
    "La_nl:\n" \
    "s_mov_b32 s44, s63\n" \
    "s_cmp_lt_u32 s44, s69\n" \
    "s_cbranch_scc1 La_loop\n" \
    "s_cmp_lg_u32 s90, 0\n" \
    "s_cbranch_scc1 La_end\n" \
    "v_mov_b32 v0, 1\n" \
    "s_mov_b32 s62, 32768\n" \
    "buffer_store_dword v0, v241, s[40:43], s62 offen sc1\n" \
    "La_end:\n" \
    "s_endpgm\n"
#define FB_ASM \
    "s_nop 4\n" \
    "s_xor_b32 s70, s57, 0\n" \
    "s_lshl_b32 s70, s70, 13\n" \
    "s_xor_b32 s71, s57, 1\n" \
    "s_lshl_b32 s71, s71, 13\n" \
    "s_xor_b32 s72, s57, 2\n" \
    "s_lshl_b32 s72, s72, 13\n" \
    "s_xor_b32 s73, s57, 3\n" \
    "s_lshl_b32 s73, s73, 13\n" \
    "s_xor_b32 s74, s57, 4\n" \
    "s_lshl_b32 s74, s74, 13\n" \
    "s_xor_b32 s75, s57, 5\n" \
    "s_lshl_b32 s75, s75, 13\n" \
    "s_xor_b32 s76, s57, 6\n" \
    "s_lshl_b32 s76, s76, 13\n" \
    "s_xor_b32 s77, s57, 7\n" \
    "s_lshl_b32 s77, s77, 13\n" \
    "s_mul_i32 s68, s56, 36864\n" \
    "s_mov_b32 s67, s42\n" \
    "s_lshl_b32 s61, s70, 5\n" \
    "s_add_u32 m0, s68, 0\n" \
    "s_add_u32 s61, s61, 0\n" \
    "buffer_load_dwordx4 v118, s[52:55], s61 offen offset:0 lds\n" \
    "buffer_load_dwordx4 v119, s[52:55], s61 offen offset:1024 lds\n" \
    "buffer_load_dwordx4 v120, s[52:55], s61 offen offset:2048 lds\n" \
    "s_lshl_b32 s61, s70, 5\n" \
    "s_add_u32 m0, s68, 3072\n" \
    "s_add_u32 s61, s61, 65536\n" \
    "buffer_load_dwordx4 v118, s[52:55], s61 offen offset:0 lds\n" \
    "buffer_load_dwordx4 v119, s[52:55], s61 offen offset:1024 lds\n" \
    "buffer_load_dwordx4 v120, s[52:55], s61 offen offset:2048 lds\n" \
    "s_lshl_b32 s61, s70, 5\n" \
    "s_add_u32 m0, s68, 6144\n" \
    "s_add_u32 s61, s61, 131072\n" \
    "buffer_load_dwordx4 v118, s[52:55], s61 offen offset:0 lds\n" \
    "buffer_load_dwordx4 v119, s[52:55], s61 offen offset:1024 lds\n" \
    "buffer_load_dwordx4 v120, s[52:55], s61 offen offset:2048 lds\n" \
    "s_lshl_b32 s61, s70, 5\n" \
    "s_add_u32 m0, s68, 9216\n" \
    "s_add_u32 s61, s61, 196608\n" \
    "buffer_load_dwordx4 v118, s[52:55], s61 offen offset:0 lds\n" \
    "buffer_load_dwordx4 v119, s[52:55], s61 offen offset:1024 lds\n" \
    "buffer_load_dwordx4 v120, s[52:55], s61 offen offset:2048 lds\n" \
    "s_lshl_b32 s61, s71, 5\n" \
    "s_add_u32 m0, s68, 12288\n" \
    "s_add_u32 s61, s61, 0\n" \
    "buffer_load_dwordx4 v118, s[52:55], s61 offen offset:0 lds\n" \
    "buffer_load_dwordx4 v119, s[52:55], s61 offen offset:1024 lds\n" \
    "buffer_load_dwordx4 v120, s[52:55], s61 offen offset:2048 lds\n" \
    "s_lshl_b32 s61, s71, 5\n" \
    "s_add_u32 m0, s68, 15360\n" \
    "s_add_u32 s61, s61, 65536\n" \
    "buffer_load_dwordx4 v118, s[52:55], s61 offen offset:0 lds\n" \
    "buffer_load_dwordx4 v119, s[52:55], s61 offen offset:1024 lds\n" \
    "buffer_load_dwordx4 v120, s[52:55], s61 offen offset:2048 lds\n" \
    "s_lshl_b32 s61, s71, 5\n" \
    "s_add_u32 m0, s68, 18432\n" \
    "s_add_u32 s61, s61, 131072\n" \
    "buffer_load_dwordx4 v118, s[52:55], s61 offen offset:0 lds\n" \
    "buffer_load_dwordx4 v119, s[52:55], s61 offen offset:1024 lds\n" \
    "buffer_load_dwordx4 v120, s[52:55], s61 offen offset:2048 lds\n" \
    "s_lshl_b32 s61, s71, 5\n" \
    "s_add_u32 m0, s68, 21504\n" \
    "s_add_u32 s61, s61, 196608\n" \
    "buffer_load_dwordx4 v118, s[52:55], s61 offen offset:0 lds\n" \
    "buffer_load_dwordx4 v119, s[52:55], s61 offen offset:1024 lds\n" \
    "buffer_load_dwordx4 v120, s[52:55], s61 offen offset:2048 lds\n" \
    "s_lshl_b32 s61, s72, 5\n" \
    "s_add_u32 m0, s68, 24576\n" \
    "s_add_u32 s61, s61, 0\n" \
    "buffer_load_dwordx4 v118, s[52:55], s61 offen offset:0 lds\n" \
    "buffer_load_dwordx4 v119, s[52:55], s61 offen offset:1024 lds\n" \
    "buffer_load_dwordx4 v120, s[52:55], s61 offen offset:2048 lds\n" \
    "s_lshl_b32 s61, s72, 5\n" \
    "s_add_u32 m0, s68, 27648\n" \
    "s_add_u32 s61, s61, 65536\n" \
    "buffer_load_dwordx4 v118, s[52:55], s61 offen offset:0 lds\n" \
    "buffer_load_dwordx4 v119, s[52:55], s61 offen offset:1024 lds\n" \
    "buffer_load_dwordx4 v120, s[52:55], s61 offen offset:2048 lds\n" \
    "s_lshl_b32 s61, s72, 5\n" \
    "s_add_u32 m0, s68, 30720\n" \
    "s_add_u32 s61, s61, 131072\n" \
    "buffer_load_dwordx4 v118, s[52:55], s61 offen offset:0 lds\n" \
    "buffer_load_dwordx4 v119, s[52:55], s61 offen offset:1024 lds\n" \
    "buffer_load_dwordx4 v120, s[52:55], s61 offen offset:2048 lds\n" \
    "s_lshl_b32 s61, s72, 5\n" \
    "s_add_u32 m0, s68, 33792\n" \
    "s_add_u32 s61, s61, 196608\n" \
    "buffer_load_dwordx4 v118, s[52:55], s61 offen offset:0 lds\n" \
    "buffer_load_dwordx4 v119, s[52:55], s61 offen offset:1024 lds\n" \
    "buffer_load_dwordx4 v120, s[52:55], s61 offen offset:2048 lds\n" \
    "s_waitcnt vmcnt(33)\n" \
    "ds_read_b64_tr_b16 a[0:1], v117 offset:0\n" \
    "ds_read_b64_tr_b16 a[2:3], v117 offset:384\n" \
    "ds_read_b64_tr_b16 a[4:5], v117 offset:32\n" \
    "ds_read_b64_tr_b16 a[6:7], v117 offset:416\n" \
    "ds_read_b64_tr_b16 v[128:129], v117 offset:64\n" \
    "ds_read_b64_tr_b16 v[130:131], v117 offset:448\n" \
    "s_waitcnt lgkmcnt(0)\n" \
    "s_lshl_b32 s61, s73, 5\n" \
    "s_add_u32 m0, s68, 0\n" \
    "s_add_u32 s61, s61, 0\n" \
    "buffer_load_dwordx4 v118, s[52:55], s61 offen offset:0 lds\n" \
    "buffer_load_dwordx4 v119, s[52:55], s61 offen offset:1024 lds\n" \
    "buffer_load_dwordx4 v120, s[52:55], s61 offen offset:2048 lds\n" \
    "s_waitcnt vmcnt(33)\n" \
    "ds_read_b64_tr_b16 a[8:9], v117 offset:3072\n" \
    "ds_read_b64_tr_b16 a[10:11], v117 offset:3456\n" \
    "ds_read_b64_tr_b16 a[12:13], v117 offset:3104\n" \
    "ds_read_b64_tr_b16 a[14:15], v117 offset:3488\n" \
    "ds_read_b64_tr_b16 v[132:133], v117 offset:3136\n" \
    "ds_read_b64_tr_b16 v[134:135], v117 offset:3520\n" \
    "s_waitcnt lgkmcnt(0)\n" \
    "s_lshl_b32 s61, s73, 5\n" \
    "s_add_u32 m0, s68, 3072\n" \
    "s_add_u32 s61, s61, 65536\n" \
    "buffer_load_dwordx4 v118, s[52:55], s61 offen offset:0 lds\n" \
    "buffer_load_dwordx4 v119, s[52:55], s61 offen offset:1024 lds\n" \
    "buffer_load_dwordx4 v120, s[52:55], s61 offen offset:2048 lds\n" \
    "s_waitcnt vmcnt(33)\n" \
    "ds_read_b64_tr_b16 a[16:17], v117 offset:6144\n" \
    "ds_read_b64_tr_b16 a[18:19], v117 offset:6528\n" \
    "ds_read_b64_tr_b16 a[20:21], v117 offset:6176\n" \
    "ds_read_b64_tr_b16 a[22:23], v117 offset:6560\n" \
    "ds_read_b64_tr_b16 v[136:137], v117 offset:6208\n" \
    "ds_read_b64_tr_b16 v[138:139], v117 offset:6592\n" \
    "s_waitcnt lgkmcnt(0)\n" \
    "s_lshl_b32 s61, s73, 5\n" \
    "s_add_u32 m0, s68, 6144\n" \
    "s_add_u32 s61, s61, 131072\n" \
    "buffer_load_dwordx4 v118, s[52:55], s61 offen offset:0 lds\n" \
    "buffer_load_dwordx4 v119, s[52:55], s61 offen offset:1024 lds\n" \
    "buffer_load_dwordx4 v120, s[52:55], s61 offen offset:2048 lds\n" \
    "s_waitcnt vmcnt(33)\n" \
    "ds_read_b64_tr_b16 a[24:25], v117 offset:9216\n" \
    "ds_read_b64_tr_b16 a[26:27], v117 offset:9600\n" \
    "ds_read_b64_tr_b16 a[28:29], v117 offset:9248\n" \
    "ds_read_b64_tr_b16 a[30:31], v117 offset:9632\n" \
    "ds_read_b64_tr_b16 v[140:141], v117 offset:9280\n" \
    "ds_read_b64_tr_b16 v[142:143], v117 offset:9664\n" \
    "s_waitcnt lgkmcnt(0)\n" \
    "s_lshl_b32 s61, s73, 5\n" \
    "s_add_u32 m0, s68, 9216\n" \
    "s_add_u32 s61, s61, 196608\n" \
    "buffer_load_dwordx4 v118, s[52:55], s61 offen offset:0 lds\n" \
    "buffer_load_dwordx4 v119, s[52:55], s61 offen offset:1024 lds\n" \
    "buffer_load_dwordx4 v120, s[52:55], s61 offen offset:2048 lds\n" \
    "s_waitcnt vmcnt(33)\n" \
    "ds_read_b64_tr_b16 a[32:33], v117 offset:12288\n" \
    "ds_read_b64_tr_b16 a[34:35], v117 offset:12672\n" \
    "ds_read_b64_tr_b16 a[36:37], v117 offset:12320\n" \
    "ds_read_b64_tr_b16 a[38:39], v117 offset:12704\n" \
    "ds_read_b64_tr_b16 v[144:145], v117 offset:12352\n" \
    "ds_read_b64_tr_b16 v[146:147], v117 offset:12736\n" \
    "s_waitcnt lgkmcnt(0)\n" \
    "s_lshl_b32 s61, s74, 5\n" \
    "s_add_u32 m0, s68, 12288\n" \
    "s_add_u32 s61, s61, 0\n" \
    "buffer_load_dwordx4 v118, s[52:55], s61 offen offset:0 lds\n" \
    "buffer_load_dwordx4 v119, s[52:55], s61 offen offset:1024 lds\n" \
    "buffer_load_dwordx4 v120, s[52:55], s61 offen offset:2048 lds\n" \
    "s_waitcnt vmcnt(33)\n" \
    "ds_read_b64_tr_b16 a[40:41], v117 offset:15360\n" \
    "ds_read_b64_tr_b16 a[42:43], v117 offset:15744\n" \
    "ds_read_b64_tr_b16 a[44:45], v117 offset:15392\n" \
    "ds_read_b64_tr_b16 a[46:47], v117 offset:15776\n" \
    "ds_read_b64_tr_b16 v[148:149], v117 offset:15424\n" \
    "ds_read_b64_tr_b16 v[150:151], v117 offset:15808\n" \
    "s_waitcnt lgkmcnt(0)\n" \
    "s_lshl_b32 s61, s74, 5\n" \
    "s_add_u32 m0, s68, 15360\n" \
    "s_add_u32 s61, s61, 65536\n" \
    "buffer_load_dwordx4 v118, s[52:55], s61 offen offset:0 lds\n" \
    "buffer_load_dwordx4 v119, s[52:55], s61 offen offset:1024 lds\n" \
    "buffer_load_dwordx4 v120, s[52:55], s61 offen offset:2048 lds\n" \
    "s_waitcnt vmcnt(33)\n" \
    "ds_read_b64_tr_b16 a[48:49], v117 offset:18432\n" \
    "ds_read_b64_tr_b16 a[50:51], v117 offset:18816\n" \
    "ds_read_b64_tr_b16 a[52:53], v117 offset:18464\n" \
    "ds_read_b64_tr_b16 a[54:55], v117 offset:18848\n" \
    "ds_read_b64_tr_b16 v[152:153], v117 offset:18496\n" \
    "ds_read_b64_tr_b16 v[154:155], v117 offset:18880\n" \
    "s_waitcnt lgkmcnt(0)\n" \
    "s_lshl_b32 s61, s74, 5\n" \
    "s_add_u32 m0, s68, 18432\n" \
    "s_add_u32 s61, s61, 131072\n" \
    "buffer_load_dwordx4 v118, s[52:55], s61 offen offset:0 lds\n" \
    "buffer_load_dwordx4 v119, s[52:55], s61 offen offset:1024 lds\n" \
    "buffer_load_dwordx4 v120, s[52:55], s61 offen offset:2048 lds\n" \
    "s_waitcnt vmcnt(33)\n" \
    "ds_read_b64_tr_b16 a[56:57], v117 offset:21504\n" \
    "ds_read_b64_tr_b16 a[58:59], v117 offset:21888\n" \
    "ds_read_b64_tr_b16 a[60:61], v117 offset:21536\n" \
    "ds_read_b64_tr_b16 a[62:63], v117 offset:21920\n" \
    "ds_read_b64_tr_b16 v[156:157], v117 offset:21568\n" \
    "ds_read_b64_tr_b16 v[158:159], v117 offset:21952\n" \
    "s_waitcnt lgkmcnt(0)\n" \
    "s_lshl_b32 s61, s74, 5\n" \
    "s_add_u32 m0, s68, 21504\n" \
    "s_add_u32 s61, s61, 196608\n" \
    "buffer_load_dwordx4 v118, s[52:55], s61 offen offset:0 lds\n" \
    "buffer_load_dwordx4 v119, s[52:55], s61 offen offset:1024 lds\n" \
    "buffer_load_dwordx4 v120, s[52:55], s61 offen offset:2048 lds\n" \
    "s_waitcnt vmcnt(33)\n" \
    "ds_read_b64_tr_b16 a[64:65], v117 offset:24576\n" \
    "ds_read_b64_tr_b16 a[66:67], v117 offset:24960\n" \
    "ds_read_b64_tr_b16 a[68:69], v117 offset:24608\n" \
    "ds_read_b64_tr_b16 a[70:71], v117 offset:24992\n" \
    "ds_read_b64_tr_b16 v[160:161], v117 offset:24640\n" \
    "ds_read_b64_tr_b16 v[162:163], v117 offset:25024\n" \
    "s_waitcnt lgkmcnt(0)\n" \
    "s_lshl_b32 s61, s75, 5\n" \
    "s_add_u32 m0, s68, 24576\n" \
    "s_add_u32 s61, s61, 0\n" \
    "buffer_load_dwordx4 v118, s[52:55], s61 offen offset:0 lds\n" \
    "buffer_load_dwordx4 v119, s[52:55], s61 offen offset:1024 lds\n" \
    "buffer_load_dwordx4 v120, s[52:55], s61 offen offset:2048 lds\n" \
    "s_waitcnt vmcnt(33)\n" \
    "ds_read_b64_tr_b16 a[72:73], v117 offset:27648\n" \
    "ds_read_b64_tr_b16 a[74:75], v117 offset:28032\n" \
    "ds_read_b64_tr_b16 a[76:77], v117 offset:27680\n" \
    "ds_read_b64_tr_b16 a[78:79], v117 offset:28064\n" \
    "ds_read_b64_tr_b16 v[164:165], v117 offset:27712\n" \
    "ds_read_b64_tr_b16 v[166:167], v117 offset:28096\n" \
    "s_waitcnt lgkmcnt(0)\n" \
    "s_lshl_b32 s61, s75, 5\n" \
    "s_add_u32 m0, s68, 27648\n" \
    "s_add_u32 s61, s61, 65536\n" \
    "buffer_load_dwordx4 v118, s[52:55], s61 offen offset:0 lds\n" \
    "buffer_load_dwordx4 v119, s[52:55], s61 offen offset:1024 lds\n" \
    "buffer_load_dwordx4 v120, s[52:55], s61 offen offset:2048 lds\n" \
    "s_waitcnt vmcnt(33)\n" \
    "ds_read_b64_tr_b16 a[80:81], v117 offset:30720\n" \
    "ds_read_b64_tr_b16 a[82:83], v117 offset:31104\n" \
    "ds_read_b64_tr_b16 a[84:85], v117 offset:30752\n" \
    "ds_read_b64_tr_b16 a[86:87], v117 offset:31136\n" \
    "ds_read_b64_tr_b16 v[168:169], v117 offset:30784\n" \
    "ds_read_b64_tr_b16 v[170:171], v117 offset:31168\n" \
    "s_waitcnt lgkmcnt(0)\n" \
    "s_lshl_b32 s61, s75, 5\n" \
    "s_add_u32 m0, s68, 30720\n" \
    "s_add_u32 s61, s61, 131072\n" \
    "buffer_load_dwordx4 v118, s[52:55], s61 offen offset:0 lds\n" \
    "buffer_load_dwordx4 v119, s[52:55], s61 offen offset:1024 lds\n" \
    "buffer_load_dwordx4 v120, s[52:55], s61 offen offset:2048 lds\n" \
    "s_waitcnt vmcnt(33)\n" \
    "ds_read_b64_tr_b16 a[88:89], v117 offset:33792\n" \
    "ds_read_b64_tr_b16 a[90:91], v117 offset:34176\n" \
    "ds_read_b64_tr_b16 a[92:93], v117 offset:33824\n" \
    "ds_read_b64_tr_b16 a[94:95], v117 offset:34208\n" \
    "ds_read_b64_tr_b16 v[172:173], v117 offset:33856\n" \
    "ds_read_b64_tr_b16 v[174:175], v117 offset:34240\n" \
    "s_waitcnt lgkmcnt(0)\n" \
    "s_lshl_b32 s61, s75, 5\n" \
    "s_add_u32 m0, s68, 33792\n" \
    "s_add_u32 s61, s61, 196608\n" \
    "buffer_load_dwordx4 v118, s[52:55], s61 offen offset:0 lds\n" \
    "buffer_load_dwordx4 v119, s[52:55], s61 offen offset:1024 lds\n" \
    "buffer_load_dwordx4 v120, s[52:55], s61 offen offset:2048 lds\n" \
    "s_waitcnt vmcnt(33)\n" \
    "ds_read_b64_tr_b16 a[96:97], v117 offset:0\n" \
    "ds_read_b64_tr_b16 a[98:99], v117 offset:384\n" \
    "ds_read_b64_tr_b16 a[100:101], v117 offset:32\n" \
    "ds_read_b64_tr_b16 a[102:103], v117 offset:416\n" \
    "ds_read_b64_tr_b16 v[176:177], v117 offset:64\n" \
    "ds_read_b64_tr_b16 v[178:179], v117 offset:448\n" \
    "s_waitcnt lgkmcnt(0)\n" \
    "s_lshl_b32 s61, s76, 5\n" \
    "s_add_u32 m0, s68, 0\n" \
    "s_add_u32 s61, s61, 0\n" \
    "buffer_load_dwordx4 v118, s[52:55], s61 offen offset:0 lds\n" \
    "buffer_load_dwordx4 v119, s[52:55], s61 offen offset:1024 lds\n" \
    "buffer_load_dwordx4 v120, s[52:55], s61 offen offset:2048 lds\n" \
    "s_waitcnt vmcnt(33)\n" \
    "ds_read_b64_tr_b16 a[104:105], v117 offset:3072\n" \
    "ds_read_b64_tr_b16 a[106:107], v117 offset:3456\n" \
    "ds_read_b64_tr_b16 a[108:109], v117 offset:3104\n" \
    "ds_read_b64_tr_b16 a[110:111], v117 offset:3488\n" \
    "ds_read_b64_tr_b16 v[180:181], v117 offset:3136\n" \
    "ds_read_b64_tr_b16 v[182:183], v117 offset:3520\n" \
    "s_waitcnt lgkmcnt(0)\n" \
    "s_lshl_b32 s61, s76, 5\n" \
    "s_add_u32 m0, s68, 3072\n" \
    "s_add_u32 s61, s61, 65536\n" \
    "buffer_load_dwordx4 v118, s[52:55], s61 offen offset:0 lds\n" \
    "buffer_load_dwordx4 v119, s[52:55], s61 offen offset:1024 lds\n" \
    "buffer_load_dwordx4 v120, s[52:55], s61 offen offset:2048 lds\n" \
    "s_waitcnt vmcnt(33)\n" \
    "ds_read_b64_tr_b16 a[112:113], v117 offset:6144\n" \
    "ds_read_b64_tr_b16 a[114:115], v117 offset:6528\n" \
    "ds_read_b64_tr_b16 a[116:117], v117 offset:6176\n" \
    "ds_read_b64_tr_b16 a[118:119], v117 offset:6560\n" \
    "ds_read_b64_tr_b16 v[184:185], v117 offset:6208\n" \
    "ds_read_b64_tr_b16 v[186:187], v117 offset:6592\n" \
    "s_waitcnt lgkmcnt(0)\n" \
    "s_lshl_b32 s61, s76, 5\n" \
    "s_add_u32 m0, s68, 6144\n" \
    "s_add_u32 s61, s61, 131072\n" \
    "buffer_load_dwordx4 v118, s[52:55], s61 offen offset:0 lds\n" \
    "buffer_load_dwordx4 v119, s[52:55], s61 offen offset:1024 lds\n" \
    "buffer_load_dwordx4 v120, s[52:55], s61 offen offset:2048 lds\n" \
    "s_waitcnt vmcnt(33)\n" \
    "ds_read_b64_tr_b16 a[120:121], v117 offset:9216\n" \
    "ds_read_b64_tr_b16 a[122:123], v117 offset:9600\n" \
    "ds_read_b64_tr_b16 a[124:125], v117 offset:9248\n" \
    "ds_read_b64_tr_b16 a[126:127], v117 offset:9632\n" \
    "ds_read_b64_tr_b16 v[188:189], v117 offset:9280\n" \
    "ds_read_b64_tr_b16 v[190:191], v117 offset:9664\n" \
    "s_waitcnt lgkmcnt(0)\n" \
    "s_lshl_b32 s61, s76, 5\n" \
    "s_add_u32 m0, s68, 9216\n" \
    "s_add_u32 s61, s61, 196608\n" \
    "buffer_load_dwordx4 v118, s[52:55], s61 offen offset:0 lds\n" \
    "buffer_load_dwordx4 v119, s[52:55], s61 offen offset:1024 lds\n" \
    "buffer_load_dwordx4 v120, s[52:55], s61 offen offset:2048 lds\n" \
    "s_waitcnt vmcnt(33)\n" \
    "ds_read_b64_tr_b16 a[128:129], v117 offset:12288\n" \
    "ds_read_b64_tr_b16 a[130:131], v117 offset:12672\n" \
    "ds_read_b64_tr_b16 a[132:133], v117 offset:12320\n" \
    "ds_read_b64_tr_b16 a[134:135], v117 offset:12704\n" \
    "ds_read_b64_tr_b16 v[192:193], v117 offset:12352\n" \
    "ds_read_b64_tr_b16 v[194:195], v117 offset:12736\n" \
    "s_waitcnt lgkmcnt(0)\n" \
    "s_lshl_b32 s61, s77, 5\n" \
    "s_add_u32 m0, s68, 12288\n" \
    "s_add_u32 s61, s61, 0\n" \
    "buffer_load_dwordx4 v118, s[52:55], s61 offen offset:0 lds\n" \
    "buffer_load_dwordx4 v119, s[52:55], s61 offen offset:1024 lds\n" \
    "buffer_load_dwordx4 v120, s[52:55], s61 offen offset:2048 lds\n" \
    "s_waitcnt vmcnt(33)\n" \
    "ds_read_b64_tr_b16 a[136:137], v117 offset:15360\n" \
    "ds_read_b64_tr_b16 a[138:139], v117 offset:15744\n" \
    "ds_read_b64_tr_b16 a[140:141], v117 offset:15392\n" \
    "ds_read_b64_tr_b16 a[142:143], v117 offset:15776\n" \
    "ds_read_b64_tr_b16 v[196:197], v117 offset:15424\n" \
    "ds_read_b64_tr_b16 v[198:199], v117 offset:15808\n" \
    "s_waitcnt lgkmcnt(0)\n" \
    "s_lshl_b32 s61, s77, 5\n" \
    "s_add_u32 m0, s68, 15360\n" \
    "s_add_u32 s61, s61, 65536\n" \
    "buffer_load_dwordx4 v118, s[52:55], s61 offen offset:0 lds\n" \
    "buffer_load_dwordx4 v119, s[52:55], s61 offen offset:1024 lds\n" \
    "buffer_load_dwordx4 v120, s[52:55], s61 offen offset:2048 lds\n" \
    "s_waitcnt vmcnt(33)\n" \
    "ds_read_b64_tr_b16 a[144:145], v117 offset:18432\n" \
    "ds_read_b64_tr_b16 a[146:147], v117 offset:18816\n" \
    "ds_read_b64_tr_b16 a[148:149], v117 offset:18464\n" \
    "ds_read_b64_tr_b16 a[150:151], v117 offset:18848\n" \
    "ds_read_b64_tr_b16 v[200:201], v117 offset:18496\n" \
    "ds_read_b64_tr_b16 v[202:203], v117 offset:18880\n" \
    "s_waitcnt lgkmcnt(0)\n" \
    "s_lshl_b32 s61, s77, 5\n" \
    "s_add_u32 m0, s68, 18432\n" \
    "s_add_u32 s61, s61, 131072\n" \
    "buffer_load_dwordx4 v118, s[52:55], s61 offen offset:0 lds\n" \
    "buffer_load_dwordx4 v119, s[52:55], s61 offen offset:1024 lds\n" \
    "buffer_load_dwordx4 v120, s[52:55], s61 offen offset:2048 lds\n" \
    "s_waitcnt vmcnt(33)\n" \
    "ds_read_b64_tr_b16 a[152:153], v117 offset:21504\n" \
    "ds_read_b64_tr_b16 a[154:155], v117 offset:21888\n" \
    "ds_read_b64_tr_b16 a[156:157], v117 offset:21536\n" \
    "ds_read_b64_tr_b16 a[158:159], v117 offset:21920\n" \
    "ds_read_b64_tr_b16 v[204:205], v117 offset:21568\n" \
    "ds_read_b64_tr_b16 v[206:207], v117 offset:21952\n" \
    "s_waitcnt lgkmcnt(0)\n" \
    "s_lshl_b32 s61, s77, 5\n" \
    "s_add_u32 m0, s68, 21504\n" \
    "s_add_u32 s61, s61, 196608\n" \
    "buffer_load_dwordx4 v118, s[52:55], s61 offen offset:0 lds\n" \
    "buffer_load_dwordx4 v119, s[52:55], s61 offen offset:1024 lds\n" \
    "buffer_load_dwordx4 v120, s[52:55], s61 offen offset:2048 lds\n" \
    "s_waitcnt vmcnt(33)\n" \
    "ds_read_b64_tr_b16 a[160:161], v117 offset:24576\n" \
    "ds_read_b64_tr_b16 a[162:163], v117 offset:24960\n" \
    "ds_read_b64_tr_b16 a[164:165], v117 offset:24608\n" \
    "ds_read_b64_tr_b16 a[166:167], v117 offset:24992\n" \
    "ds_read_b64_tr_b16 v[208:209], v117 offset:24640\n" \
    "ds_read_b64_tr_b16 v[210:211], v117 offset:25024\n" \
    "s_waitcnt vmcnt(30)\n" \
    "ds_read_b64_tr_b16 a[168:169], v117 offset:27648\n" \
    "ds_read_b64_tr_b16 a[170:171], v117 offset:28032\n" \
    "ds_read_b64_tr_b16 a[172:173], v117 offset:27680\n" \
    "ds_read_b64_tr_b16 a[174:175], v117 offset:28064\n" \
    "ds_read_b64_tr_b16 v[212:213], v117 offset:27712\n" \
    "ds_read_b64_tr_b16 v[214:215], v117 offset:28096\n" \
    "s_waitcnt vmcnt(27)\n" \
    "ds_read_b64_tr_b16 a[176:177], v117 offset:30720\n" \
    "ds_read_b64_tr_b16 a[178:179], v117 offset:31104\n" \
    "ds_read_b64_tr_b16 a[180:181], v117 offset:30752\n" \
    "ds_read_b64_tr_b16 a[182:183], v117 offset:31136\n" \
    "ds_read_b64_tr_b16 v[216:217], v117 offset:30784\n" \
    "ds_read_b64_tr_b16 v[218:219], v117 offset:31168\n" \
    "s_waitcnt vmcnt(24)\n" \
    "ds_read_b64_tr_b16 a[184:185], v117 offset:33792\n" \
    "ds_read_b64_tr_b16 a[186:187], v117 offset:34176\n" \
    "ds_read_b64_tr_b16 a[188:189], v117 offset:33824\n" \
    "ds_read_b64_tr_b16 a[190:191], v117 offset:34208\n" \
    "ds_read_b64_tr_b16 v[220:221], v117 offset:33856\n" \
    "ds_read_b64_tr_b16 v[222:223], v117 offset:34240\n" \
    "s_waitcnt vmcnt(21)\n" \
    "ds_read_b64_tr_b16 a[192:193], v117 offset:0\n" \
    "ds_read_b64_tr_b16 a[194:195], v117 offset:384\n" \
    "ds_read_b64_tr_b16 a[196:197], v117 offset:32\n" \
    "ds_read_b64_tr_b16 a[198:199], v117 offset:416\n" \
    "ds_read_b64_tr_b16 v[224:225], v117 offset:64\n" \
    "ds_read_b64_tr_b16 v[226:227], v117 offset:448\n" \
    "s_waitcnt vmcnt(18)\n" \
    "ds_read_b64_tr_b16 a[200:201], v117 offset:3072\n" \
    "ds_read_b64_tr_b16 a[202:203], v117 offset:3456\n" \
    "ds_read_b64_tr_b16 a[204:205], v117 offset:3104\n" \
    "ds_read_b64_tr_b16 a[206:207], v117 offset:3488\n" \
    "ds_read_b64_tr_b16 v[228:229], v117 offset:3136\n" \
    "ds_read_b64_tr_b16 v[230:231], v117 offset:3520\n" \
    "s_waitcnt vmcnt(15)\n" \
    "ds_read_b64_tr_b16 a[208:209], v117 offset:6144\n" \
    "ds_read_b64_tr_b16 a[210:211], v117 offset:6528\n" \
    "ds_read_b64_tr_b16 a[212:213], v117 offset:6176\n" \
    "ds_read_b64_tr_b16 a[214:215], v117 offset:6560\n" \
    "ds_read_b64_tr_b16 v[232:233], v117 offset:6208\n" \
    "ds_read_b64_tr_b16 v[234:235], v117 offset:6592\n" \
    "s_waitcnt vmcnt(12)\n" \
    "ds_read_b64_tr_b16 a[216:217], v117 offset:9216\n" \
    "ds_read_b64_tr_b16 a[218:219], v117 offset:9600\n" \
    "ds_read_b64_tr_b16 a[220:221], v117 offset:9248\n" \
    "ds_read_b64_tr_b16 a[222:223], v117 offset:9632\n" \
    "ds_read_b64_tr_b16 v[236:237], v117 offset:9280\n" \
    "ds_read_b64_tr_b16 v[238:239], v117 offset:9664\n" \
    "s_waitcnt vmcnt(9)\n" \
    "ds_read_b64_tr_b16 a[224:225], v117 offset:12288\n" \
    "ds_read_b64_tr_b16 a[226:227], v117 offset:12672\n" \
    "ds_read_b64_tr_b16 a[228:229], v117 offset:12320\n" \
    "ds_read_b64_tr_b16 a[230:231], v117 offset:12704\n" \
    "ds_read_b64_tr_b16 v[240:241], v117 offset:12352\n" \
    "ds_read_b64_tr_b16 v[242:243], v117 offset:12736\n" \
    "s_waitcnt vmcnt(6)\n" \
    "ds_read_b64_tr_b16 a[232:233], v117 offset:15360\n" \
    "ds_read_b64_tr_b16 a[234:235], v117 offset:15744\n" \
    "ds_read_b64_tr_b16 a[236:237], v117 offset:15392\n" \
    "ds_read_b64_tr_b16 a[238:239], v117 offset:15776\n" \
    "ds_read_b64_tr_b16 v[244:245], v117 offset:15424\n" \
    "ds_read_b64_tr_b16 v[246:247], v117 offset:15808\n" \
    "s_waitcnt vmcnt(3)\n" \
    "ds_read_b64_tr_b16 a[240:241], v117 offset:18432\n" \
    "ds_read_b64_tr_b16 a[242:243], v117 offset:18816\n" \
    "ds_read_b64_tr_b16 a[244:245], v117 offset:18464\n" \
    "ds_read_b64_tr_b16 a[246:247], v117 offset:18848\n" \
    "ds_read_b64_tr_b16 v[248:249], v117 offset:18496\n" \
    "ds_read_b64_tr_b16 v[250:251], v117 offset:18880\n" \
    "s_waitcnt vmcnt(0)\n" \
    "ds_read_b64_tr_b16 a[248:249], v117 offset:21504\n" \
    "ds_read_b64_tr_b16 a[250:251], v117 offset:21888\n" \
    "ds_read_b64_tr_b16 a[252:253], v117 offset:21536\n" \
    "ds_read_b64_tr_b16 a[254:255], v117 offset:21920\n" \
    "ds_read_b64_tr_b16 v[252:253], v117 offset:21568\n" \
    "ds_read_b64_tr_b16 v[254:255], v117 offset:21952\n" \
    "s_waitcnt lgkmcnt(0)\n" \
    "s_barrier\n" \
    "Lb_wait:\n" \
    "s_lshl_b32 s63, s58, 7\n" \
    "s_add_u32 s63, s63, 49152\n" \
    "s_branch Lb_c_i\n" \
    "Lb_c:\n" \
    "s_sleep 2\n" \
    "Lb_c_i:\n" \
    "s_cmp_eq_u32 s90, 0\n" \
    "s_cbranch_scc1 Lb_c_x\n" \
    "s_sub_u32 s90, s90, 1\n" \
    "buffer_load_dword v88, off, s[40:43], s63 sc1\n" \
    "s_waitcnt vmcnt(0)\n" \
    "v_cmp_gt_u32 vcc, 1, v88\n" \
    "s_cbranch_vccnz Lb_c\n" \
    "Lb_c_x:\n" \
    "s_waitcnt vmcnt(0)\n" \
    "s_lshl_b32 s65, s58, 18\n" \
    "s_lshl_b32 s61, s56, 16\n" \
    "s_add_u32 s65, s65, s61\n" \
    "s_add_u32 s65, s65, 62914560\n" \
    "s_add_u32 s62, s65, s70\n" \
    "buffer_load_dwordx4 v[24:27], v112, s[40:43], s62 offen offset:0 sc1\n" \
    "s_nop 11\n" \
    "buffer_load_dwordx4 v[28:31], v112, s[40:43], s62 offen offset:1024 sc1\n" \
    "s_nop 11\n" \
    "buffer_load_dwordx4 v[32:35], v112, s[40:43], s62 offen offset:2048 sc1\n" \
    "s_nop 11\n" \
    "buffer_load_dwordx4 v[36:39], v112, s[40:43], s62 offen offset:3072 sc1\n" \
    "s_nop 11\n" \
    "s_add_u32 s62, s62, 4096\n" \
    "buffer_load_dwordx4 v[40:43], v112, s[40:43], s62 offen offset:0 sc1\n" \
    "s_nop 11\n" \
    "buffer_load_dwordx4 v[44:47], v112, s[40:43], s62 offen offset:1024 sc1\n" \
    "s_nop 11\n" \
    "buffer_load_dwordx4 v[48:51], v112, s[40:43], s62 offen offset:2048 sc1\n" \
    "buffer_load_dwordx4 v[52:55], v112, s[40:43], s62 offen offset:3072 sc1\n" \
    "s_add_u32 s62, s65, s71\n" \
    "buffer_load_dwordx4 v[56:59], v112, s[40:43], s62 offen offset:0 sc1\n" \
    "buffer_load_dwordx4 v[60:63], v112, s[40:43], s62 offen offset:1024 sc1\n" \
    "buffer_load_dwordx4 v[64:67], v112, s[40:43], s62 offen offset:2048 sc1\n" \
    "buffer_load_dwordx4 v[68:71], v112, s[40:43], s62 offen offset:3072 sc1\n" \
    "s_add_u32 s62, s62, 4096\n" \
    "buffer_load_dwordx4 v[72:75], v112, s[40:43], s62 offen offset:0 sc1\n" \
    "buffer_load_dwordx4 v[76:79], v112, s[40:43], s62 offen offset:1024 sc1\n" \
    "buffer_load_dwordx4 v[80:83], v112, s[40:43], s62 offen offset:2048 sc1\n" \
    "buffer_load_dwordx4 v[84:87], v112, s[40:43], s62 offen offset:3072 sc1\n" \
    "Lb_seg:\n" \
    "s_add_u32 s61, s58, 1\n" \
    "s_add_u32 s63, s58, s60\n" \
    "s_cmp_lt_u32 s58, s59\n" \
    "s_cselect_b32 s64, s61, s63\n" \
    "s_min_u32 s63, s64, 63\n" \
    "s_lshl_b32 s66, s63, 18\n" \
    "s_lshl_b32 s61, s56, 16\n" \
    "s_add_u32 s66, s66, s61\n" \
    "s_add_u32 s66, s66, 62914560\n" \
    "s_lshl_b32 s63, s63, 7\n" \
    "s_add_u32 s63, s63, 49152\n" \
    "s_waitcnt vmcnt(15)\n" \
    "v_mfma_f32_16x16x32_bf16 v[0:3], a[0:3], v[24:27], 0\n" \
    "v_mfma_f32_16x16x32_bf16 v[4:7], a[4:7], v[24:27], 0\n" \
    "v_mfma_f32_16x16x32_bf16 v[8:11], v[128:131], v[24:27], 0\n" \
    "s_add_u32 s62, s65, s72\n" \
    "buffer_load_dwordx4 v[24:27], v112, s[40:43], s62 offen offset:0 sc1\n" \
    "s_waitcnt vmcnt(15)\n" \
    "v_mfma_f32_16x16x32_bf16 v[0:3], a[8:11], v[28:31], v[0:3]\n" \
    "v_mfma_f32_16x16x32_bf16 v[4:7], a[12:15], v[28:31], v[4:7]\n" \
    "v_mfma_f32_16x16x32_bf16 v[8:11], v[132:135], v[28:31], v[8:11]\n" \
    "buffer_load_dwordx4 v[28:31], v112, s[40:43], s62 offen offset:1024 sc1\n" \
    "s_waitcnt vmcnt(15)\n" \
    "v_mfma_f32_16x16x32_bf16 v[0:3], a[16:19], v[32:35], v[0:3]\n" \
    "v_mfma_f32_16x16x32_bf16 v[4:7], a[20:23], v[32:35], v[4:7]\n" \
    "v_mfma_f32_16x16x32_bf16 v[8:11], v[136:139], v[32:35], v[8:11]\n" \
    "buffer_load_dwordx4 v[32:35], v112, s[40:43], s62 offen offset:2048 sc1\n" \
    "s_waitcnt vmcnt(15)\n" \
    "v_mfma_f32_16x16x32_bf16 v[0:3], a[24:27], v[36:39], v[0:3]\n" \
    "v_mfma_f32_16x16x32_bf16 v[4:7], a[28:31], v[36:39], v[4:7]\n" \
    "v_mfma_f32_16x16x32_bf16 v[8:11], v[140:143], v[36:39], v[8:11]\n" \
    "buffer_load_dwordx4 v[36:39], v112, s[40:43], s62 offen offset:3072 sc1\n" \
    "s_waitcnt vmcnt(15)\n" \
    "v_mfma_f32_16x16x32_bf16 v[12:15], a[0:3], v[40:43], 0\n" \
    "v_mfma_f32_16x16x32_bf16 v[16:19], a[4:7], v[40:43], 0\n" \
    "v_mfma_f32_16x16x32_bf16 v[20:23], v[128:131], v[40:43], 0\n" \
    "s_add_u32 s62, s62, 4096\n" \
    "buffer_load_dwordx4 v[40:43], v112, s[40:43], s62 offen offset:0 sc1\n" \
    "s_waitcnt vmcnt(15)\n" \
    "v_mfma_f32_16x16x32_bf16 v[12:15], a[8:11], v[44:47], v[12:15]\n" \
    "v_mfma_f32_16x16x32_bf16 v[16:19], a[12:15], v[44:47], v[16:19]\n" \
    "v_mfma_f32_16x16x32_bf16 v[20:23], v[132:135], v[44:47], v[20:23]\n" \
    "buffer_load_dwordx4 v[44:47], v112, s[40:43], s62 offen offset:1024 sc1\n" \
    "s_waitcnt vmcnt(15)\n" \
    "v_mfma_f32_16x16x32_bf16 v[12:15], a[16:19], v[48:51], v[12:15]\n" \
    "v_mfma_f32_16x16x32_bf16 v[16:19], a[20:23], v[48:51], v[16:19]\n" \
    "v_mfma_f32_16x16x32_bf16 v[20:23], v[136:139], v[48:51], v[20:23]\n" \
    "buffer_load_dwordx4 v[48:51], v112, s[40:43], s62 offen offset:2048 sc1\n" \
    "s_waitcnt vmcnt(15)\n" \
    "v_mfma_f32_16x16x32_bf16 v[12:15], a[24:27], v[52:55], v[12:15]\n" \
    "v_mfma_f32_16x16x32_bf16 v[16:19], a[28:31], v[52:55], v[16:19]\n" \
    "v_mfma_f32_16x16x32_bf16 v[20:23], v[140:143], v[52:55], v[20:23]\n" \
    "buffer_load_dwordx4 v[52:55], v112, s[40:43], s62 offen offset:3072 sc1\n" \
    "s_waitcnt vmcnt(15)\n" \
    "v_mfma_f32_16x16x32_bf16 v[0:3], a[32:35], v[56:59], v[0:3]\n" \
    "v_mfma_f32_16x16x32_bf16 v[4:7], a[36:39], v[56:59], v[4:7]\n" \
    "v_mfma_f32_16x16x32_bf16 v[8:11], v[144:147], v[56:59], v[8:11]\n" \
    "s_add_u32 s62, s65, s73\n" \
    "buffer_load_dwordx4 v[56:59], v112, s[40:43], s62 offen offset:0 sc1\n" \
    "s_waitcnt vmcnt(15)\n" \
    "v_mfma_f32_16x16x32_bf16 v[0:3], a[40:43], v[60:63], v[0:3]\n" \
    "v_mfma_f32_16x16x32_bf16 v[4:7], a[44:47], v[60:63], v[4:7]\n" \
    "v_mfma_f32_16x16x32_bf16 v[8:11], v[148:151], v[60:63], v[8:11]\n" \
    "buffer_load_dwordx4 v[60:63], v112, s[40:43], s62 offen offset:1024 sc1\n" \
    "s_waitcnt vmcnt(15)\n" \
    "v_mfma_f32_16x16x32_bf16 v[0:3], a[48:51], v[64:67], v[0:3]\n" \
    "v_mfma_f32_16x16x32_bf16 v[4:7], a[52:55], v[64:67], v[4:7]\n" \
    "v_mfma_f32_16x16x32_bf16 v[8:11], v[152:155], v[64:67], v[8:11]\n" \
    "buffer_load_dwordx4 v[64:67], v112, s[40:43], s62 offen offset:2048 sc1\n" \
    "s_waitcnt vmcnt(15)\n" \
    "v_mfma_f32_16x16x32_bf16 v[0:3], a[56:59], v[68:71], v[0:3]\n" \
    "v_mfma_f32_16x16x32_bf16 v[4:7], a[60:63], v[68:71], v[4:7]\n" \
    "v_mfma_f32_16x16x32_bf16 v[8:11], v[156:159], v[68:71], v[8:11]\n" \
    "buffer_load_dwordx4 v[68:71], v112, s[40:43], s62 offen offset:3072 sc1\n" \
    "s_waitcnt vmcnt(15)\n" \
    "v_mfma_f32_16x16x32_bf16 v[12:15], a[32:35], v[72:75], v[12:15]\n" \
    "v_mfma_f32_16x16x32_bf16 v[16:19], a[36:39], v[72:75], v[16:19]\n" \
    "v_mfma_f32_16x16x32_bf16 v[20:23], v[144:147], v[72:75], v[20:23]\n" \
    "s_add_u32 s62, s62, 4096\n" \
    "buffer_load_dwordx4 v[72:75], v112, s[40:43], s62 offen offset:0 sc1\n" \
    "s_waitcnt vmcnt(15)\n" \
    "v_mfma_f32_16x16x32_bf16 v[12:15], a[40:43], v[76:79], v[12:15]\n" \
    "v_mfma_f32_16x16x32_bf16 v[16:19], a[44:47], v[76:79], v[16:19]\n" \
    "v_mfma_f32_16x16x32_bf16 v[20:23], v[148:151], v[76:79], v[20:23]\n" \
    "buffer_load_dwordx4 v[76:79], v112, s[40:43], s62 offen offset:1024 sc1\n" \
    "s_waitcnt vmcnt(15)\n" \
    "v_mfma_f32_16x16x32_bf16 v[12:15], a[48:51], v[80:83], v[12:15]\n" \
    "v_mfma_f32_16x16x32_bf16 v[16:19], a[52:55], v[80:83], v[16:19]\n" \
    "v_mfma_f32_16x16x32_bf16 v[20:23], v[152:155], v[80:83], v[20:23]\n" \
    "buffer_load_dwordx4 v[80:83], v112, s[40:43], s62 offen offset:2048 sc1\n" \
    "s_waitcnt vmcnt(15)\n" \
    "v_mfma_f32_16x16x32_bf16 v[12:15], a[56:59], v[84:87], v[12:15]\n" \
    "v_mfma_f32_16x16x32_bf16 v[16:19], a[60:63], v[84:87], v[16:19]\n" \
    "v_mfma_f32_16x16x32_bf16 v[20:23], v[156:159], v[84:87], v[20:23]\n" \
    "buffer_load_dwordx4 v[84:87], v112, s[40:43], s62 offen offset:3072 sc1\n" \
    "s_waitcnt vmcnt(15)\n" \
    "v_mfma_f32_16x16x32_bf16 v[0:3], a[64:67], v[24:27], v[0:3]\n" \
    "v_mfma_f32_16x16x32_bf16 v[4:7], a[68:71], v[24:27], v[4:7]\n" \
    "v_mfma_f32_16x16x32_bf16 v[8:11], v[160:163], v[24:27], v[8:11]\n" \
    "s_add_u32 s62, s65, s74\n" \
    "buffer_load_dwordx4 v[24:27], v112, s[40:43], s62 offen offset:0 sc1\n" \
    "s_waitcnt vmcnt(15)\n" \
    "v_mfma_f32_16x16x32_bf16 v[0:3], a[72:75], v[28:31], v[0:3]\n" \
    "v_mfma_f32_16x16x32_bf16 v[4:7], a[76:79], v[28:31], v[4:7]\n" \
    "v_mfma_f32_16x16x32_bf16 v[8:11], v[164:167], v[28:31], v[8:11]\n" \
    "buffer_load_dwordx4 v[28:31], v112, s[40:43], s62 offen offset:1024 sc1\n" \
    "s_waitcnt vmcnt(15)\n" \
    "v_mfma_f32_16x16x32_bf16 v[0:3], a[80:83], v[32:35], v[0:3]\n" \
    "v_mfma_f32_16x16x32_bf16 v[4:7], a[84:87], v[32:35], v[4:7]\n" \
    "v_mfma_f32_16x16x32_bf16 v[8:11], v[168:171], v[32:35], v[8:11]\n" \
    "buffer_load_dwordx4 v[32:35], v112, s[40:43], s62 offen offset:2048 sc1\n" \
    "s_waitcnt vmcnt(15)\n" \
    "v_mfma_f32_16x16x32_bf16 v[0:3], a[88:91], v[36:39], v[0:3]\n" \
    "v_mfma_f32_16x16x32_bf16 v[4:7], a[92:95], v[36:39], v[4:7]\n" \
    "v_mfma_f32_16x16x32_bf16 v[8:11], v[172:175], v[36:39], v[8:11]\n" \
    "buffer_load_dwordx4 v[36:39], v112, s[40:43], s62 offen offset:3072 sc1\n" \
    "s_waitcnt vmcnt(15)\n" \
    "v_mfma_f32_16x16x32_bf16 v[12:15], a[64:67], v[40:43], v[12:15]\n" \
    "v_mfma_f32_16x16x32_bf16 v[16:19], a[68:71], v[40:43], v[16:19]\n" \
    "v_mfma_f32_16x16x32_bf16 v[20:23], v[160:163], v[40:43], v[20:23]\n" \
    "s_add_u32 s62, s62, 4096\n" \
    "buffer_load_dwordx4 v[40:43], v112, s[40:43], s62 offen offset:0 sc1\n" \
    "s_waitcnt vmcnt(15)\n" \
    "v_mfma_f32_16x16x32_bf16 v[12:15], a[72:75], v[44:47], v[12:15]\n" \
    "v_mfma_f32_16x16x32_bf16 v[16:19], a[76:79], v[44:47], v[16:19]\n" \
    "v_mfma_f32_16x16x32_bf16 v[20:23], v[164:167], v[44:47], v[20:23]\n" \
    "buffer_load_dwordx4 v[44:47], v112, s[40:43], s62 offen offset:1024 sc1\n" \
    "s_waitcnt vmcnt(15)\n" \
    "v_mfma_f32_16x16x32_bf16 v[12:15], a[80:83], v[48:51], v[12:15]\n" \
    "v_mfma_f32_16x16x32_bf16 v[16:19], a[84:87], v[48:51], v[16:19]\n" \
    "v_mfma_f32_16x16x32_bf16 v[20:23], v[168:171], v[48:51], v[20:23]\n" \
    "buffer_load_dwordx4 v[48:51], v112, s[40:43], s62 offen offset:2048 sc1\n" \
    "s_waitcnt vmcnt(15)\n" \
    "v_mfma_f32_16x16x32_bf16 v[12:15], a[88:91], v[52:55], v[12:15]\n" \
    "v_mfma_f32_16x16x32_bf16 v[16:19], a[92:95], v[52:55], v[16:19]\n" \
    "v_mfma_f32_16x16x32_bf16 v[20:23], v[172:175], v[52:55], v[20:23]\n" \
    "buffer_load_dwordx4 v[52:55], v112, s[40:43], s62 offen offset:3072 sc1\n" \
    "s_waitcnt vmcnt(15)\n" \
    "v_mfma_f32_16x16x32_bf16 v[0:3], a[96:99], v[56:59], v[0:3]\n" \
    "v_mfma_f32_16x16x32_bf16 v[4:7], a[100:103], v[56:59], v[4:7]\n" \
    "v_mfma_f32_16x16x32_bf16 v[8:11], v[176:179], v[56:59], v[8:11]\n" \
    "s_add_u32 s62, s65, s75\n" \
    "buffer_load_dwordx4 v[56:59], v112, s[40:43], s62 offen offset:0 sc1\n" \
    "s_waitcnt vmcnt(15)\n" \
    "v_mfma_f32_16x16x32_bf16 v[0:3], a[104:107], v[60:63], v[0:3]\n" \
    "v_mfma_f32_16x16x32_bf16 v[4:7], a[108:111], v[60:63], v[4:7]\n" \
    "v_mfma_f32_16x16x32_bf16 v[8:11], v[180:183], v[60:63], v[8:11]\n" \
    "buffer_load_dwordx4 v[60:63], v112, s[40:43], s62 offen offset:1024 sc1\n" \
    "s_waitcnt vmcnt(15)\n" \
    "v_mfma_f32_16x16x32_bf16 v[0:3], a[112:115], v[64:67], v[0:3]\n" \
    "v_mfma_f32_16x16x32_bf16 v[4:7], a[116:119], v[64:67], v[4:7]\n" \
    "v_mfma_f32_16x16x32_bf16 v[8:11], v[184:187], v[64:67], v[8:11]\n" \
    "buffer_load_dwordx4 v[64:67], v112, s[40:43], s62 offen offset:2048 sc1\n" \
    "s_waitcnt vmcnt(15)\n" \
    "v_mfma_f32_16x16x32_bf16 v[0:3], a[120:123], v[68:71], v[0:3]\n" \
    "v_mfma_f32_16x16x32_bf16 v[4:7], a[124:127], v[68:71], v[4:7]\n" \
    "v_mfma_f32_16x16x32_bf16 v[8:11], v[188:191], v[68:71], v[8:11]\n" \
    "buffer_load_dwordx4 v[68:71], v112, s[40:43], s62 offen offset:3072 sc1\n" \
    "s_waitcnt vmcnt(15)\n" \
    "v_mfma_f32_16x16x32_bf16 v[12:15], a[96:99], v[72:75], v[12:15]\n" \
    "v_mfma_f32_16x16x32_bf16 v[16:19], a[100:103], v[72:75], v[16:19]\n" \
    "v_mfma_f32_16x16x32_bf16 v[20:23], v[176:179], v[72:75], v[20:23]\n" \
    "buffer_load_dword v88, off, s[40:43], s63 sc1\n" \
    "s_add_u32 s62, s62, 4096\n" \
    "buffer_load_dwordx4 v[72:75], v112, s[40:43], s62 offen offset:0 sc1\n" \
    "s_waitcnt vmcnt(16)\n" \
    "v_mfma_f32_16x16x32_bf16 v[12:15], a[104:107], v[76:79], v[12:15]\n" \
    "v_mfma_f32_16x16x32_bf16 v[16:19], a[108:111], v[76:79], v[16:19]\n" \
    "v_mfma_f32_16x16x32_bf16 v[20:23], v[180:183], v[76:79], v[20:23]\n" \
    "buffer_load_dwordx4 v[76:79], v112, s[40:43], s62 offen offset:1024 sc1\n" \
    "s_waitcnt vmcnt(16)\n" \
    "v_mfma_f32_16x16x32_bf16 v[12:15], a[112:115], v[80:83], v[12:15]\n" \
    "v_mfma_f32_16x16x32_bf16 v[16:19], a[116:119], v[80:83], v[16:19]\n" \
    "v_mfma_f32_16x16x32_bf16 v[20:23], v[184:187], v[80:83], v[20:23]\n" \
    "buffer_load_dwordx4 v[80:83], v112, s[40:43], s62 offen offset:2048 sc1\n" \
    "s_waitcnt vmcnt(16)\n" \
    "v_mfma_f32_16x16x32_bf16 v[12:15], a[120:123], v[84:87], v[12:15]\n" \
    "v_mfma_f32_16x16x32_bf16 v[16:19], a[124:127], v[84:87], v[16:19]\n" \
    "v_mfma_f32_16x16x32_bf16 v[20:23], v[188:191], v[84:87], v[20:23]\n" \
    "buffer_load_dwordx4 v[84:87], v112, s[40:43], s62 offen offset:3072 sc1\n" \
    "s_waitcnt vmcnt(16)\n" \
    "v_mfma_f32_16x16x32_bf16 v[0:3], a[128:131], v[24:27], v[0:3]\n" \
    "v_mfma_f32_16x16x32_bf16 v[4:7], a[132:135], v[24:27], v[4:7]\n" \
    "v_mfma_f32_16x16x32_bf16 v[8:11], v[192:195], v[24:27], v[8:11]\n" \
    "s_add_u32 s62, s65, s76\n" \
    "buffer_load_dwordx4 v[24:27], v112, s[40:43], s62 offen offset:0 sc1\n" \
    "s_waitcnt vmcnt(16)\n" \
    "v_mfma_f32_16x16x32_bf16 v[0:3], a[136:139], v[28:31], v[0:3]\n" \
    "v_mfma_f32_16x16x32_bf16 v[4:7], a[140:143], v[28:31], v[4:7]\n" \
    "v_mfma_f32_16x16x32_bf16 v[8:11], v[196:199], v[28:31], v[8:11]\n" \
    "buffer_load_dwordx4 v[28:31], v112, s[40:43], s62 offen offset:1024 sc1\n" \
    "s_waitcnt vmcnt(16)\n" \
    "v_mfma_f32_16x16x32_bf16 v[0:3], a[144:147], v[32:35], v[0:3]\n" \
    "v_mfma_f32_16x16x32_bf16 v[4:7], a[148:151], v[32:35], v[4:7]\n" \
    "v_mfma_f32_16x16x32_bf16 v[8:11], v[200:203], v[32:35], v[8:11]\n" \
    "buffer_load_dwordx4 v[32:35], v112, s[40:43], s62 offen offset:2048 sc1\n" \
    "s_waitcnt vmcnt(16)\n" \
    "v_mfma_f32_16x16x32_bf16 v[0:3], a[152:155], v[36:39], v[0:3]\n" \
    "v_mfma_f32_16x16x32_bf16 v[4:7], a[156:159], v[36:39], v[4:7]\n" \
    "v_mfma_f32_16x16x32_bf16 v[8:11], v[204:207], v[36:39], v[8:11]\n" \
    "buffer_load_dwordx4 v[36:39], v112, s[40:43], s62 offen offset:3072 sc1\n" \
    "s_waitcnt vmcnt(16)\n" \
    "v_mfma_f32_16x16x32_bf16 v[12:15], a[128:131], v[40:43], v[12:15]\n" \
    "v_mfma_f32_16x16x32_bf16 v[16:19], a[132:135], v[40:43], v[16:19]\n" \
    "v_mfma_f32_16x16x32_bf16 v[20:23], v[192:195], v[40:43], v[20:23]\n" \
    "s_add_u32 s62, s62, 4096\n" \
    "buffer_load_dwordx4 v[40:43], v112, s[40:43], s62 offen offset:0 sc1\n" \
    "s_waitcnt vmcnt(16)\n" \
    "v_mfma_f32_16x16x32_bf16 v[12:15], a[136:139], v[44:47], v[12:15]\n" \
    "v_mfma_f32_16x16x32_bf16 v[16:19], a[140:143], v[44:47], v[16:19]\n" \
    "v_mfma_f32_16x16x32_bf16 v[20:23], v[196:199], v[44:47], v[20:23]\n" \
    "buffer_load_dwordx4 v[44:47], v112, s[40:43], s62 offen offset:1024 sc1\n" \
    "s_waitcnt vmcnt(16)\n" \
    "v_mfma_f32_16x16x32_bf16 v[12:15], a[144:147], v[48:51], v[12:15]\n" \
    "v_mfma_f32_16x16x32_bf16 v[16:19], a[148:151], v[48:51], v[16:19]\n" \
    "v_mfma_f32_16x16x32_bf16 v[20:23], v[200:203], v[48:51], v[20:23]\n" \
    "buffer_load_dwordx4 v[48:51], v112, s[40:43], s62 offen offset:2048 sc1\n" \
    "s_waitcnt vmcnt(16)\n" \
    "v_mfma_f32_16x16x32_bf16 v[12:15], a[152:155], v[52:55], v[12:15]\n" \
    "v_mfma_f32_16x16x32_bf16 v[16:19], a[156:159], v[52:55], v[16:19]\n" \
    "v_mfma_f32_16x16x32_bf16 v[20:23], v[204:207], v[52:55], v[20:23]\n" \
    "buffer_load_dwordx4 v[52:55], v112, s[40:43], s62 offen offset:3072 sc1\n" \
    "s_waitcnt vmcnt(16)\n" \
    "v_mfma_f32_16x16x32_bf16 v[0:3], a[160:163], v[56:59], v[0:3]\n" \
    "v_mfma_f32_16x16x32_bf16 v[4:7], a[164:167], v[56:59], v[4:7]\n" \
    "v_mfma_f32_16x16x32_bf16 v[8:11], v[208:211], v[56:59], v[8:11]\n" \
    "s_add_u32 s62, s65, s77\n" \
    "buffer_load_dwordx4 v[56:59], v112, s[40:43], s62 offen offset:0 sc1\n" \
    "s_waitcnt vmcnt(16)\n" \
    "v_mfma_f32_16x16x32_bf16 v[0:3], a[168:171], v[60:63], v[0:3]\n" \
    "v_mfma_f32_16x16x32_bf16 v[4:7], a[172:175], v[60:63], v[4:7]\n" \
    "v_mfma_f32_16x16x32_bf16 v[8:11], v[212:215], v[60:63], v[8:11]\n" \
    "buffer_load_dwordx4 v[60:63], v112, s[40:43], s62 offen offset:1024 sc1\n" \
    "s_waitcnt vmcnt(16)\n" \
    "v_mfma_f32_16x16x32_bf16 v[0:3], a[176:179], v[64:67], v[0:3]\n" \
    "v_mfma_f32_16x16x32_bf16 v[4:7], a[180:183], v[64:67], v[4:7]\n" \
    "v_mfma_f32_16x16x32_bf16 v[8:11], v[216:219], v[64:67], v[8:11]\n" \
    "buffer_load_dwordx4 v[64:67], v112, s[40:43], s62 offen offset:2048 sc1\n" \
    "s_waitcnt vmcnt(16)\n" \
    "v_mfma_f32_16x16x32_bf16 v[0:3], a[184:187], v[68:71], v[0:3]\n" \
    "v_mfma_f32_16x16x32_bf16 v[4:7], a[188:191], v[68:71], v[4:7]\n" \
    "v_mfma_f32_16x16x32_bf16 v[8:11], v[220:223], v[68:71], v[8:11]\n" \
    "buffer_load_dwordx4 v[68:71], v112, s[40:43], s62 offen offset:3072 sc1\n" \
    "s_waitcnt vmcnt(15)\n" \
    "v_mfma_f32_16x16x32_bf16 v[12:15], a[160:163], v[72:75], v[12:15]\n" \
    "v_mfma_f32_16x16x32_bf16 v[16:19], a[164:167], v[72:75], v[16:19]\n" \
    "v_mfma_f32_16x16x32_bf16 v[20:23], v[208:211], v[72:75], v[20:23]\n" \
    "s_add_u32 s62, s62, 4096\n" \
    "buffer_load_dwordx4 v[72:75], v112, s[40:43], s62 offen offset:0 sc1\n" \
    "s_waitcnt vmcnt(15)\n" \
    "v_mfma_f32_16x16x32_bf16 v[12:15], a[168:171], v[76:79], v[12:15]\n" \
    "v_mfma_f32_16x16x32_bf16 v[16:19], a[172:175], v[76:79], v[16:19]\n" \
    "v_mfma_f32_16x16x32_bf16 v[20:23], v[212:215], v[76:79], v[20:23]\n" \
    "buffer_load_dwordx4 v[76:79], v112, s[40:43], s62 offen offset:1024 sc1\n" \
    "s_waitcnt vmcnt(15)\n" \
    "v_mfma_f32_16x16x32_bf16 v[12:15], a[176:179], v[80:83], v[12:15]\n" \
    "v_mfma_f32_16x16x32_bf16 v[16:19], a[180:183], v[80:83], v[16:19]\n" \
    "v_mfma_f32_16x16x32_bf16 v[20:23], v[216:219], v[80:83], v[20:23]\n" \
    "v_cmp_gt_u32 vcc, 1, v88\n" \
    "buffer_load_dwordx4 v[80:83], v112, s[40:43], s62 offen offset:2048 sc1\n" \
    "s_waitcnt vmcnt(15)\n" \
    "v_mfma_f32_16x16x32_bf16 v[12:15], a[184:187], v[84:87], v[12:15]\n" \
    "v_mfma_f32_16x16x32_bf16 v[16:19], a[188:191], v[84:87], v[16:19]\n" \
    "v_mfma_f32_16x16x32_bf16 v[20:23], v[220:223], v[84:87], v[20:23]\n" \
    "s_cmp_eq_u64 vcc, 0\n" \
    "s_cselect_b32 s46, s67, 0\n" \
    "buffer_load_dwordx4 v[84:87], v112, s[40:43], s62 offen offset:3072 sc1\n" \
    "s_waitcnt vmcnt(15)\n" \
    "v_mfma_f32_16x16x32_bf16 v[0:3], a[192:195], v[24:27], v[0:3]\n" \
    "v_mfma_f32_16x16x32_bf16 v[4:7], a[196:199], v[24:27], v[4:7]\n" \
    "v_mfma_f32_16x16x32_bf16 v[8:11], v[224:227], v[24:27], v[8:11]\n" \
    "s_add_u32 s62, s66, s70\n" \
    "buffer_load_dwordx4 v[24:27], v112, s[44:47], s62 offen offset:0 sc1\n" \
    "s_waitcnt vmcnt(15)\n" \
    "v_mfma_f32_16x16x32_bf16 v[0:3], a[200:203], v[28:31], v[0:3]\n" \
    "v_mfma_f32_16x16x32_bf16 v[4:7], a[204:207], v[28:31], v[4:7]\n" \
    "v_mfma_f32_16x16x32_bf16 v[8:11], v[228:231], v[28:31], v[8:11]\n" \
    "buffer_load_dwordx4 v[28:31], v112, s[44:47], s62 offen offset:1024 sc1\n" \
    "s_waitcnt vmcnt(15)\n" \
    "v_mfma_f32_16x16x32_bf16 v[0:3], a[208:211], v[32:35], v[0:3]\n" \
    "v_mfma_f32_16x16x32_bf16 v[4:7], a[212:215], v[32:35], v[4:7]\n" \
    "v_mfma_f32_16x16x32_bf16 v[8:11], v[232:235], v[32:35], v[8:11]\n" \
    "buffer_load_dwordx4 v[32:35], v112, s[44:47], s62 offen offset:2048 sc1\n" \
    "s_waitcnt vmcnt(15)\n" \
    "v_mfma_f32_16x16x32_bf16 v[0:3], a[216:219], v[36:39], v[0:3]\n" \
    "v_mfma_f32_16x16x32_bf16 v[4:7], a[220:223], v[36:39], v[4:7]\n" \
    "v_mfma_f32_16x16x32_bf16 v[8:11], v[236:239], v[36:39], v[8:11]\n" \
    "buffer_load_dwordx4 v[36:39], v112, s[44:47], s62 offen offset:3072 sc1\n" \
    "s_waitcnt vmcnt(15)\n" \
    "v_mfma_f32_16x16x32_bf16 v[12:15], a[192:195], v[40:43], v[12:15]\n" \
    "v_mfma_f32_16x16x32_bf16 v[16:19], a[196:199], v[40:43], v[16:19]\n" \
    "v_mfma_f32_16x16x32_bf16 v[20:23], v[224:227], v[40:43], v[20:23]\n" \
    "s_add_u32 s62, s62, 4096\n" \
    "buffer_load_dwordx4 v[40:43], v112, s[44:47], s62 offen offset:0 sc1\n" \
    "s_waitcnt vmcnt(15)\n" \
    "v_mfma_f32_16x16x32_bf16 v[12:15], a[200:203], v[44:47], v[12:15]\n" \
    "v_mfma_f32_16x16x32_bf16 v[16:19], a[204:207], v[44:47], v[16:19]\n" \
    "v_mfma_f32_16x16x32_bf16 v[20:23], v[228:231], v[44:47], v[20:23]\n" \
    "buffer_load_dwordx4 v[44:47], v112, s[44:47], s62 offen offset:1024 sc1\n" \
    "s_waitcnt vmcnt(15)\n" \
    "v_mfma_f32_16x16x32_bf16 v[12:15], a[208:211], v[48:51], v[12:15]\n" \
    "v_mfma_f32_16x16x32_bf16 v[16:19], a[212:215], v[48:51], v[16:19]\n" \
    "v_mfma_f32_16x16x32_bf16 v[20:23], v[232:235], v[48:51], v[20:23]\n" \
    "buffer_load_dwordx4 v[48:51], v112, s[44:47], s62 offen offset:2048 sc1\n" \
    "s_waitcnt vmcnt(15)\n" \
    "v_mfma_f32_16x16x32_bf16 v[12:15], a[216:219], v[52:55], v[12:15]\n" \
    "v_mfma_f32_16x16x32_bf16 v[16:19], a[220:223], v[52:55], v[16:19]\n" \
    "v_mfma_f32_16x16x32_bf16 v[20:23], v[236:239], v[52:55], v[20:23]\n" \
    "buffer_load_dwordx4 v[52:55], v112, s[44:47], s62 offen offset:3072 sc1\n" \
    "s_waitcnt vmcnt(15)\n" \
    "v_mfma_f32_16x16x32_bf16 v[0:3], a[224:227], v[56:59], v[0:3]\n" \
    "v_mfma_f32_16x16x32_bf16 v[4:7], a[228:231], v[56:59], v[4:7]\n" \
    "v_mfma_f32_16x16x32_bf16 v[8:11], v[240:243], v[56:59], v[8:11]\n" \
    "s_add_u32 s62, s66, s71\n" \
    "buffer_load_dwordx4 v[56:59], v112, s[44:47], s62 offen offset:0 sc1\n" \
    "s_waitcnt vmcnt(15)\n" \
    "v_mfma_f32_16x16x32_bf16 v[0:3], a[232:235], v[60:63], v[0:3]\n" \
    "v_mfma_f32_16x16x32_bf16 v[4:7], a[236:239], v[60:63], v[4:7]\n" \
    "v_mfma_f32_16x16x32_bf16 v[8:11], v[244:247], v[60:63], v[8:11]\n" \
    "buffer_load_dwordx4 v[60:63], v112, s[44:47], s62 offen offset:1024 sc1\n" \
    "s_waitcnt vmcnt(15)\n" \
    "v_mfma_f32_16x16x32_bf16 v[0:3], a[240:243], v[64:67], v[0:3]\n" \
    "v_mfma_f32_16x16x32_bf16 v[4:7], a[244:247], v[64:67], v[4:7]\n" \
    "v_mfma_f32_16x16x32_bf16 v[8:11], v[248:251], v[64:67], v[8:11]\n" \
    "buffer_load_dwordx4 v[64:67], v112, s[44:47], s62 offen offset:2048 sc1\n" \
    "s_waitcnt vmcnt(15)\n" \
    "v_mfma_f32_16x16x32_bf16 v[0:3], a[248:251], v[68:71], v[0:3]\n" \
    "v_mfma_f32_16x16x32_bf16 v[4:7], a[252:255], v[68:71], v[4:7]\n" \
    "v_mfma_f32_16x16x32_bf16 v[8:11], v[252:255], v[68:71], v[8:11]\n" \
    "buffer_load_dwordx4 v[68:71], v112, s[44:47], s62 offen offset:3072 sc1\n" \
    "s_waitcnt vmcnt(15)\n" \
    "v_mfma_f32_16x16x32_bf16 v[12:15], a[224:227], v[72:75], v[12:15]\n" \
    "v_mfma_f32_16x16x32_bf16 v[16:19], a[228:231], v[72:75], v[16:19]\n" \
    "v_mfma_f32_16x16x32_bf16 v[20:23], v[240:243], v[72:75], v[20:23]\n" \
    "s_add_u32 s62, s62, 4096\n" \
    "buffer_load_dwordx4 v[72:75], v112, s[44:47], s62 offen offset:0 sc1\n" \
    "s_waitcnt vmcnt(15)\n" \
    "v_mfma_f32_16x16x32_bf16 v[12:15], a[232:235], v[76:79], v[12:15]\n" \
    "v_mfma_f32_16x16x32_bf16 v[16:19], a[236:239], v[76:79], v[16:19]\n" \
    "v_mfma_f32_16x16x32_bf16 v[20:23], v[244:247], v[76:79], v[20:23]\n" \
    "buffer_load_dwordx4 v[76:79], v112, s[44:47], s62 offen offset:1024 sc1\n" \
    "s_waitcnt vmcnt(15)\n" \
    "v_mfma_f32_16x16x32_bf16 v[12:15], a[240:243], v[80:83], v[12:15]\n" \
    "v_mfma_f32_16x16x32_bf16 v[16:19], a[244:247], v[80:83], v[16:19]\n" \
    "v_mfma_f32_16x16x32_bf16 v[20:23], v[248:251], v[80:83], v[20:23]\n" \
    "buffer_load_dwordx4 v[80:83], v112, s[44:47], s62 offen offset:2048 sc1\n" \
    "s_waitcnt vmcnt(15)\n" \
    "v_mfma_f32_16x16x32_bf16 v[12:15], a[248:251], v[84:87], v[12:15]\n" \
    "v_mfma_f32_16x16x32_bf16 v[16:19], a[252:255], v[84:87], v[16:19]\n" \
    "v_mfma_f32_16x16x32_bf16 v[20:23], v[252:255], v[84:87], v[20:23]\n" \
    "buffer_load_dwordx4 v[84:87], v112, s[44:47], s62 offen offset:3072 sc1\n" \
    "ds_write_b128 v114, v[0:3] offset:0\n" \
    "ds_write_b128 v114, v[4:7] offset:1024\n" \
    "ds_write_b128 v114, v[8:11] offset:2048\n" \
    "ds_write_b128 v114, v[12:15] offset:3072\n" \
    "ds_write_b128 v114, v[16:19] offset:4096\n" \
    "s_nop 0\n" \
    "s_nop 0\n" \
    "ds_write_b128 v114, v[20:23] offset:5120\n" \
    "s_lshl_b32 s63, s58, 17\n" \
    "s_cmp_eq_u32 s56, 3\n" \
    "s_waitcnt lgkmcnt(0)\n" \
    "s_barrier\n" \
    "s_cbranch_scc1 Lb_x\n" \
    "ds_read_b128 v[88:91], v115 offset:0\n" \
    "ds_read_b128 v[92:95], v115 offset:256\n" \
    "ds_read_b128 v[96:99], v115 offset:6144\n" \
    "ds_read_b128 v[100:103], v115 offset:6400\n" \
    "s_waitcnt lgkmcnt(1)\n" \
    "v_pk_add_f32 v[88:89], v[88:89], v[96:97]\n" \
    "v_pk_add_f32 v[90:91], v[90:91], v[98:99]\n" \
    "s_waitcnt lgkmcnt(0)\n" \
    "v_pk_add_f32 v[92:93], v[92:93], v[100:101]\n" \
    "v_pk_add_f32 v[94:95], v[94:95], v[102:103]\n" \
    "ds_read_b128 v[96:99], v115 offset:12288\n" \
    "ds_read_b128 v[100:103], v115 offset:12544\n" \
    "ds_read_b128 v[104:107], v115 offset:18432\n" \
    "ds_read_b128 v[108:111], v115 offset:18688\n" \
    "s_waitcnt lgkmcnt(1)\n" \
    "v_pk_add_f32 v[96:97], v[96:97], v[104:105]\n" \
    "v_pk_add_f32 v[98:99], v[98:99], v[106:107]\n" \
    "s_waitcnt lgkmcnt(0)\n" \
    "v_pk_add_f32 v[100:101], v[100:101], v[108:109]\n" \
    "v_pk_add_f32 v[102:103], v[102:103], v[110:111]\n" \
    "v_pk_add_f32 v[88:89], v[88:89], v[96:97]\n" \
    "v_pk_add_f32 v[90:91], v[90:91], v[98:99]\n" \
    "v_pk_add_f32 v[92:93], v[92:93], v[100:101]\n" \
    "v_pk_add_f32 v[94:95], v[94:95], v[102:103]\n" \
    "v_cvt_pk_bf16_f32 v104, v88, v89\n" \
    "v_cvt_pk_bf16_f32 v105, v90, v91\n" \
    "v_cvt_pk_bf16_f32 v106, v92, v93\n" \
    "v_cvt_pk_bf16_f32 v107, v94, v95\n" \
    "buffer_store_dwordx4 v[104:107], v116, s[48:51], s63 offen\n" \
    "Lb_x:\n" \
    "v_xor_b32 v114, 0x8000, v114\n" \
    "v_xor_b32 v115, 0x8000, v115\n" \
    "s_mov_b32 s58, s64\n" \
    "s_mov_b32 s65, s66\n" \
    "s_cmp_gt_u32 s58, 63\n" \
    "s_cbranch_scc1 Lb_done\n" \
    "s_cmp_lg_u32 s46, 0\n" \
    "s_cbranch_scc1 Lb_seg\n" \
    "s_branch Lb_wait\n" \
    "Lb_done:\n" \
    "s_cmp_lg_u32 s90, 0\n" \
    "s_cbranch_scc1 Lb_end\n" \
    "v_mov_b32 v0, 1\n" \
    "s_mov_b32 s62, 32768\n" \
    "buffer_store_dword v0, v113, s[40:43], s62 offen sc1\n" \
    "Lb_end:\n" \
    "s_endpgm\n"

constexpr int NSEG = 64;
constexpr unsigned CB = 32768, C_TMO = CB, C_START = CB + 128, C_SEL = CB + 256;
constexpr unsigned C_AQ = CB + 512, C_AKV = CB + 896, C_AO = CB + 1024;
constexpr unsigned C_TILE = CB + 8192;
constexpr unsigned CTRL_BYTES = CB + 24576, C_TICK = CTRL_BYTES;
constexpr unsigned uslot(int i) { return 65536 + i * 32768; }
constexpr unsigned oslot(int i) { return (1u << 19) + i * 131072; }
constexpr unsigned PROBE_OFF = 0x60000, DBG_OFF = (3u << 19) + 65536;
constexpr unsigned KX_OFF = 12u << 20, VX_OFF = 28u << 20, QX_OFF = 44u << 20;
constexpr unsigned QA_OFF = 76u << 20, KVA_OFF = 80u << 20, OA_OFF = 81u << 20, WS_USED = 86u << 20;
constexpr unsigned EMPTY = 0xFFFFFFFFu;
constexpr unsigned OOB = 0xFFFFFF00u;
constexpr unsigned SPIN_CAP = 1u << 20;
constexpr int SC1 = 16;

struct Cfg { unsigned d1, d1b, d2, d3, near, split, nhelp, s2, plan[15], pad; };
struct Params {
  const u16 *Ax, *Awq, *Awk, *Awv, *Awo, *Bx, *mem0, *Wq, *Wk, *Wv, *Wo; u16* out; unsigned char* ws;
};
constexpr Cfg CFG = {0, 3, 1, 6, 0x15, 46, 2, 54, {0x70707043u, 0x70707070u, 0x70707070u, 0x03300980u, 0x26170660u, 0x70707050u, 0x13311981u, 0x27071661u, 0x70707051u, 0x22212882u, 0x63152414u, 0x70707070u, 0x23120283u, 0x62250504u, 0x70707070u}, 0};

DEVI rsrc_t mkrs(const void* p, unsigned n) { return __builtin_amdgcn_make_buffer_rsrc((void*)p, 0, n, 0x00020000); }
template <int AUX> DEVI v4u ld128(rsrc_t r, unsigned v, unsigned s) { return __builtin_amdgcn_raw_buffer_load_b128(r, v, s, AUX); }
template <int AUX> DEVI unsigned ld32(rsrc_t r, unsigned v, unsigned s) { return __builtin_amdgcn_raw_buffer_load_b32(r, v, s, AUX); }
template <int AUX> DEVI void st128(rsrc_t r, unsigned v, unsigned s, v4u x) { __builtin_amdgcn_raw_buffer_store_b128(x, r, v, s, AUX); }
template <int AUX> DEVI void st32(rsrc_t r, unsigned v, unsigned s, unsigned x) { __builtin_amdgcn_raw_buffer_store_b32(x, r, v, s, AUX); }
DEVI unsigned f2u(float f) { return __builtin_bit_cast(unsigned, f); }
DEVI unsigned rfl(unsigned x) { return __builtin_amdgcn_readfirstlane(x); }
DEVI int lane_id() { return __builtin_amdgcn_mbcnt_hi(~0u, __builtin_amdgcn_mbcnt_lo(~0u, 0u)); }
DEVI long long now() { long long t; asm volatile("s_memtime %0\n\ts_waitcnt lgkmcnt(0)" : "=s"(t)::"memory"); return t; }
typedef LAS char* lptr;
DEVI lptr lp(unsigned off) { asm volatile("" : "+v"(off)); return (lptr)(size_t)off; }
template <typename T> DEVI T lds_ld(lptr b, int off) { return *(const LAS T*)(b + off); }
template <typename T> DEVI void lds_st(lptr b, int off, T v) { *(LAS T*)(b + off) = v; }
DEVI v4u srd(const void* p, unsigned n) { return v4u{rfl((unsigned)(size_t)p), rfl((unsigned)((size_t)p >> 32)) & 0xFFFFu, rfl(n), 0x20000u}; }
DEVI v8u cat(v4u a, v4u b) { return __builtin_shufflevector(a, b, 0, 1, 2, 3, 4, 5, 6, 7); }
DEVI void add_word(unsigned char* ws, unsigned off, unsigned n) { __hip_atomic_fetch_add((unsigned*)(ws + off), n, __ATOMIC_RELAXED, __HIP_MEMORY_SCOPE_AGENT); }

DEVI unsigned piece_col(int lane) { const int rho = lane >> 3, q = lane & 7; return ((((q >> 1) ^ (rho >> 1)) << 1) | (q & 1)) * 16; }
DEVI unsigned piece_rd(int lane) { const int g = lane >> 4, rho = 4 * (g & 1) + ((lane >> 2) & 3); return (g >> 1) * 1024 + rho * 128 + (rho >> 1) * 32 + (lane & 3) * 8; }
DEVI unsigned wrow_src(int lane) { const int rho = lane >> 3; return (4 * (rho & 3) + 2 * (rho >> 2)) * 8192 + piece_col(lane); }

constexpr unsigned L1_U = 131072, L1_K = L1_U + 16384, L1_F = L1_K + 8192;
DEVI void role_qk(const Params& p, int h) {
  const int lane = lane_id(), w = rfl(threadIdx.x >> 6), c = lane & 15, g = lane >> 4;
  const rsrc_t ws = mkrs(p.ws, WS_USED);
  const unsigned o_v = (c < 8) ? (unsigned)(((c >> 2) * 4 + (h >> 3)) * 16384 + (h & 7) * 1024 + w * 256 + g * 64 + (c & 3) * 16) : OOB;
#pragma unroll
  for (int i = 0; i < 4; i++) st128<SC1>(ws, o_v, oslot(i >> 1) + (i & 1) * 8192, v4u{EMPTY, EMPTY, EMPTY, EMPTY});
  const v2u w_v = {w * 32768 + piece_rd(lane), wrow_src(lane) + (h * 128 + 64 * (w & 1)) * 2};
  const int erow = 32 * w + 8 * (c >> 2) + (c & 3);
  v16u ad;
  ad[0] = lane * 16 + w * 1024; ad[1] = L1_U + w * 1024 + lane * 16; ad[2] = L1_U + lane * 16; ad[3] = w * 32768 + lane * 16; ad[4] = o_v;
  ad[5] = h * 8192 + w * 2048 + lane * 16;
  ad[6] = h * 8192 + (erow >> 4) * 1024 + (g * 16 + (erow & 15)) * 16;
  ad[7] = (h >> 2) * 16384 + (h & 3) * 2048 + (erow >> 3) * 128 + (g & 1) * 64 + (c & 3) * 16;
  ad[8] = L1_F + (64 * (w & 1) + 32 * (g & 1) + c) * 32 + (((2 * (w >> 1) + (g >> 1)) ^ (c >> 2)) * 8);
  ad[9] = L1_F + (lane >> 2) * 32 + (((lane & 3) ^ g) * 8);
  ad[10] = (lane & 4) ? 0xEEEEu : 0x4444u;
  ad[11] = (c >= 8) ? (unsigned)(h * 2048 + w * 512 + g * 128 + (c - 8) * 16) : OOB;
  ad[12] = L1_U + lane * 256 + (lane & 31) * 4;
  ad[13] = L1_K + w * 2048 + lane * 16; ad[14] = lane == 0 ? 0u : OOB; ad[15] = ad[9] + w * 1024;
  const v4u d = srd(p.ws, WS_USED), dw = srd((w >> 1) ? p.Wk : p.Wq, 1024 * 4096 * 2);
  const v16u sin = {d.x, d.y, d.z, d.w, d.x, d.y, (h == 0 && w == 0) ? WS_USED : 0u, d.w, d.x, d.y, w == 3 ? WS_USED : 0u, d.w, 0u, (CFG.d2 & 255) + (((h & 7) * (CFG.d2 >> 8)) >> 2), (unsigned)w, (unsigned)(h * 128)};
  asm volatile(QK_LOOP_ASM
               :
               : "{s90}"(SPIN_CAP), "{s91}"(DBG_OFF + (h * 4 + w) * 64), "{s[40:55]}"(sin), "{s56}"(C_TILE + h * 16), "{s[64:71]}"(cat(dw, dw)), "{v[112:127]}"(ad), "{v[110:111]}"(w_v)
               : "v255", "a255", "s99", "vcc", "memory");
  __builtin_unreachable();
}

DEVI void role_vo(const Params& p, int j) {
  const int lane = lane_id(), w = rfl(threadIdx.x >> 6), c = lane & 15, g = lane >> 4, th = (CFG.near & 8) ? j >> 5 : j & 1, r = (CFG.near & 8) ? j & 31 : j >> 1;
  const rsrc_t ws = mkrs(p.ws, WS_USED);
  const v4u empty = {EMPTY, EMPTY, EMPTY, EMPTY};
  const unsigned ust_v = (w == 0 && (lane & 3) == 0) ? (unsigned)((r >> 4) * 16384 + (r & 15) * 512 + (c >> 3) * 256 + th * 128 + ((c >> 2) & 1) * 64 + g * 16) : OOB;
  const unsigned vst_v = (w == 1) ? (unsigned)((j >> 3) * 16384 + (j & 7) * 1024 + lane * 16) : OOB;
  if (w == 0) {
    const v4u m0 = ld128<0>(mkrs(p.mem0, 8 * 1024 * 2), (unsigned)((4 * th + g) * 2048 + r * 64 + (c >> 2) * 16), 0);
#pragma unroll
    for (int i = 2; i < 6; i++) st128<SC1>(ws, ust_v, uslot(i >> 1) + (i & 1) * 8192, empty);
    const v4u m1 = {__builtin_amdgcn_perm(m0.z, m0.x, 0x05040100), __builtin_amdgcn_perm(m0.z, m0.x, 0x07060302), __builtin_amdgcn_perm(m0.w, m0.y, 0x05040100), __builtin_amdgcn_perm(m0.w, m0.y, 0x07060302)};
    st128<SC1>(ws, ust_v, uslot(4), m1); st128<SC1>(ws, ust_v, uslot(4) + 8192, m1);
    st128<SC1>(ws, ust_v ^ 16384, uslot(5), m1); st128<SC1>(ws, ust_v ^ 16384, uslot(5) + 8192, m1);
  } else if (w == 1) {
#pragma unroll
    for (int i = 0; i < 6; i++) st128<SC1>(ws, vst_v, oslot(i >> 1) + 0x80000 + (i & 1) * 8192, empty);
  }
  v16u ad;
  ad[0] = lane * 16 + (w >> 1) * 16384 + (w & 1) * 4096; ad[1] = (th * 4 + w) * 16384 + lane * 16;
  ad[2] = ust_v; ad[3] = vst_v; ad[4] = (lane & 8) ? 0xEEEEu : 0x4444u; ad[5] = lane == 0 ? 0u : OOB;
  ad[6] = w * 2304 + g * 272 + c * 16; ad[7] = 10240 + w * 512 + lane * 8;
  ad[8] = (lane >> 5) * 1088 + ((lane >> 2) & 1) * 544 + ((lane >> 4) & 1) * 272 + ((lane >> 3) & 1) * 128 + (lane & 3) * 16;
  ad[9] = (lane & 4) ? 0xEEEEu : 0x4444u;
  ad[10] = 256 * w * 8192 + wrow_src(lane) + j * 128;
  ad[11] = (1024 * w + 32 * (g >> 1) + 4 * (g & 1) + 16 * ((c >> 3) & 1) + ((c >> 2) & 1)) * 2048 + r * 64 + (lane & 3) * 16;
  ad[12] = w * 40960 + piece_rd(lane); ad[13] = w * 40960 + lane * 16;
  ad[14] = (1024 * w + 256 * ((r & 1) * 2 + th) + lane) * 2048 + r * 64; ad[15] = c * 128;
  const v2u u_w = {(unsigned)((r >> 4) * 16384 + (r & 15) * 512 + (lane >> 5) * 256 + th * 128 + (lane & 31) * 4), (unsigned)(10240 + (((lane >> 2) & 3) * 16 + g * 4 + (lane & 3)) * 8)};
  const v4u d = srd(p.ws, WS_USED);
  const v16u sin = {d.x, d.y, d.z, d.w, d.x, d.y, (j == 0 && w == 0) ? WS_USED : 0u, d.w, d.x, d.y, w == 0 ? WS_USED : 0u, d.w, 0u, j < 32 ? CFG.d1 : CFG.d1b, (unsigned)w, CFG.d3};
  asm volatile(VO_LOOP_ASM
               :
               : "{s90}"(SPIN_CAP), "{s91}"(DBG_OFF + (128 + j * 4 + w) * 64), "{s[40:55]}"(sin), "{s56}"((r & 15) == 0 ? 256 + (((r >> 4) * 2 + th) * 4 + w) * 128 : 0),
                 "{s[64:71]}"(cat(srd(p.Wv, 1024 * 4096 * 2), srd(p.Wo, 4096 * 1024 * 2))), "{v[112:127]}"(ad), "{v[110:111]}"(u_w)
               : "v255", "a255", "s99", "vcc", "memory");
  __builtin_unreachable();
}

DEVI void role_setup(const Params& p, int w, unsigned& budget) {
  const rsrc_t ws = mkrs(p.ws, WS_USED);
  const int lane = lane_id();
  const unsigned l0 = lane == 0 ? 0u : OOB, t0 = w == 0 ? l0 : OOB;
  unsigned tick = ld32<SC1>(ws, 0, C_TICK), best = ~0u;
  const unsigned blk0 = (w & 1) | (w >> 1) << 2;
  const unsigned pv = lane < 32 ? (lane >> 4) * 8192 + (lane & 15) * 128 : OOB;
  unsigned warm = ld32<SC1>(ws, pv, PROBE_OFF + blk0 * 4096);
#pragma unroll
  for (int i = 0; i < 14; i++) st128<SC1>(ws, lane * 16, (w * 14 + i) * 1024, v4u{0, 0, 0, 0});
  asm volatile("s_waitcnt vmcnt(0)" : "+v"(warm) :: "memory");
  RAWBAR();
  st32<SC1>(ws, pv, PROBE_OFF + blk0 * 4096, 0u);
#pragma unroll 1
  for (int rep = 0; rep < 32; rep++) {
    if (!(rep & 7)) { st32<SC1>(ws, t0, C_TICK, ++tick); DRAIN(); }
    const unsigned b = rep < 8 ? rep & 1 : (best >> 1) & 1, line = rep < 8 ? rep >> 1 : rep - 4;
    if (line > 15) break;
    const long long c0 = now();
    unsigned x = ld32<SC1>(ws, 0, PROBE_OFF + (blk0 | b << 1) * 4096 + line * 128);
    asm volatile("s_waitcnt vmcnt(0)" : "+v"(x) :: "memory");
    best = min(best, (unsigned)(now() - c0) << 3 | blk0 | b << 1);
  }
  const lptr f = lp(0);
  lds_st<unsigned>(f, w * 4, best);
  RAWBAR();
  if (w == 0) {
    const v4u x = lds_ld<v4u>(f, 0);
    const unsigned blk = rfl(min(min(x.x, x.y), min(x.z, x.w))) & 7;
    const unsigned sel = (blk ^ (CFG.near & 4)) << 12 | (blk ^ ((CFG.near >> 4) & 4)) << 28 | (((blk >> 1) ^ CFG.near) & 2) | 1;
    bool told = false;
    for (;;) {
      MEMFENCE();
      const unsigned n = rfl(ld32<SC1>(ws, 0, C_START));
      if (!told && ((n >> 16) == 96 || budget == 0)) { st32<SC1>(ws, l0, C_SEL, sel); told = true; }
      st32<SC1>(ws, l0, C_TICK, ++tick);
      if ((n & 0xFFFF) == 255 || budget == 0) break;
      --budget;
      __builtin_amdgcn_s_sleep(8);
    }
  }
  RAWBAR();
}
DEVI void check_in(const Params& p, int w, unsigned& budget) {
  if (w == 0) {
    const rsrc_t ws = mkrs(p.ws, WS_USED);
    const unsigned t = rfl(ld32<SC1>(ws, 0, C_TICK));
    for (;;) {
      __builtin_amdgcn_s_sleep(16);
      MEMFENCE();
      if (rfl(ld32<SC1>(ws, 0, C_TICK)) != t || budget == 0) break;
      --budget;
    }
    if (lane_id() == 0) add_word(p.ws, C_START, 1);
  }
  RAWBAR();
}
DEVI void wg_wait(const Params& p, unsigned off0, unsigned want0, unsigned off1, unsigned want1, int w, unsigned& budget) {
  if (w == 0) {
    const rsrc_t ws = mkrs(p.ws, WS_USED);
    for (;;) {
      MEMFENCE();
      const unsigned x = ld32<SC1>(ws, 0, off0), y = ld32<SC1>(ws, 0, off1);
      if ((rfl(x) >= want0 && rfl(y) >= want1) || budget == 0) break;
      --budget;
      __builtin_amdgcn_s_sleep(4);
    }
  }
  RAWBAR();
}
DEVI void wg_signal(const Params& p, unsigned off, int w, unsigned n) {
  DRAIN();
  RAWBAR();
  if (w == 0 && lane_id() == 0) add_word(p.ws, off, n);
}

enum { U_BK = 0, U_BV = 1, U_BQ = 2, U_AQ = 3, U_AKV = 4, U_AO = 5, U_AT = 6, U_NONE = 7, U_HALF = 8 };
constexpr int NROUND = 12;
constexpr unsigned L_REC = 147456;
DEVI unsigned plan_at(const v4u& pl, int r) { return r < NROUND ? ((r < 4 ? pl.x : r < 8 ? pl.y : pl.z) >> (8 * (r & 3))) & 0xFF : 0x70; }
DEVI void write_records(const Params& p, const v4u& pl, int idx, int w) {
  const int r = lane_id(), wm = w >> 1, wn = w & 1;
  const unsigned e = plan_at(pl, r), n = e & 15;
  unsigned t = e >> 4, a = n & 7, b = idx, row = wm * 4, xoff = 0, xbytes = 524288;
  const bool last = t > U_AQ || (plan_at(pl, r + 1) >> 4) > U_AQ;
  if (t == U_HALF) { t = n >> 1; a = 0; b = (n & 1) * 16 + (idx & 15); row = (idx >> 4) * 4 + wm * 2; xoff = (idx >> 4) * 262144; xbytes = 262144; }
  else if (t == U_AKV) { a = idx & 7; b = 0; }
  else if (t > U_BQ) { a = 4 * (n & 1) + (idx >> 3); b = idx & 7; }
  row += a * 8;
  const unsigned co = (b * 128 + wm * 64) * 2;
  const char* x = (const char*)p.Bx; const char* wp; const char* o = (const char*)p.ws;
  unsigned wbytes = 8388608 - co, stride = 8192, mode = 0, k0 = (b * 5 + (b >> 4) * 8) & 15, obytes = WS_USED, base, s0 = 262144, sa = 4096, sb = 1024, flag = C_TILE + a * 512 + b * 16 + t * 4;
  if (t <= U_BQ) {
    wp = (const char*)(t == U_BK ? p.Wk : t == U_BV ? p.Wv : p.Wq) + co;
    base = (t == U_BK ? KX_OFF : t == U_BV ? VX_OFF : QX_OFF) + row * 262144 + b * 8192 + wn * (t == U_BV ? 4096 : 2048);
    if (t == U_BV) { mode = 1; sa = 2048; }
    if (n & 8) mode |= 64;
  } else if (t == U_AKV) {
    x = (const char*)p.Ax; wp = (const char*)(wm ? p.Awv : p.Awk); wbytes = 65536; stride = 64; mode = wn; k0 = a * 2;
    base = KVA_OFF + wn * 131072 + row * 1024; s0 = 1024; sa = wn ? 0x40000u : 128u; sb = wn ? 256u : 0x40000u; flag = C_AKV;
  } else {
    wbytes = 2097152 - co; stride = 2048; k0 = b * 2 + (a & 1);
    if (t == U_AQ) {
      x = (const char*)p.Ax; wp = (const char*)p.Awq + co; mode = 8 | 256;
      base = QA_OFF + (b * 4 + wn * 2) * 131072 + row * 2048; s0 = 2048; sa = 256; sb = 131072; flag = C_AQ + (a * 8 + b) * 4;
    } else {
      x = (const char*)p.ws + OA_OFF; wp = (const char*)p.Awo + co; mode = 4 | 32;
      o = (const char*)p.out; obytes = 2048 * 4096; base = row * 131072 + (b * 128 + wn * 64) * 2; s0 = 131072; sa = 65536; sb = 64; flag = C_AKV + 64;
    }
  }
  x += a * 524288 + xoff;
  if (last) mode |= 64 | 128;
  if (r <= NROUND) {
    const lptr q = lp(L_REC + r * 256 + w * 64);
    lds_st<v4u>(q, 0, v4u{(unsigned)(size_t)x, (unsigned)((size_t)x >> 32) & 0xFFFFu, xbytes, (unsigned)(size_t)wp});
    lds_st<v4u>(q, 16, v4u{(unsigned)((size_t)wp >> 32) & 0xFFFFu, wbytes, stride, mode | k0 << 23});
    lds_st<v4u>(q, 32, v4u{(unsigned)(size_t)o, (unsigned)((size_t)o >> 32) & 0xFFFFu, obytes, base});
    lds_st<v4u>(q, 48, v4u{s0, sa, sb, flag});
  }
  asm volatile("s_waitcnt lgkmcnt(0)" ::: "memory");
}
DEVI unsigned gemm_chain(const Params& p, int round, unsigned t, int w, unsigned& budget) {
  const int lane = lane_id(), r = lane & 15, g = lane >> 4, rho = lane >> 3;
  v16u va;
  va[0] = (w >> 1) * (t == U_HALF ? 8192 : 16384) + r * 128 + ((g ^ (r >> 1)) * 16); va[1] = va[0] ^ 64;
#pragma unroll
  for (int i = 0; i < 4; i++) va[2 + i] = (32768 + (w & 1) * 8192 + piece_rd(lane)) ^ (32 * i);
  va[6] = (8 * w + rho) * 2048 + (((lane & 7) ^ ((8 * (w & 1) + rho) >> 1)) * 16);
  va[7] = 32 * (w & 1) + (rho & 3) + 8 * (rho >> 2);
  va[8] = t == U_AKV && piece_col(lane) >= 64 ? 0x80000000u : piece_col(lane);
  va[9] = t == U_AKV ? (g >> 1) * 65536 + (g & 1) * 512 + ((w & 1) ? r * 16 : (r >> 3) * 256 + (r & 7) * 16) : t == U_AO ? r * 4096 + (g & 1) * 32 + (g >> 1) * 16 : lane * 16;
  va[10] = g * 512 + r * 16; va[11] = lane == 0 ? 0u : OOB; va[12] = t == U_HALF ? 1 : 2; va[13] = L_REC + round * 256 + w * 64 + r * 4; va[14] = va[15] = 0;
  const v4u sg = srd(p.ws, w == 0 ? CTRL_BYTES : 0);
  unsigned flag, bud = rfl(budget);
  if (t == U_HALF) asm volatile(GEMM4_ASM : "={s67}"(flag), "+{v[240:255]}"(va), "+{s88}"(bud) : "{s[52:55]}"(sg), "{s60}"(w) : GEMM_CLOB);
  else asm volatile(GEMM8_ASM : "={s67}"(flag), "+{v[240:255]}"(va) : "{s[52:55]}"(sg), "{s60}"(w) : GEMM_CLOB);
  budget = rfl(bud);
  return rfl(flag);
}

DEVI unsigned attn_pass(const Params& p, int hp, int hq, int w, const f32x4& m, bool exact) {
  const int lane = lane_id();
  v16u va;
  va[0] = lane * 16;
  va[1] = 65536 + w * 8704 + (lane & 31) * 272 + (lane >> 5) * 8;
  va[2] = 65536 + w * 8704 + (lane >> 4) * 272 + (lane & 15) * 16;
  va[3] = (lane >> 4) * 2048 + (lane & 15) * 16;
  va[4] = (lane & 4) ? 0u : 0x3F803F80u; va[5] = (lane & 4) ? 0x3F803F80u : 0u; va[6] = (lane & 1) ? 0xEEEEu : 0x4444u; va[7] = 0;
#pragma unroll
  for (int i = 0; i < 4; i++) { va[8 + i] = f2u(m[i]); va[12 + i] = 0; }
  const v4u d = srd(p.ws, WS_USED);
  v16u sv = {d.x, d.y, d.z, d.w, QA_OFF + 4 * hq * 131072 + (hp * 4 + w) * 2048, exact ? 0u : f2u(-ATTN_LIFT), KVA_OFF + w * 65536 + (w < 2 ? 1024 : 0), KVA_OFF,
             OA_OFF + (hp * 128 + w * 32) * 2048 + hq * 256, (unsigned)w, 0u, exact ? 0x7F800000u : 0u, 0u, 0u, 0u, 0u};
  asm volatile(ATTN_ASM : "+{s[40:55]}"(sv), "+{v[240:255]}"(va) : : ATTN_CLOB);
  return sv[10];
}
DEVI f32x4 attn_max(const Params& p, int hp, int hq, int w) {
  const int lane = lane_id();
  const rsrc_t ws = mkrs(p.ws, WS_USED);
  const unsigned q = QA_OFF + 4 * hq * 131072 + (hp * 4 + w) * 2048;
  bf16x8 qf[4][2];
#pragma unroll
  for (int b = 0; b < 4; b++) { qf[b][0] = __builtin_bit_cast(bf16x8, ld128<SC1>(ws, lane * 16, q + b * 131072)); qf[b][1] = __builtin_bit_cast(bf16x8, ld128<SC1>(ws, lane * 16, q + b * 131072 + 1024)); }
  f32x4 m = {-INFINITY, -INFINITY, -INFINITY, -INFINITY};
#pragma unroll 2
  for (int n = 0; n < 64; n++) {
    const bf16x8 k0 = __builtin_bit_cast(bf16x8, ld128<SC1>(ws, lane * 16, KVA_OFF + n * 1024)), k1 = __builtin_bit_cast(bf16x8, ld128<SC1>(ws, lane * 16, KVA_OFF + 65536 + n * 1024));
#pragma unroll
    for (int b = 0; b < 4; b++) {
      f32x16 s = {};
      s = __builtin_amdgcn_mfma_f32_32x32x16_bf16(k1, qf[b][1], __builtin_amdgcn_mfma_f32_32x32x16_bf16(k0, qf[b][0], s, 0, 0, 0), 0, 0, 0);
#pragma unroll
      for (int i = 0; i < 16; i++) m[b] = fmaxf(m[b], s[i]);
    }
  }
  return m;
}

constexpr int NFB = 22, NFA = 10;
DEVI void role_fa(const Params& p, unsigned first, unsigned stride, unsigned end, unsigned budget) {
  const int lane = lane_id(), c = lane & 15, g = lane >> 4;
  const int erow = 8 * (c >> 2) + (c & 3);
  v8u va;
  va[0] = lane * 16; va[1] = lane == 0 ? 0u : OOB; va[2] = g * 128 + (c & 7) * 16;
  va[3] = (erow >> 4) * 1024 + (g * 16 + (erow & 15)) * 16;
  va[4] = (c >> 2) * 128 + (g & 1) * 64 + (c & 3) * 16;
  va[5] = c - 4 * g; va[6] = va[7] = 0;
  const v2u s1 = {rfl(first), rfl(stride)};
  asm volatile(FA_ASM : : "{s[40:43]}"(srd(p.ws, WS_USED)), "{s[44:45]}"(s1), "{s69}"(rfl(end)), "{s90}"(rfl(budget)), "{v[240:247]}"(va) : "v255", "a255", "s99", "vcc", "memory");
  __builtin_unreachable();
}
DEVI void role_fb(const Params& p, unsigned first, int j, int w, unsigned budget) {
  const int lane = lane_id(), g = lane >> 4;
  const int id = w * 64 + lane, tok = id / 6, k6 = id % 6;
  v16u va;
  va[0] = lane * 16; va[1] = lane == 0 ? 0u : OOB; va[2] = w * 6144 + lane * 16;
  va[3] = (((tok >> 4) & 1) * 3 + (k6 >> 1)) * 1024 + (k6 & 1) * 512 + (tok & 15) * 16;
  va[4] = j * 6 + k6 < 128 ? (unsigned)(tok * 4096 + 2048 + j * 96 + k6 * 16) : OOB;
  va[5] = w * 36864 + (8 * g + ((lane >> 2) & 3)) * 96 + (lane & 3) * 8;
#pragma unroll
  for (int i = 0; i < 3; i++) { const int q = 64 * i + lane, row = q / 6, ch = q % 6; va[6 + i] = j * 6 + ch < 128 ? (unsigned)((1024 * w + row) * 2048 + j * 96 + ch * 16 - i * 1024) : 0x80000000u; }
#pragma unroll
  for (int i = 9; i < 16; i++) va[i] = 0;
  const v4u d = srd(p.ws, WS_USED), o = srd(p.out, 2048 * 4096), wo = srd(p.Wo, 4096 * 2048);
  const v16u s0 = {d.x, d.y, d.z, d.w, d.x, d.y, 0u, d.w, o.x, o.y, o.z, o.w, wo.x, wo.y, wo.z, wo.w};
  const v8u s1 = {(unsigned)w, (unsigned)(j & 7), rfl(first), CFG.split, CFG.nhelp + 1, 0u, 0u, 0u};
  asm volatile(FB_ASM : : "{s[40:55]}"(s0), "{s[56:63]}"(s1), "{s90}"(rfl(budget)), "{s91}"(j == 0 && w == 0 ? DBG_OFF + 33792 : DBG_OFF + 60000), "{v[112:127]}"(va) : "v255", "a255", "s99", "vcc", "memory");
  __builtin_unreachable();
}

DEVI void role_worker(const Params& p, int grp, int idx) {
  const int w = rfl(threadIdx.x >> 6);
  unsigned budget = SPIN_CAP;
  v4u pl = {CFG.plan[0], CFG.plan[1], CFG.plan[2], 0};
#pragma unroll
  for (int i = 1; i < 5; i++) {
    asm volatile("" : "+s"(pl));
    if (grp == i) pl = v4u{CFG.plan[3 * i], CFG.plan[3 * i + 1], CFG.plan[3 * i + 2], 0};
  }
  write_records(p, pl, idx, w);
  if (grp == 0) {
    if (CFG.pad && idx < 31) {
      const rsrc_t q = mkrs(p.Wq, 8388608), k = mkrs(p.Wk, 8388608);
      unsigned x = 0;
#pragma unroll 4
      for (unsigned row = (CFG.pad & 1023) + idx * 4 + w; row < 1024; row += 124) { x ^= ld32<0>(q, lane_id() * 128, row * 8192); if (CFG.pad & 1024) x ^= ld32<0>(k, lane_id() * 128, row * 8192); }
      asm volatile("" :: "v"(x));
    }
    if (idx == 31) role_setup(p, w, budget); else check_in(p, w, budget);
    if (CFG.near & 1) wg_wait(p, C_SEL, 1, idx < NFB ? C_TILE + idx * 16 : C_SEL, idx < NFB ? 2 : 1, w, budget);
    if ((CFG.near & 16) && idx < NFB) __builtin_amdgcn_s_sleep(127);
  }
#pragma unroll 1
  for (int round = 0; round < NROUND; round++) {
    const unsigned e = plan_at(pl, round), t = e >> 4;
    if (t == U_NONE) break;
    if (t == U_AT) {
      const int a = 4 * (e & 3) + (idx >> 3), b = idx & 7;
      wg_wait(p, C_AKV, 16, C_AQ + ((a >> 1) * 8 + b) * 4, 2, w, budget);
      f32x4 m = {-INFINITY, -INFINITY, -INFINITY, -INFINITY};
#pragma unroll 1
      for (int pass = 0;; pass++) {
        const unsigned bad = attn_pass(p, a, b, w, m, pass);
        if (pass) break;
        const lptr f = lp(131072);
        lds_st<unsigned>(f, w * 4, bad);
        RAWBAR();
        const v4u x = lds_ld<v4u>(f, 0);
        if (!rfl(x.x | x.y | x.z | x.w)) break;
        m = attn_max(p, a, b, w);
        RAWBAR();
      }
      wg_signal(p, C_AO + (a >> 1) * 128, w, 1);
    } else if (t != U_AKV || (idx >> 3) == (e & 3)) {
      if (t == U_AO) { const unsigned o = C_AO + (4 * (e & 1) + (idx >> 3)) * 128; wg_wait(p, o, 16, o, 16, w, budget); }
      const unsigned flag = gemm_chain(p, round, t, w, budget);
      wg_signal(p, flag, w, t == U_HALF ? 1 : 2);
      if (t <= U_AQ) while ((plan_at(pl, round + 1) >> 4) <= U_AQ) round++;
    }
  }
  const unsigned u = w * NFA + idx - NFB;
  const unsigned first = grp == 0 ? 0 : 5 - grp <= CFG.nhelp ? CFG.split + 5 - grp : NSEG;
  if (idx >= NFB) { if (grp == 0 || (grp >= 3 && u < 32)) role_fa(p, grp ? (CFG.s2 + grp - 3) * 32 + u : u, grp ? 64 : 4 * NFA, grp ? 2048 : CFG.s2 * 32, budget); }
  else if (first < NSEG) role_fb(p, first, idx, w, budget);
  if (budget == 0) st32<SC1>(mkrs(p.ws, WS_USED), 0, C_TMO, 1u);
}

__global__ void __launch_bounds__(256, 1) k_main(Params p) {
  const int res = blockIdx.x & 7, idx = blockIdx.x >> 3;
  if (res >= 3) role_worker(p, res - 3, idx);
  else if (res == 0) role_qk(p, idx);
  else role_vo(p, (res - 1) * 32 + idx);
}

static Params make_params(void* const* d_in, void* d_out, void* d_ws) {
  Params p;
  p.Ax = (const u16*)d_in[0]; p.Awq = (const u16*)d_in[1]; p.Awk = (const u16*)d_in[2]; p.Awv = (const u16*)d_in[3]; p.Awo = (const u16*)d_in[4];
  p.Bx = (const u16*)d_in[5]; p.mem0 = (const u16*)d_in[6]; p.Wq = (const u16*)d_in[7]; p.Wk = (const u16*)d_in[8]; p.Wv = (const u16*)d_in[9]; p.Wo = (const u16*)d_in[10];
  p.out = (u16*)d_out;
  p.ws = (unsigned char*)(((size_t)d_ws + (2u << 20) - 1) & ~(size_t)((2u << 20) - 1));
  return p;
}
static void launch(const Params& p, hipStream_t stream) {
  (void)hipFuncSetAttribute((const void*)k_main, hipFuncAttributeMaxDynamicSharedMemorySize, 160 * 1024);
  k_main<<<256, 256, 160 * 1024, stream>>>(p);
}

extern "C" void kernel_launch(void* const* d_in, const int* in_sizes, int n_in, void* d_out, int out_size, void* d_ws, size_t ws_size, hipStream_t stream) {
  launch(make_params(d_in, d_out, d_ws), stream);
}
```
